```python
import torch

def _softmax(x, axis=-1):
    return torch.softmax(x, dim=axis)
def _gelu(x):
    return torch.nn.functional.gelu(x, approximate="tanh")
def _silu(x):
    return torch.nn.functional.silu(x)
def _sigmoid(x):
    return torch.sigmoid(x)
def _layernorm(x, axis=-1, eps=1e-5):
    m = x.mean(dim=axis, keepdim=True)
    v = x.var(dim=axis, keepdim=True, unbiased=False)
    return (x - m) / torch.sqrt(v + eps)
def _scatter_add(tgt, idx, src, axis=0):
    return tgt.clone().index_add_(axis, idx, src)
def _segment_sum(src, seg, n):
    out = torch.zeros((n,) + src.shape[1:], dtype=src.dtype)
    return out.index_add_(0, seg, src)
def _pad(x, pw):
    flat = tuple(v for pair in reversed(pw) for v in pair)
    return torch.nn.functional.pad(x, flat)

kernel_name = 'recurrent_scan__ssm_conv_h3__mixer_channel_mlp__prompt'

def setup_inputs():

    _w = (lambda a: (lambda t: t.to(torch.bfloat16) if t.is_floating_point() else t)(torch.as_tensor(a)))
    def _A_setup():
        B, S, D = 1, 2048, 512
        ks = 4
        return {
            "q": _w((lambda *s: torch.randn(*s, dtype=torch.bfloat16) * 0.05)(B, S, D)),
            "k": _w((lambda *s: torch.randn(*s, dtype=torch.bfloat16) * 0.8)(B, S, D)),
            "v": _w((lambda *s: torch.randn(*s, dtype=torch.bfloat16) * 0.8)(B, S, D)),
            "shift_k": _w((lambda *s: torch.randn(*s, dtype=torch.bfloat16) * 0.8)(D, ks) * 0.5),
            "ssm_k": _w((lambda *s: torch.randn(*s, dtype=torch.bfloat16) * 0.8)(D, S) * (1.0 / S ** 0.5)),
            "ssm_D": _w((lambda *s: torch.randn(*s, dtype=torch.bfloat16) * 0.8)(D)),
        }
    _A_in = _A_setup()

    def _B_setup():
        B, S, D, F = 1, 2048, 512, 1792
        return {
            "x": _w((lambda *s: torch.randn(*s, dtype=torch.bfloat16) * 0.05)(B, S, D)),
            "w1": _w((lambda *s: torch.randn(*s, dtype=torch.bfloat16) * 0.8)(D, F) * (D ** -0.5)),
            "b1": _w((lambda *s: torch.randn(*s, dtype=torch.bfloat16) * 0.4)(F)),
            "w2": _w((lambda *s: torch.randn(*s, dtype=torch.bfloat16) * 0.2)(F, D) * (F ** -0.5)),
            "b2": _w((lambda *s: torch.randn(*s, dtype=torch.bfloat16) * 0.2)(D)),
        }
    _B_in = _B_setup()

    return {
        "A_q": _A_in["q"],
        "A_k": _A_in["k"],
        "A_v": _A_in["v"],
        "A_shift_k": _A_in["shift_k"],
        "A_ssm_k": _A_in["ssm_k"],
        "A_ssm_D": _A_in["ssm_D"],
        "B_x": _B_in["x"],
        "B_w1": _B_in["w1"],
        "B_b1": _B_in["b1"],
        "B_w2": _B_in["w2"],
        "B_b2": _B_in["b2"],
    }

def reference(A_q, A_k, A_v, A_shift_k, A_ssm_k, A_ssm_D, B_x, B_w1, B_b1, B_w2, B_b2):
    def _A_ref(q, k, v, shift_k, ssm_k, ssm_D):
        B, S, D = q.shape
        ks = shift_k.shape[-1]
        k_pad = _pad(k, ((0, 0), (ks - 1, 0), (0, 0)))
        k_shift = torch.zeros_like(k)
        for t in range(ks):
            k_shift = k_shift + k_pad[:, t:t + S, :] * shift_k[:, ks - 1 - t][None, None, :]
        kv = k_shift * v
        L = 2 * S
        kv_pad = _pad(kv, ((0, 0), (0, S), (0, 0)))
        kf_pad = _pad(ssm_k, ((0, 0), (0, S)))
        kv_f = (lambda x, axes=(-2,-1): torch.fft.rfft2(x.float(), dim=axes))(kv_pad[:, None, :, :], axes=(1, 2))
        kf_f = (lambda x, axes=(-2,-1): torch.fft.rfft2(x.float(), dim=axes))(kf_pad[:, None, :], axes=(1, 2))
        kf_f = (lambda x, *d: x.permute(*d))(kf_f, 1, 2, 0)
        y_f = kv_f * kf_f[None, :, :, :]
        y = (lambda x, s, axes=(-2,-1): torch.fft.irfft2(x, s=s, dim=axes).to(torch.bfloat16))(y_f, (1, L), axes=(1, 2))[:, 0, :S, :]
        y = y + kv * ssm_D[None, None, :]
        return q * y

    def _B_ref(x, w1, b1, w2, b2):
        y = _layernorm(x, axis=-1)
        h = _gelu(torch.einsum("bsd,df->bsf", y, w1) + b1)
        return torch.einsum("bsf,fd->bsd", h, w2) + b2

    x = A_q
    xn = _layernorm(x, axis=-1)
    s = _A_ref(xn, A_k, A_v, A_shift_k, A_ssm_k, A_ssm_D)
    g = _silu(_B_ref(xn, B_w1, B_b1, B_w2, B_b2))
    return x + s * g
```

```cpp
#include <hip/hip_runtime.h>
#include <hip/hip_bf16.h>
typedef unsigned short u16;
constexpr int S = 2048, D = 512, F = 1792;
__device__ __forceinline__ float b2f(u16 x) { return __uint_as_float(((unsigned)x) << 16); }
__device__ __forceinline__ u16 f2b(float f) { unsigned u = __float_as_uint(f); u += 0x7FFF + ((u >> 16) & 1); return (u16)(u >> 16); }
__device__ __forceinline__ float rb(float f) { return b2f(f2b(f)); }

__global__ void k_ln(const u16* x, u16* xn, u16* y2) {
  int row = blockIdx.x; int lane = threadIdx.x;
  float v[8];
  for (int i = 0; i < 8; ++i) v[i] = b2f(x[row * D + lane * 8 + i]);
  float s = 0; for (int i = 0; i < 8; ++i) s += v[i];
  for (int o = 32; o; o >>= 1) s += __shfl_xor(s, o);
  float m = s / D;
  float q = 0; for (int i = 0; i < 8; ++i) q += (v[i] - m) * (v[i] - m);
  for (int o = 32; o; o >>= 1) q += __shfl_xor(q, o);
  float r = rsqrtf(q / D + 1e-5f);
  float w[8];
  for (int i = 0; i < 8; ++i) { w[i] = rb((v[i] - m) * r); xn[row * D + lane * 8 + i] = f2b(w[i]); }
  s = 0; for (int i = 0; i < 8; ++i) s += w[i];
  for (int o = 32; o; o >>= 1) s += __shfl_xor(s, o);
  m = s / D;
  q = 0; for (int i = 0; i < 8; ++i) q += (w[i] - m) * (w[i] - m);
  for (int o = 32; o; o >>= 1) q += __shfl_xor(q, o);
  r = rsqrtf(q / D + 1e-5f);
  for (int i = 0; i < 8; ++i) y2[row * D + lane * 8 + i] = f2b((w[i] - m) * r);
}
__global__ void k_kv(const u16* k, const u16* v, const u16* sk, u16* kv) {
  int idx = blockIdx.x * 256 + threadIdx.x; int s = idx / D, d = idx % D;
  float a = 0;
  for (int j = 3; j >= 0; --j) if (s - j >= 0) a += b2f(k[(s - j) * D + d]) * b2f(sk[d * 4 + j]);
  kv[idx] = f2b(rb(a) * b2f(v[idx]));
}
__global__ void k_conv(const u16* kv, const u16* h, const u16* Dp, float* y) {
  int idx = blockIdx.x * 256 + threadIdx.x; int s = idx / D, d = idx % D;
  float a = 0;
  for (int j = 0; j <= s; ++j) a += b2f(h[d * S + j]) * b2f(kv[(s - j) * D + d]);
  y[idx] = a + b2f(kv[idx]) * b2f(Dp[d]);
}
__global__ void k_g1(const u16* y2, const u16* w1, const u16* b1, u16* h) {
  int idx = blockIdx.x * 256 + threadIdx.x; int s = idx / F, f = idx % F;
  float a = 0;
  for (int k = 0; k < D; ++k) a += b2f(y2[s * D + k]) * b2f(w1[k * F + f]);
  a += b2f(b1[f]);
  float t = tanhf(0.7978845608028654f * (a + 0.044715f * a * a * a));
  h[idx] = f2b(0.5f * a * (1.f + t));
}
__global__ void k_g2(const u16* h, const u16* w2, const u16* b2, const u16* x, const u16* xn, const float* y, u16* out) {
  int idx = blockIdx.x * 256 + threadIdx.x; int s = idx / D, d = idx % D;
  float a = 0;
  for (int k = 0; k < F; ++k) a += b2f(h[s * F + k]) * b2f(w2[k * D + d]);
  a += b2f(b2[d]);
  float g = a / (1.f + __expf(-a));
  out[idx] = f2b(b2f(x[idx]) + b2f(xn[idx]) * y[idx] * g);
}
extern "C" void kernel_launch(void* const* d_in, const int* in_sizes, int n_in, void* d_out, int out_size, void* d_ws, size_t ws_size, hipStream_t stream) {
  const u16* q = (const u16*)d_in[0]; const u16* k = (const u16*)d_in[1]; const u16* v = (const u16*)d_in[2];
  const u16* sk = (const u16*)d_in[3]; const u16* ssm = (const u16*)d_in[4]; const u16* Dp = (const u16*)d_in[5];
  const u16* w1 = (const u16*)d_in[7]; const u16* b1 = (const u16*)d_in[8]; const u16* w2 = (const u16*)d_in[9]; const u16* b2 = (const u16*)d_in[10];
  char* ws = (char*)d_ws;
  u16* xn = (u16*)ws; u16* y2 = (u16*)(ws + (2 << 20)); u16* kv = (u16*)(ws + (4 << 20)); float* y = (float*)(ws + (6 << 20)); u16* h = (u16*)(ws + (10 << 20));
  k_ln<<<S, 64, 0, stream>>>(q, xn, y2);
  k_kv<<<S * D / 256, 256, 0, stream>>>(k, v, sk, kv);
  k_conv<<<S * D / 256, 256, 0, stream>>>(kv, ssm, Dp, y);
  k_g1<<<S * F / 256, 256, 0, stream>>>(y2, w1, b1, h);
  k_g2<<<S * D / 256, 256, 0, stream>>>(h, w2, b2, q, xn, y, (u16*)d_out);
}
```

```cpp
#include <hip/hip_runtime.h>
#include <stdint.h>

namespace mk {
typedef unsigned short u16;
typedef unsigned u32;
typedef unsigned long long u64;
typedef u32 v4u __attribute__((ext_vector_type(4)));
typedef u32 v2u __attribute__((ext_vector_type(2)));
typedef float f4 __attribute__((ext_vector_type(4)));
typedef float f2 __attribute__((ext_vector_type(2)));
typedef short s8v __attribute__((ext_vector_type(8)));
typedef short s4v __attribute__((ext_vector_type(4)));
typedef __bf16 bf2 __attribute__((ext_vector_type(2)));
#define GAS __attribute__((address_space(1)))
#define LAS __attribute__((address_space(3)))
#define CAS __attribute__((address_space(4)))
#define DI __device__ __forceinline__
typedef __amdgpu_buffer_rsrc_t srd_t;

constexpr int S = 2048, D = 512, F = 1792;
constexpr float EPS = 1e-5f;
constexpr u32 WS_EPOCH = 0, WS_FKV = 4096, WS_FST = 36864, WS_FH = 40960, WS_FY = 69632, WS_STATS = 131072, WS_CSUM = 163840,
              WS_KVT = 1u << 20, WS_YT = 3u << 20, WS_H = 5u << 20, WS_BYTES = 13u << 20;
constexpr u32 FLAG_STRIDE = 128;
constexpr u32 L_RING = 0, L_SLOT = 32768;
constexpr u32 L_PK = 131072, L_PV = L_PK + 72 * 128, L_PW = L_PV + 64 * 128;
constexpr u32 L_TOK = 155648, L_CS = L_TOK + 2048, L_B1 = L_CS + 512;
constexpr u32 L_HC = 0, HC_STRIDE = 4160, L_U = 8 * HC_STRIDE, U_STRIDE = 4608, L_RED = 49152;
constexpr u32 L_Y = 131072;
constexpr u32 LDS_BYTES = 163840;
constexpr u32 SPIN_CAP = 1u << 16;

struct Params {
  const u16 *x, *k, *v, *sk, *ssm, *dd, *w1, *b1, *w2, *b2;
  u16* out;
  unsigned char* ws;
};

DI srd_t mk_srd(const void* p, u32 bytes) { return __builtin_amdgcn_make_buffer_rsrc((void*)p, (short)0, (int)bytes, 0x00020000); }
DI float bl(u32 d) { return __uint_as_float(d << 16); }
DI float bh(u32 d) { return __uint_as_float(d & 0xffff0000u); }
DI u32 pk(float a, float b) { f2 t = {a, b}; return __builtin_bit_cast(u32, __builtin_convertvector(t, bf2)); }
DI f4 mfma(v4u a, v4u b, f4 c) { return __builtin_amdgcn_mfma_f32_16x16x32_bf16(__builtin_bit_cast(s8v, a), __builtin_bit_cast(s8v, b), c, 0, 0, 0); }
DI v2u trread(LAS unsigned char* p) { return __builtin_bit_cast(v2u, __builtin_amdgcn_ds_read_tr16_b64_v4i16((LAS s4v*)p)); }
DI void dma16(srd_t rs, u32 voff, u32 soff, u32 lds) {
  asm volatile("s_mov_b32 m0, %0\n\ts_nop 0\n\tbuffer_load_dwordx4 %1, %2, %3 offen lds" ::"s"(lds), "v"(voff), "s"(rs), "s"(soff) : "memory");
}
#define WAITV(n) asm volatile("s_waitcnt vmcnt(" #n ")" ::: "memory")
#define WAITL() asm volatile("s_waitcnt lgkmcnt(0)" ::: "memory")
#define BAR() __builtin_amdgcn_s_barrier()
DI void bar_lds() { WAITL(); BAR(); asm volatile("" ::: "memory"); }
DI v4u ld_sc1(srd_t rs, u32 off) { return __builtin_amdgcn_raw_buffer_load_b128(rs, off, 0, 16); }
DI void st_sc1(srd_t rs, u32 off, v4u v) { __builtin_amdgcn_raw_buffer_store_b128(v, rs, off, 0, 16); }
DI void put_flag(unsigned char* ws, u32 off, u64 tag) { __hip_atomic_store((GAS u64*)(ws + off), tag, __ATOMIC_RELAXED, __HIP_MEMORY_SCOPE_AGENT); }
DI void wait_flags(unsigned char* ws, u32 off, u64 tag) {
  for (u32 spins = 0; spins < SPIN_CAP; ++spins) {
    const u64 v = __hip_atomic_load((GAS u64*)(ws + off), __ATOMIC_RELAXED, __HIP_MEMORY_SCOPE_AGENT);
    if (__all(v == tag)) break;
    __builtin_amdgcn_s_sleep(1);
  }
  __builtin_amdgcn_fence(__ATOMIC_ACQUIRE, "wavefront");
}
#define STAMP_AT(i)

__global__ void __launch_bounds__(512, 2) fused(Params p) {
  extern __shared__ __attribute__((aligned(16))) unsigned char smem[];
  LAS unsigned char* L = (LAS unsigned char*)smem;
  const u32 lbase = (u32)(uintptr_t)L;
  const int tid = threadIdx.x, lane = tid & 63, w = __builtin_amdgcn_readfirstlane(tid >> 6);
  const int b = blockIdx.x, xcd = b & 7, idx = b >> 3;
  const int g = lane >> 4, r16 = lane & 15;
  STAMP_AT(0);
  const u64 tag = *(const CAS u64*)(p.ws + WS_EPOCH) + 0x9E3779B97F4A7C15ull;
  const srd_t rws = mk_srd(p.ws, WS_BYTES);
  const srd_t rx = mk_srd(p.x, S * D * 2), rw1 = mk_srd(p.w1, D * F * 2);
  const bool is_g1 = idx < 28;
  const int ti = 4 * (xcd >> 1) + (idx & 3), tj = 7 * (xcd & 1) + (idx >> 2);

  {
    const srd_t rk = mk_srd(p.k, S * D * 2), rv = mk_srd(p.v, S * D * 2), rsk = mk_srd(p.sk, D * 4 * 2);
    const int s0 = 64 * idx, rr = lane >> 3;
    const u32 swk = (((rr >> 1) & 1) | (((w + 1) & 1) << 1)) << 1, swv = (((rr >> 1) & 1) | ((w & 1) << 1)) << 1;
    dma16(rk, (u32)((s0 + 8 * w + rr) * 1024 + xcd * 128 + (((lane & 7) ^ swk) << 4)), 0, lbase + L_PK + (w + 1) * 1024);
    dma16(rv, (u32)((s0 + 8 * w + rr) * 1024 + xcd * 128 + (((lane & 7) ^ swv) << 4)), 0, lbase + L_PV + w * 1024);
    if (w == 0) dma16(rk, (u32)((s0 - 8 + rr) * 1024 + xcd * 128 + (((lane & 7) ^ swv) << 4)), 0, lbase + L_PK);
    if (w == 1) dma16(rsk, (u32)(xcd * 512 + lane * 16), 0, lbase + L_PW);
  }
  const u32 xvo = (u32)((128 * ti + 8 * w + (lane >> 3)) * 1024 + (((lane & 7) ^ ((4 * w + g) & 7)) << 4));
  const u32 wvo = (u32)((8 * w + g) * 3584 + tj * 256 + ((r16 ^ ((g << 1) | ((w & 1) << 3))) << 4));
  auto issue_chunk = [&](int c) {
    const u32 sl = lbase + L_RING + (c & 3) * L_SLOT;
    dma16(rx, xvo, c * 128, sl + w * 1024);
    dma16(rx, xvo, c * 128 + 65536, sl + 8192 + w * 1024);
    dma16(rw1, wvo, c * 64 * 3584, sl + 16384 + w * 2048);
    dma16(rw1, wvo, c * 64 * 3584 + 4 * 3584, sl + 16384 + w * 2048 + 1024);
  };
  if (is_g1) {
    issue_chunk(0); issue_chunk(1); issue_chunk(2); issue_chunk(3);
    WAITV(16);
  } else {
    WAITV(0);
  }
  BAR();
  STAMP_AT(1);
  {
    const int q = r16 >> 2, pp = lane & 3, half = w >> 2, cq = w & 3;
    float kk[12], vv[8];
#pragma unroll
    for (int t = 0; t < 3; ++t) {
      const int row = 32 * half + 8 * g + 4 + 4 * t + q;
      const int sw = (((row >> 1) & 1) | (((row >> 3) & 1) << 1)) << 1;
      const v2u d = trread(L + L_PK + row * 128 + (((2 * cq + (pp >> 1)) ^ sw) << 4) + 8 * (pp & 1));
      kk[4 * t] = bl(d.x); kk[4 * t + 1] = bh(d.x); kk[4 * t + 2] = bl(d.y); kk[4 * t + 3] = bh(d.y);
    }
#pragma unroll
    for (int t = 0; t < 2; ++t) {
      const int row = 32 * half + 8 * g + 4 * t + q;
      const int sw = (((row >> 1) & 1) | (((row >> 3) & 1) << 1)) << 1;
      const v2u d = trread(L + L_PV + row * 128 + (((2 * cq + (pp >> 1)) ^ sw) << 4) + 8 * (pp & 1));
      vv[4 * t] = bl(d.x); vv[4 * t + 1] = bh(d.x); vv[4 * t + 2] = bl(d.y); vv[4 * t + 3] = bh(d.y);
    }
    const v2u tw = *(LAS v2u*)(L + L_PW + (16 * cq + r16) * 8);
    const float w0 = bl(tw.x), w1 = bh(tw.x), w2 = bl(tw.y), w3 = bh(tw.y);
    float o[8];
#pragma unroll
    for (int j = 0; j < 8; ++j) o[j] = (kk[j + 4] * w0 + kk[j + 3] * w1 + kk[j + 2] * w2 + kk[j + 1] * w3) * vv[j];
    const v4u pkd = {pk(o[7], o[6]), pk(o[5], o[4]), pk(o[3], o[2]), pk(o[1], o[0])};
    const int ch = 64 * xcd + 16 * cq + r16, i0 = 2040 - (64 * idx + 32 * half + 8 * g);
    st_sc1(rws, WS_KVT + (u32)((ch >> 1) * 8192 + (i0 >> 5) * 128 + (ch & 1) * 64 + (i0 & 31) * 2), pkd);
  }
  STAMP_AT(2);

  if (is_g1) {
    const int mi = w & 3, ni = w >> 2, q = r16 >> 2, pp = lane & 3;
    f4 acc[4][2];
#pragma unroll
    for (int n = 0; n < 4; ++n) { acc[n][0] = f4{0, 0, 0, 0}; acc[n][1] = f4{0, 0, 0, 0}; }
    u32 xa[2], wa[4];
#pragma unroll
    for (int ks = 0; ks < 2; ++ks) xa[ks] = (32 * mi + r16) * 128 + (((4 * ks + g) ^ (r16 >> 1)) << 4);
#pragma unroll
    for (int n = 0; n < 4; ++n) wa[n] = 16384 + (8 * g + q) * 256 + (((8 * ni + 2 * n + (pp >> 1)) ^ ((q << 1) | ((g & 1) << 3))) << 4) + 8 * (pp & 1);
#pragma unroll
    for (int c = 0; c < 8; ++c) {
      if (c == 0) WAITV(13);
      else if (c <= 3) WAITV(9);
      else if (c <= 5) WAITV(8);
      else if (c == 6) WAITV(4);
      else WAITV(0);
      WAITL();
      BAR();
      if (c >= 1 && c <= 4) issue_chunk(c + 3);
      if (c == 5 && tid == 0) put_flag(p.ws, WS_FKV + b * FLAG_STRIDE, tag);
      LAS unsigned char* sl = L + L_RING + (c & 3) * L_SLOT;
#pragma unroll
      for (int ks = 0; ks < 2; ++ks) {
        v4u xf[2], wf[4];
#pragma unroll
        for (int m = 0; m < 2; ++m) xf[m] = *(LAS v4u*)(sl + xa[ks] + m * 2048);
#pragma unroll
        for (int n = 0; n < 4; ++n) {
          const v2u lo = trread(sl + wa[n] + (32 * ks) * 256), hi = trread(sl + wa[n] + (32 * ks + 4) * 256);
          wf[n] = v4u{lo.x, lo.y, hi.x, hi.y};
        }
#pragma unroll
        for (int n = 0; n < 4; ++n)
#pragma unroll
          for (int m = 0; m < 2; ++m) acc[n][m] = mfma(wf[n], xf[m], acc[n][m]);
      }
    }
    STAMP_AT(3);
    if (w < 4) {
      const int sel = w >> 1;
      int prod;
      if (sel == 0) prod = 2 * ti + (w & 1);
      else { const int cgp = 2 * tj + (w & 1), m = cgp % 14; prod = ((m >> 2) * 2 + cgp / 14) * 4 + (m & 3); }
      wait_flags(p.ws, WS_FST + prod * FLAG_STRIDE, tag);
      if (sel == 0) {
        const v4u st = ld_sc1(rws, WS_STATS + (u32)(128 * ti + tid) * 16);
        *(LAS v2u*)(L + L_TOK + tid * 8) = v2u{st.x, st.y};
      } else {
        const int c = tid - 128;
        const u32 cs = __builtin_amdgcn_raw_buffer_load_b32(rws, WS_CSUM + (u32)(128 * tj + c) * 4, 0, 16);
        *(LAS u32*)(L + L_CS + c * 4) = cs;
        *(LAS float*)(L + L_B1 + c * 4) = bl(p.b1[128 * tj + c]);
      }
    }
    bar_lds();
    STAMP_AT(4);
    f2 ab[2];
#pragma unroll
    for (int m = 0; m < 2; ++m) ab[m] = *(LAS f2*)(L + L_TOK + (32 * mi + 16 * m + r16) * 8);
#pragma unroll
    for (int pr = 0; pr < 2; ++pr) {
      f4 cs[2], bb[2];
#pragma unroll
      for (int h = 0; h < 2; ++h) {
        cs[h] = *(LAS f4*)(L + L_CS + (64 * ni + 32 * pr + 16 * h + 4 * g) * 4);
        bb[h] = *(LAS f4*)(L + L_B1 + (64 * ni + 32 * pr + 16 * h + 4 * g) * 4);
      }
#pragma unroll
      for (int m = 0; m < 2; ++m) {
        float hv[8];
#pragma unroll
        for (int h = 0; h < 2; ++h)
#pragma unroll
          for (int e = 0; e < 4; ++e) {
            const float a = acc[2 * pr + h][m][e] * ab[m].x + (ab[m].y * cs[h][e] + bb[h][e]);
            const float z = a * (a * a * (-0.10294324f) + (-2.3022082f));
            hv[4 * h + e] = a * __builtin_amdgcn_rcpf(1.0f + __builtin_amdgcn_exp2f(z));
          }
        const v4u o = {pk(hv[0], hv[1]), pk(hv[2], hv[3]), pk(hv[4], hv[5]), pk(hv[6], hv[7])};
        st_sc1(rws, WS_H + (u32)((((8 * ti + 2 * mi + m) * 56 + 4 * tj + 2 * ni + pr) * 64 + lane) * 16), o);
      }
    }
    STAMP_AT(5);
    WAITV(0);
    BAR();
    if (tid == 0) put_flag(p.ws, WS_FH + (ti * 14 + tj) * FLAG_STRIDE, tag);
  } else {
    const int sidx = idx - 28, sp = 4 * xcd + sidx;
    const int m = 4 * (xcd >> 1) + sidx, cgp = 14 * (xcd & 1) + m;
    v4u xr[8], wr[8];
    const int tok = 64 * sp + (tid >> 3);
#pragma unroll
    for (int i = 0; i < 8; ++i) xr[i] = __builtin_amdgcn_raw_buffer_load_b128(rx, tok * 1024 + (tid & 7) * 16 + i * 128, 0, 0);
    const u32 wbase = m < 14 ? (u32)((tid >> 3) * 3584 + cgp * 128 + (tid & 7) * 16) : 0x80000000u;
#pragma unroll
    for (int i = 0; i < 8; ++i) wr[i] = __builtin_amdgcn_raw_buffer_load_b128(rw1, wbase, i * 64 * 3584, 0);
    float s = 0.f;
#pragma unroll
    for (int i = 0; i < 8; ++i)
#pragma unroll
      for (int j = 0; j < 4; ++j) s += bl(xr[i][j]) + bh(xr[i][j]);
    s += __shfl_xor(s, 1); s += __shfl_xor(s, 2); s += __shfl_xor(s, 4);
    const float mean = s * (1.0f / D);
    float qv = 0.f;
#pragma unroll
    for (int i = 0; i < 8; ++i)
#pragma unroll
      for (int j = 0; j < 4; ++j) { const float a = bl(xr[i][j]) - mean, c = bh(xr[i][j]) - mean; qv += a * a + c * c; }
    qv += __shfl_xor(qv, 1); qv += __shfl_xor(qv, 2); qv += __shfl_xor(qv, 4);
    const float var = qv * (1.0f / D);
    const float r1 = 1.0f / sqrtf(var + EPS);
    const float r2 = 1.0f / sqrtf(var * r1 * r1 + EPS);
    if ((tid & 7) == 0) {
      const f4 st = {r1 * r2, -mean * r1 * r2, mean, r1};
      st_sc1(rws, WS_STATS + (u32)tok * 16, __builtin_bit_cast(v4u, st));
    }
    float cs[8];
#pragma unroll
    for (int j = 0; j < 8; ++j) cs[j] = 0.f;
#pragma unroll
    for (int i = 0; i < 8; ++i)
#pragma unroll
      for (int j = 0; j < 4; ++j) { cs[2 * j] += bl(wr[i][j]); cs[2 * j + 1] += bh(wr[i][j]); }
#pragma unroll
    for (int j = 0; j < 8; ++j) { cs[j] += __shfl_xor(cs[j], 8); cs[j] += __shfl_xor(cs[j], 16); cs[j] += __shfl_xor(cs[j], 32); }
    if (lane < 8) {
      *(LAS f4*)(L + L_RING + w * 256 + lane * 32) = f4{cs[0], cs[1], cs[2], cs[3]};
      *(LAS f4*)(L + L_RING + w * 256 + lane * 32 + 16) = f4{cs[4], cs[5], cs[6], cs[7]};
    }
    bar_lds();
    if (tid < 16 && m < 14) {
      f4 t = {0, 0, 0, 0};
#pragma unroll
      for (int ww = 0; ww < 8; ++ww) t += *(LAS f4*)(L + L_RING + ww * 256 + tid * 16);
      st_sc1(rws, WS_CSUM + (u32)(cgp * 256 + tid * 16), __builtin_bit_cast(v4u, t));
    }
    WAITV(0);
    BAR();
    if (tid == 0) { put_flag(p.ws, WS_FST + sp * FLAG_STRIDE, tag); put_flag(p.ws, WS_FKV + b * FLAG_STRIDE, tag); }
    STAMP_AT(5);
    if (b == 255) {
      if (w < 4) wait_flags(p.ws, WS_FKV + tid * FLAG_STRIDE, tag);
      BAR();
      if (tid == 0) put_flag(p.ws, WS_EPOCH, tag);
    }
  }
  STAMP_AT(6);

  const int cp = 32 * xcd + idx;
  const int tm = 4 * xcd + (idx & 3), tn = idx >> 2;
  v4u w2r[7][4];
  {
    const int c = tid >> 8, i8 = tid & 255;
    const srd_t rh = mk_srd(p.ssm, D * S * 2);
    const v4u d = __builtin_amdgcn_raw_buffer_load_b128(rh, (2 * cp + c) * 4096 + i8 * 16, 0, 0);
    const v2u dn = __builtin_amdgcn_raw_buffer_load_b64(rh, (2 * cp + c) * 4096 + i8 * 16 + 16, 0, 0);
    wait_flags(p.ws, WS_FKV + ((31 - 4 * w - (lane & 3)) * 8 + xcd) * FLAG_STRIDE, tag);
    const v4u u = ld_sc1(rws, WS_KVT + (u32)(cp * 8192 + tid * 16));
    const srd_t rw2 = mk_srd(p.w2, F * D * 2);
    const u32 w2vo = (u32)((16 * ((lane >> 3) & 1) + 4 * g) * 1024 + (lane & 7) * 16);
#pragma unroll
    for (int s = 0; s < 7; ++s)
#pragma unroll
      for (int i = 0; i < 4; ++i) w2r[s][i] = __builtin_amdgcn_raw_buffer_load_b128(rw2, w2vo + i * 1024, (224 * w + 32 * s) * 1024 + tn * 128, 0);
    __builtin_amdgcn_sched_barrier(0);
    const u32 a0 = __builtin_amdgcn_alignbit(d.y, d.x, 16), a1 = __builtin_amdgcn_alignbit(d.z, d.y, 16), a2 = __builtin_amdgcn_alignbit(d.w, d.z, 16),
              a3 = __builtin_amdgcn_alignbit(dn.x, d.w, 16), a4 = __builtin_amdgcn_alignbit(dn.y, dn.x, 16);
    LAS unsigned char* hc = L + L_HC + c * 4 * HC_STRIDE + 32 + i8 * 16;
    *(LAS v4u*)(hc) = d;
    *(LAS v4u*)(hc + HC_STRIDE) = v4u{a0, a1, a2, a3};
    *(LAS v4u*)(hc + 2 * HC_STRIDE) = v4u{d.y, d.z, d.w, dn.x};
    *(LAS v4u*)(hc + 3 * HC_STRIDE) = v4u{a1, a2, a3, a4};
    if (i8 == 0) {
      const v4u z = {0, 0, 0, 0};
#pragma unroll
      for (int rho = 0; rho < 4; ++rho) *(LAS v4u*)(hc + rho * HC_STRIDE - 32) = z;
      *(LAS v4u*)(hc - 16) = z;
      *(LAS v4u*)(hc + HC_STRIDE - 16) = v4u{0, 0, 0, d.x << 16};
      *(LAS v4u*)(hc + 2 * HC_STRIDE - 16) = v4u{0, 0, 0, d.x};
      *(LAS v4u*)(hc + 3 * HC_STRIDE - 16) = v4u{0, 0, d.x << 16, a0};
    }
    if (tid < 64) *(LAS v4u*)(L + L_U + (tid >> 5) * U_STRIDE + 4096 + (tid & 31) * 16) = v4u{0, 0, 0, 0};
    *(LAS v4u*)(L + L_U + ((tid >> 2) & 1) * U_STRIDE + (tid >> 3) * 64 + (tid & 3) * 16) = u;
  }
  bar_lds();
  STAMP_AT(7);
  {
    const int c = w >> 2, kq = w & 3;
    const int rho = (r16 + 1) & 3;
    LAS unsigned char* ap = L + L_HC + (c * 4 + rho) * HC_STRIDE + (r16 + 1 - rho + 8 * g + 64 * kq) * 2;
    LAS unsigned char* bp = L + L_U + c * U_STRIDE + (240 - 16 * r16 + 8 * g + 64 * kq) * 2;
    f4 acc[8];
#pragma unroll
    for (int T = 0; T < 8; ++T) acc[T] = f4{0, 0, 0, 0};
#pragma unroll
    for (int kl = 0; kl < 2; ++kl) {
      v4u A[8], B[8];
#pragma unroll
      for (int i = 0; i < 8; ++i) {
        const v2u lo = *(volatile LAS v2u*)(ap + i * 512 + kl * 64), hi = *(volatile LAS v2u*)(ap + i * 512 + kl * 64 + 8);
        A[i] = v4u{lo.x, lo.y, hi.x, hi.y};
        B[i] = *(LAS v4u*)(bp + (7 - i) * 512 + kl * 64);
      }
#pragma unroll
      for (int T = 0; T < 8; ++T)
#pragma unroll
        for (int kh = 0; kh <= T; ++kh) acc[T] = mfma(A[kh], B[T - kh], acc[T]);
    }
    LAS unsigned char* rp = L + L_RED + w * 8192 + ((lane ^ ((lane >> 5) << 3)) << 4);
#pragma unroll
    for (int T = 0; T < 8; ++T) *(LAS f4*)(rp + T * 1024) = acc[T];
  }
  bar_lds();
  STAMP_AT(8);
  {
    const int c = tid >> 8, t8 = tid & 255;
    const int T = t8 >> 5, c16 = (t8 >> 1) & 15, hi = t8 & 1;
    LAS unsigned char* rp = L + L_RED + c * 32768 + T * 1024 + (((c16 ^ (hi << 3)) + 32 * hi) << 4);
    f4 y0 = {0, 0, 0, 0}, y1 = {0, 0, 0, 0};
#pragma unroll
    for (int q = 0; q < 4; ++q) { y0 += *(LAS f4*)(rp + q * 8192); y1 += *(LAS f4*)(rp + q * 8192 + 256); }
    const v4u u = *(LAS v4u*)(L + L_U + c * U_STRIDE + (2040 - 8 * t8) * 2);
    const float dc = bl(p.dd[2 * cp + c]);
    const v4u o = {pk(y0[0] + dc * bh(u.w), y0[1] + dc * bl(u.w)), pk(y0[2] + dc * bh(u.z), y0[3] + dc * bl(u.z)),
                   pk(y1[0] + dc * bh(u.y), y1[1] + dc * bl(u.y)), pk(y1[2] + dc * bh(u.x), y1[3] + dc * bl(u.x))};
    st_sc1(rws, WS_YT + (u32)((2 * cp + c) * 4096 + t8 * 16), o);
  }
  WAITV(0);
  BAR();
  if (tid == 0) put_flag(p.ws, WS_FY + cp * FLAG_STRIDE, tag);
  STAMP_AT(9);

  {
    const int tj0 = (224 * w) >> 7, tj1 = (224 * w + 223) >> 7;
    const int tjp = min(tj0 + (lane & 3), tj1);
    wait_flags(p.ws, WS_FH + ((tm >> 1) * 14 + tjp) * FLAG_STRIDE, tag);
    STAMP_AT(10);
    f4 acc[4][4];
#pragma unroll
    for (int t = 0; t < 4; ++t)
#pragma unroll
      for (int m = 0; m < 4; ++m) acc[t][m] = f4{0, 0, 0, 0};
    const u32 hb = WS_H + (u32)(((4 * tm * 56 + 7 * w) * 64 + lane) * 16);
    v4u hf[4][4];
#pragma unroll
    for (int s = 0; s < 4; ++s)
#pragma unroll
      for (int m = 0; m < 4; ++m) hf[s][m] = ld_sc1(rws, hb + (m * 56 + s) * 1024);
    __builtin_amdgcn_sched_barrier(0);
#pragma unroll
    for (int s = 0; s < 7; ++s) {
      u32 lo[4][2], hi[4][2];
#pragma unroll
      for (int i = 0; i < 4; ++i)
#pragma unroll
        for (int d = 0; d < 2; ++d) {
          lo[i][d] = __builtin_amdgcn_update_dpp(w2r[s][i][d], w2r[s][i][2 + d], 0x128, 0xF, 0xC, false);
          hi[i][d] = __builtin_amdgcn_update_dpp(w2r[s][i][2 + d], w2r[s][i][d], 0x128, 0xF, 0x3, false);
        }
#pragma unroll
      for (int t = 0; t < 4; ++t) {
        const u32 sel = (t & 1) ? 0x07060302u : 0x05040100u;
        const int d = t >> 1;
        w2r[s][t] = v4u{__builtin_amdgcn_perm(lo[1][d], lo[0][d], sel), __builtin_amdgcn_perm(lo[3][d], lo[2][d], sel),
                        __builtin_amdgcn_perm(hi[1][d], hi[0][d], sel), __builtin_amdgcn_perm(hi[3][d], hi[2][d], sel)};
      }
    }
    __builtin_amdgcn_sched_barrier(0);
#pragma unroll
    for (int s = 0; s < 7; ++s) {
#pragma unroll
      for (int m = 0; m < 4; ++m)
#pragma unroll
        for (int t = 0; t < 4; ++t) acc[t][m] = mfma(w2r[s][t], hf[s & 3][m], acc[t][m]);
      if (s + 4 < 7) {
#pragma unroll
        for (int m = 0; m < 4; ++m) hf[s & 3][m] = ld_sc1(rws, hb + (m * 56 + s + 4) * 1024);
      }
      __builtin_amdgcn_sched_barrier(0);
    }
    STAMP_AT(11);
    LAS unsigned char* pp = L + w * 16384 + ((lane ^ (((lane >> 4) & 1) << 3)) << 4);
#pragma unroll
    for (int m = 0; m < 4; ++m)
#pragma unroll
      for (int t = 0; t < 4; ++t) *(LAS f4*)(pp + (m * 4 + t) * 1024) = acc[t][m];
  }
  const int hh = lane >> 5, tok = 8 * w + ((lane >> 2) & 7), q = lane & 3, chunk = 2 * q + hh;
  {
    const u32 fo = (lane & 4) ? WS_FST + tm * FLAG_STRIDE : WS_FY + (32 * tn + 4 * w + (lane & 3)) * FLAG_STRIDE;
    wait_flags(p.ws, fo, tag);
    const int ch = tid >> 3, tg = tid & 7;
    const v4u y = ld_sc1(rws, WS_YT + (u32)((64 * tn + ch) * 4096 + (64 * tm + 8 * tg) * 2));
    LAS unsigned char* yp = L + L_Y + (8 * tg) * 128 + ((w ^ tg) << 4) + (ch & 7) * 2;
#pragma unroll
    for (int j = 0; j < 4; ++j) {
      *(LAS u16*)(yp + (2 * j) * 128) = (u16)(y[j] & 0xffff);
      *(LAS u16*)(yp + (2 * j + 1) * 128) = (u16)(y[j] >> 16);
    }
  }
  const v4u st = ld_sc1(rws, WS_STATS + (u32)(64 * tm + tok) * 16);
  const u32 oidx = (u32)((64 * tm + tok) * 512 + 64 * tn + 8 * chunk);
  const v4u xv = *(const GAS v4u*)(p.x + oidx);
  const v4u b2v = *(const GAS v4u*)(p.b2 + 64 * tn + 8 * chunk);
  bar_lds();
  STAMP_AT(12);
  {
    const int gp = 2 * hh + (q >> 1), lp = (tok & 15) + 16 * gp;
    LAS unsigned char* rp = L + (tok >> 4) * 4096 + ((lp ^ ((gp & 1) << 3)) << 4) + 8 * (q & 1);
    f2 sum[4];
#pragma unroll
    for (int t = 0; t < 4; ++t) sum[t] = f2{0, 0};
#pragma unroll
    for (int pw = 0; pw < 8; ++pw)
#pragma unroll
      for (int t = 0; t < 4; ++t) sum[t] += *(volatile LAS f2*)(rp + pw * 16384 + t * 1024);
    float o[8];
#pragma unroll
    for (int t = 0; t < 4; ++t) {
      const auto r = __builtin_amdgcn_permlane32_swap(__float_as_uint(sum[t].x), __float_as_uint(sum[t].y), false, false);
      o[t] = __uint_as_float(r[0]); o[4 + t] = __uint_as_float(r[1]);
    }
    const v4u yv = *(LAS v4u*)(L + L_Y + tok * 128 + ((chunk ^ (tok >> 3)) << 4));
    const float mean = __uint_as_float(st.z), r1 = __uint_as_float(st.w);
    float res[8];
#pragma unroll
    for (int j = 0; j < 8; ++j) {
      const float ov = o[j] + ((j & 1) ? bh(b2v[j >> 1]) : bl(b2v[j >> 1]));
      const float gate = ov * __builtin_amdgcn_rcpf(1.0f + __builtin_amdgcn_exp2f(ov * -1.4426950409f));
      const float xx = (j & 1) ? bh(xv[j >> 1]) : bl(xv[j >> 1]);
      const float yy = (j & 1) ? bh(yv[j >> 1]) : bl(yv[j >> 1]);
      res[j] = xx + (xx - mean) * r1 * yy * gate;
    }
    *(GAS v4u*)(p.out + oidx) = v4u{pk(res[0], res[1]), pk(res[2], res[3]), pk(res[4], res[5]), pk(res[6], res[7])};
  }
}
}

extern "C" void kernel_launch(void* const* d_in, const int* in_sizes, int n_in, void* d_out, int out_size, void* d_ws, size_t ws_size,
                              hipStream_t stream) {
  using namespace mk;
  Params p;
  p.x = (const u16*)d_in[0]; p.k = (const u16*)d_in[1]; p.v = (const u16*)d_in[2]; p.sk = (const u16*)d_in[3];
  p.ssm = (const u16*)d_in[4]; p.dd = (const u16*)d_in[5]; p.w1 = (const u16*)d_in[7]; p.b1 = (const u16*)d_in[8];
  p.w2 = (const u16*)d_in[9]; p.b2 = (const u16*)d_in[10];
  p.out = (u16*)d_out; p.ws = (unsigned char*)d_ws;
  (void)hipFuncSetAttribute((const void*)fused, hipFuncAttributeMaxDynamicSharedMemorySize, LDS_BYTES);
  fused<<<256, 512, LDS_BYTES, stream>>>(p);
}
```

```cpp
#include <hip/hip_runtime.h>
#include <stdint.h>

#if defined(__HIP_DEVICE_COMPILE__)
#pragma clang attribute push(__attribute__((target("no-packed-fp32-ops"))), apply_to = function)
#endif
namespace mk {
typedef unsigned short u16;
typedef unsigned u32;
typedef unsigned long long u64;
typedef u32 v4u __attribute__((ext_vector_type(4)));
typedef u32 v2u __attribute__((ext_vector_type(2)));
typedef float f4 __attribute__((ext_vector_type(4)));
typedef float f2 __attribute__((ext_vector_type(2)));
typedef float f16v __attribute__((ext_vector_type(16)));
typedef short s8v __attribute__((ext_vector_type(8)));
typedef short s4v __attribute__((ext_vector_type(4)));
typedef __bf16 bf2 __attribute__((ext_vector_type(2)));
#define GAS __attribute__((address_space(1)))
#define LAS __attribute__((address_space(3)))
#define CAS __attribute__((address_space(4)))
#define DI __device__ __forceinline__
typedef __amdgpu_buffer_rsrc_t srd_t;
typedef LAS unsigned char* lptr;

constexpr int S = 2048, D = 512, F = 1792;
constexpr float EPS = 1e-5f;
constexpr u32 WS_EPOCH = 0, WS_FKV = 4096, WS_FST = 36864, WS_FH = 40960, WS_FY = 69632, WS_STATS = 131072, WS_CSUM = 163840,
              WS_KVT = 1u << 20, WS_YT = 3u << 20, WS_H = 5u << 20, WS_BYTES = 13u << 20;
constexpr u32 FLAG_STRIDE = 128;
constexpr u32 L_SLOT = 32768;
constexpr u32 L_HC = 98304, HC_STRIDE = 4160, L_U = L_HC + 8 * HC_STRIDE, U_STRIDE = 4608;
constexpr u32 L_PK = L_U + 2 * U_STRIDE, L_PV = L_PK + 72 * 128, L_PW = L_PV + 64 * 128;
constexpr u32 L_TOK = L_PW + 1024, L_CS = L_TOK + 1024, L_B1 = L_CS + 512, L_JUNK = L_B1 + 512;
constexpr u32 L_RED = L_HC, L_CNT = L_RED + 32768;
constexpr u32 L_Y = L_PK;
constexpr u32 LDS_BYTES = 163840;
static_assert(L_JUNK + 256 <= LDS_BYTES, "LDS map");
constexpr u32 SPIN_CAP = 1u << 16;

struct Params {
  const u16 *x, *k, *v, *sk, *ssm, *dd, *w1, *b1, *w2, *b2;
  u16* out;
  unsigned char* ws;
};

DI srd_t mk_srd(const void* p, u32 bytes) { return __builtin_amdgcn_make_buffer_rsrc((void*)p, (short)0, (int)bytes, 0x00020000); }
DI float asf(u32 d) { return __builtin_bit_cast(float, d); }
DI u32 asu(float f) { return __builtin_bit_cast(u32, f); }
DI bool all_lanes(bool c) { return __builtin_amdgcn_ballot_w64(!c) == 0; }
DI float bl(u32 d) { return asf(d << 16); }
DI float bh(u32 d) { return asf(d & 0xffff0000u); }
DI u32 pk(float a, float b) { f2 t = {a, b}; return __builtin_bit_cast(u32, __builtin_convertvector(t, bf2)); }
DI f4 mfma(v4u a, v4u b, f4 c) { return __builtin_amdgcn_mfma_f32_16x16x32_bf16(__builtin_bit_cast(s8v, a), __builtin_bit_cast(s8v, b), c, 0, 0, 0); }
DI f16v mfma32(v4u a, v4u b, f16v c) { return __builtin_amdgcn_mfma_f32_32x32x16_bf16(__builtin_bit_cast(s8v, a), __builtin_bit_cast(s8v, b), c, 0, 0, 0); }
DI lptr lp(u32 a) { return (lptr)(uintptr_t)a; }
DI v2u trread(u32 a) { return __builtin_bit_cast(v2u, __builtin_amdgcn_ds_read_tr16_b64_v4i16((LAS s4v*)lp(a))); }
DI v4u trfrag(u32 a, u32 hi_off) { const v2u lo = trread(a), hi = trread(a + hi_off); return v4u{lo.x, lo.y, hi.x, hi.y}; }
DI v4u lr128(u32 a) { return *(LAS v4u*)lp(a); }
DI f4 lrf4(u32 a) { return *(LAS f4*)lp(a); }
DI void lw128(u32 a, v4u v) { *(LAS v4u*)lp(a) = v; }
DI void lwf4(u32 a, f4 v) { *(LAS f4*)lp(a) = v; }
#define DMA_(OP, FL, rs, voff, sb, SI, lb, LI) do { u32 t_; \
  asm volatile("s_add_u32 m0, %1, %2\n\ts_add_u32 %0, %3, %4\n\t" OP " %5, %6, %0 offen" FL " lds" \
               : "=&s"(t_) : "s"(lb), "i"(LI), "s"(sb), "i"(SI), "v"(voff), "s"(rs) : "memory", "scc"); } while (0)
#define DFL ""
#define DMA(rs, voff, sb, SI, lb, LI) DMA_("buffer_load_dwordx4", DFL, rs, voff, sb, SI, lb, LI)
#define SSM_FL ""
#define DMANT(rs, voff, sb, SI, lb, LI) DMA_("buffer_load_dwordx4", " nt", rs, voff, sb, SI, lb, LI)
#define TOUCH(rs, voff, sb, SI) DMA_("buffer_load_dword", "", rs, voff, sb, SI, 0u, L_JUNK)
#define ALD128(d, off, rs, FL) asm volatile("buffer_load_dwordx4 %0, %1, %2, 0 offen" FL : "=v"(d) : "v"(off), "s"(rs) : "memory")
#define ALD128S(d, off, rs, so, FL) asm volatile("buffer_load_dwordx4 %0, %1, %2, %3 offen" FL : "=v"(d) : "v"(off), "s"(rs), "s"(so) : "memory")
#define ALD64(d, off, rs, FL) asm volatile("buffer_load_dwordx2 %0, %1, %2, 0 offen" FL : "=v"(d) : "v"(off), "s"(rs) : "memory")
#define ALD16(d, off, rs, FL) asm volatile("buffer_load_ushort %0, %1, %2, 0 offen" FL : "=v"(d) : "v"(off), "s"(rs) : "memory")
#define WAITV(n) asm volatile("s_waitcnt vmcnt(" #n ")" ::: "memory")
#define PIN(a) asm volatile("" : "+v"(a)::"memory")
#define KEEP(a) asm volatile("" : "+v"(a))
#define KEEPS(a) asm volatile("" : "+s"(a))
#define WAITL() asm volatile("s_waitcnt lgkmcnt(0)" ::: "memory")
#define BAR() __builtin_amdgcn_s_barrier()
#define VBAR(n) asm volatile("s_waitcnt vmcnt(" #n ") lgkmcnt(0)\n\ts_barrier" ::: "memory")
#define LBAR() asm volatile("s_waitcnt lgkmcnt(0)\n\ts_barrier" ::: "memory")
#define SB() __builtin_amdgcn_sched_barrier(0)
DI float wave_sum(float v) {
  v += asf(__builtin_amdgcn_update_dpp(0u, asu(v), 0xB1, 0xF, 0xF, true));
  v += asf(__builtin_amdgcn_update_dpp(0u, asu(v), 0x4E, 0xF, 0xF, true));
  v += asf(__builtin_amdgcn_update_dpp(0u, asu(v), 0x141, 0xF, 0xF, true));
  v += asf(__builtin_amdgcn_update_dpp(0u, asu(v), 0x140, 0xF, 0xF, true));
  const auto a = __builtin_amdgcn_permlane16_swap(asu(v), asu(v), false, false);
  v = asf(a[0]) + asf(a[1]);
  const auto c = __builtin_amdgcn_permlane32_swap(asu(v), asu(v), false, false);
  return asf(c[0]) + asf(c[1]);
}
DI v4u ld_sc1(srd_t rs, u32 off, u32 soff = 0) { return __builtin_amdgcn_raw_buffer_load_b128(rs, off, soff, 16); }
DI void st_sc1(srd_t rs, u32 off, v4u v, u32 soff = 0) { __builtin_amdgcn_raw_buffer_store_b128(v, rs, off, soff, 16); }
DI void put_flag(srd_t rs, u32 l0, u32 off, u64 tag) { __builtin_amdgcn_raw_buffer_store_b64(__builtin_bit_cast(v2u, tag), rs, l0, off, 16); }
DI u64 get_flag(srd_t rs, u32 off) { return __builtin_bit_cast(u64, __builtin_amdgcn_raw_buffer_load_b64(rs, off, 0, 16)); }
DI void wait_flags(srd_t rs, u32 off, u64 tag, u64 v) {
  if (!all_lanes(v == tag)) {
#pragma unroll 1
    for (u32 spins = 0; spins < SPIN_CAP; ++spins) {
      u64 x;
      asm volatile("buffer_load_dwordx2 %0, %1, %2, 0 offen sc1\n\ts_waitcnt vmcnt(0)" : "=&v"(x) : "v"(off), "s"(rs) : "memory");
      if (all_lanes(x == tag)) break;
      __builtin_amdgcn_s_sleep(1);
    }
  }
  asm volatile("" ::: "memory");
}
DI void arrive_publish(u32 cnt, srd_t rs, u32 l0, u32 off, u64 tag) {
  u32 old;
  asm volatile("s_mov_b64 exec, 1\n\tds_add_rtn_u32 %0, %1, %2\n\ts_mov_b64 exec, -1\n\ts_waitcnt lgkmcnt(0)" : "=&v"(old) : "v"(cnt), "v"(1u) : "memory");
  if (__builtin_amdgcn_readfirstlane(old) == 7u) put_flag(rs, l0, off, tag);
}
#define STAMP_AT(i)
#define STAMP_F(i)
#define NPF 0
#define ZX 1
#define ZW1 1
#define ZW2 1

__global__ void __launch_bounds__(512, 2) fused(Params p) {
  const int tid = __builtin_amdgcn_workitem_id_x(), lane = tid & 63, w = __builtin_amdgcn_readfirstlane(tid >> 6);
  const int b = __builtin_amdgcn_workgroup_id_x(), xcd = b & 7, idx = b >> 3;
  const int g = lane >> 4, r16 = lane & 15, q4 = r16 >> 2, p4 = lane & 3, rr = lane >> 3;
  STAMP_AT(0);
  KEEPS(p.ws); KEEPS(p.out); KEEPS(p.w2); KEEPS(p.b2); KEEPS(p.k);
  const int cp = 32 * xcd + idx;
  u64 tag = *(const CAS u64*)(p.ws + WS_EPOCH);
  u32 ddp = *(const CAS u32*)(p.dd + 2 * cp);
  const bool is_g1 = idx < 28;
  const srd_t rx = mk_srd(p.x, S * D * 2 * ZX), rw1 = mk_srd(p.w1, D * F * 2 * ZW1);
  const u32 w1k = w * 1024;
  v4u xr;
  const int stok = 256 * xcd + 8 * idx + w;
  {
    const srd_t rk = mk_srd(p.k, S * D * 2), rv = mk_srd(p.v, S * D * 2);
    const u32 vo = (u32)(rr * 1024 + (((lane & 7) ^ (rr & 2)) << 4)), so = (u32)((64 * idx + 8 * w) * 1024 + xcd * 128);
    DMANT(rk, vo, so, 0, w1k, L_PK + 1024);
    DMANT(rv, vo, so, 0, w1k, L_PV);
    ALD128(xr, (u32)(stok * 1024 + lane * 16), rx, "");
    if (w == 0) DMANT(rk, vo + so - 8192u, 0u, 0, 0u, L_PK);
    if (w == 1) { const srd_t rsk = mk_srd(p.sk, D * 4 * 2); DMA(rsk, (u32)(lane * 16), (u32)(xcd * 512), 0, 0u, L_PW); }
    if (w == 7) {
      u64 pc; u32 len;
      asm volatile("mk_code_begin:\n\ts_getpc_b64 %0\n\ts_mov_b32 %1, mk_code_end-mk_code_begin" : "=s"(pc), "=s"(len));
      const srd_t rc = mk_srd((const void*)pc, len);
      TOUCH(rc, (u32)(lane * 128), (u32)((idx & 3) * 8192), 0);
    }
  }
  SB();
  const int ti = 4 * (xcd >> 1) + (idx & 3), tn = idx >> 2, tj = 7 * (xcd & 1) + tn;
  const int sidx = idx & 3, hm = 4 * (xcd >> 1) + sidx, cgp = 14 * (xcd & 1) + (hm < 14 ? hm : 0);
  const u32 chsw = (u32)(((lane & 7) ^ (rr & 6)) << 4);
  const u32 xvo = (u32)((128 * ti + 8 * w + rr) * 1024 + (((lane & 7) ^ ((4 * w + g) & 7)) << 4));
  const u32 wvo = (u32)((8 * w + (is_g1 ? g : rr)) * 3584 + (is_g1 ? tj * 256 : cgp * 128)) + (is_g1 ? (u32)((r16 ^ (g << 2)) << 4) : chsw);
  const u32 w2k = w * 2048;
#define ROT1 ((tn + 2 * sidx) & 7)
  u32 sox = (u32)(ROT1) * 128, sow = sox * 1792;
#define NEXT_CHUNK() do { sox = (sox + 128) & 1023; sow = sox * 1792; } while (0)
#define PIECE(c, i) do { \
    if ((i) == 0) DMA(rx, xvo, sox, 0, w1k, ((c) % 3) * L_SLOT); \
    if ((i) == 1) DMA(rx, xvo, sox, 65536, w1k, ((c) % 3) * L_SLOT + 8192); \
    if ((i) == 2) DMA(rw1, wvo, sow, 0, w2k, ((c) % 3) * L_SLOT + 16384); \
    if ((i) == 3) { DMA(rw1, wvo, sow, 4 * 3584, w2k, ((c) % 3) * L_SLOT + 17408); NEXT_CHUNK(); } } while (0)
#define HPIECE(c) DMA(rw1, wvo, 0u, (c) * 64 * 3584, w1k, (c) * 8192)
  BAR();
  if (is_g1) { PIECE(0, 0); PIECE(0, 1); PIECE(0, 2); PIECE(0, 3); }
  else { HPIECE(0); HPIECE(1); HPIECE(2); HPIECE(3); }
  SB();

  const srd_t rws = mk_srd(p.ws, WS_BYTES);
  const int tm = 4 * xcd + sidx;
  const int mi = w & 3, ni = w >> 2, hl = lane >> 5, l31 = lane & 31;
  u32 l0 = lane == 0 ? 0u : 0x80000000u;
  u32 a_kv = L_PK + (32 * ni + 8 * g + 4 + q4) * 128 + (((2 * mi + (p4 >> 1)) ^ (q4 & 2)) << 4) + 8 * (p4 & 1);
  u32 a_tap = L_PW + (16 * mi + r16) * 8;
  u32 o_kv = (u32)((r16 >> 1) * 8192 + (r16 & 1) * 64 + (3 - g) * 16);
  const u32 s_kv = WS_KVT + (u32)((32 * xcd + 8 * mi) * 8192 + (63 - 2 * idx - ni) * 128);
  u32 xa[2][4], wa[2][2];
#pragma unroll
  for (int ks = 0; ks < 4; ++ks) { xa[0][ks] = (32 * mi + l31) * 128 + (((2 * ks + hl) ^ ((l31 >> 1) & 7)) << 4); xa[1][ks] = xa[0][ks] + 2 * L_SLOT; }
#pragma unroll
  for (int n = 0; n < 2; ++n) {
    wa[0][n] = 16384 + (8 * hl + q4) * 256 + (((8 * ni + 4 * n + 2 * (g & 1) + (p4 >> 1)) ^ (q4 << 2)) << 4) + 8 * (p4 & 1);
    wa[1][n] = wa[0][n] + 2 * L_SLOT;
  }
  u32 w2vo = (u32)(rr * 1024) + chsw;
  const u32 w4k = w * 4096;
  u32 ta[4];
#pragma unroll
  for (int t = 0; t < 4; ++t)
    ta[t] = L_SLOT + w4k + (16 * hl + 4 * (g & 1) + q4) * 128 + (((2 * t + (p4 >> 1)) ^ (4 * (g & 1) + (q4 & 2))) << 4) + 8 * (p4 & 1);
  u32 t16 = tid * 16;
  u32 a_u = L_U + ((tid >> 2) & 1) * U_STRIDE + (tid >> 3) * 64 + (tid & 3) * 16;
  u32 a_hc = L_HC + ni * (4 * HC_STRIDE - 4096) + 32 + t16;
  u32 a_A, a_B;
  { const int rho = (15 - r16) & 3;
    a_A = L_HC + (ni * 4 + rho) * HC_STRIDE + (r16 + 1 + rho + 8 * g + 64 * mi) * 2;
    a_B = L_U + ni * U_STRIDE + (240 - 16 * r16 + 8 * g + 64 * mi) * 2; }
  KEEP(l0); KEEP(a_kv); KEEP(a_tap); KEEP(o_kv); KEEP(w2vo); KEEP(t16); KEEP(a_u); KEEP(a_hc); KEEP(a_A); KEEP(a_B);
#pragma unroll
  for (int i = 0; i < 4; ++i) { KEEP(xa[0][i]); KEEP(xa[1][i]); KEEP(ta[i]); KEEP(wa[i >> 1][i & 1]); }
  const srd_t rw2 = mk_srd(p.w2, F * D * 2 * ZW2), rssm = mk_srd(p.ssm + cp * 4096, 8192);
  u32 ksh[7];
#define ROT2 xcd
  { int t = ROT2;
#pragma unroll
    for (int s = 0; s < 7; ++s) { t = t >= 7 ? t - 7 : t; ksh[s] = (u32)t * 1024; KEEPS(ksh[s]); ++t; } }
  const u32 w2base = (u32)(224 * w * 1024 + tn * 128);
  const int sel = (w >> 1) & 1;
  u32 fo1, st_off, a_st, fo2;
  {
    const int m = 2 * tn + (w & 1);
    const u32 fkv = WS_FKV + ((31 - 4 * w - p4) * 8 + xcd) * FLAG_STRIDE;
    const u32 fs0 = sel ? WS_FST + (((m >> 2) * 2 + (xcd & 1)) * 4 + (m & 3)) * FLAG_STRIDE : WS_FKV + ((16 * (ti & 1) + 8 * (w & 1)) * 8 + (ti >> 1)) * FLAG_STRIDE;
    const u32 fs = fs0 + (lane & 7) * (sel ? 0 : 8 * FLAG_STRIDE);
    fo1 = (lane & (is_g1 ? 8 : 0)) ? fs : fkv;
    st_off = (sel ? WS_CSUM + 512 * tj : WS_STATS + 2048 * ti) + ((tid & 127) << (sel ? 2 : 4));
    a_st = (sel ? L_CS : L_TOK) + ((tid & 127) << (sel ? 2 : 3));
    const int tja = ((224 * w) >> 7) + p4, tjb = (224 * w + 223) >> 7;
    const u32 fh = WS_FH + ((tm >> 1) * 14 + (tja < tjb ? tja : tjb)) * FLAG_STRIDE, fk = WS_FKV + ((8 * sidx + w) * 8 + xcd) * FLAG_STRIDE;
    fo2 = p4 == 3 ? fk : fh;
  }
  u32 a_cs = L_CS + (64 * ni + 4 * hl) * 4, a_tok = L_TOK + (32 * mi + l31) * 8;
  u32 o_h = (u32)(((l31 >> 4) * 56 * 64 + r16 + 16 * hl) * 16);
  const u32 s_h = WS_H + (u32)(((8 * ti + 2 * mi) * 56 + 4 * tj + 2 * ni) * 1024);
  u32 a_red = L_RED + w * 8192 + ((lane ^ (hl << 3)) << 4);
  u32 hb = WS_H + (u32)(((4 * tm * 56 + 7 * w) * 64 + lane) * 16);
  const u32 s_cp = (u32)cp * 8192;
  KEEP(fo1); KEEP(st_off); KEEP(a_st); KEEP(fo2); KEEP(a_cs); KEEP(a_tok); KEEP(o_h); KEEP(a_red); KEEP(hb);
  if (w == 2) lw128(L_U + hl * U_STRIDE + 4096 + l31 * 16, v4u{0, 0, 0, 0});
  if (w == 3) lw128(L_HC + rr * HC_STRIDE + (lane & 1) * 16, v4u{0, 0, 0, 0});
  SB();
  VBAR(4);
  PIN(xr);
  STAMP_AT(1);
  tag += 0x9E3779B97F4A7C15ull;
  if (is_g1) { PIECE(1, 0); PIECE(1, 1); }
  else { HPIECE(4); HPIECE(5); }
  {
    const v2u k0 = trread(a_kv), k1 = trread(a_kv + 512), k2 = trread(a_kv + 1024);
    const v2u v0 = trread(a_kv + (L_PV - L_PK - 512)), v1 = trread(a_kv + (L_PV - L_PK));
    const v2u tw = *(LAS v2u*)lp(a_tap);
    float xv8[8];
#pragma unroll
    for (int j = 0; j < 4; ++j) { xv8[2 * j] = bl(xr[j]); xv8[2 * j + 1] = bh(xr[j]); }
    const float mean = wave_sum(((xv8[0] + xv8[1]) + (xv8[2] + xv8[3])) + ((xv8[4] + xv8[5]) + (xv8[6] + xv8[7]))) * (1.0f / D);
    float q0 = 0.f, q1 = 0.f;
#pragma unroll
    for (int j = 0; j < 4; ++j) { const float d0 = xv8[2 * j] - mean, d1 = xv8[2 * j + 1] - mean; q0 += d0 * d0; q1 += d1 * d1; }
    const float var = wave_sum(q0 + q1) * (1.0f / D);
    const float r1 = __builtin_amdgcn_rsqf(var + EPS);
    const float r2 = __builtin_amdgcn_rsqf(var * r1 * r1 + EPS);
    const float kk[12] = {0.f, bh(k0.x), bl(k0.y), bh(k0.y), bl(k1.x), bh(k1.x), bl(k1.y), bh(k1.y), bl(k2.x), bh(k2.x), bl(k2.y), bh(k2.y)};
    const float vv[8] = {bl(v0.x), bh(v0.x), bl(v0.y), bh(v0.y), bl(v1.x), bh(v1.x), bl(v1.y), bh(v1.y)};
    const float w0 = bl(tw.x), w1 = bh(tw.x), w2 = bl(tw.y), w3 = bh(tw.y);
    float o[8];
#pragma unroll
    for (int j = 0; j < 8; ++j) o[j] = (kk[j + 4] * w0 + kk[j + 3] * w1 + kk[j + 2] * w2 + kk[j + 1] * w3) * vv[j];
    st_sc1(rws, o_kv, v4u{pk(o[7], o[6]), pk(o[5], o[4]), pk(o[3], o[2]), pk(o[1], o[0])}, s_kv);
    st_sc1(rws, l0, __builtin_bit_cast(v4u, f4{r1 * r2, -mean * r1 * r2, mean, r1}), WS_STATS + (u32)stok * 16);
  }
  SB();
  if (is_g1) { PIECE(1, 2); PIECE(1, 3); }
  else { HPIECE(6); HPIECE(7); }
  SB();
  STAMP_AT(2);
  v4u hd; v2u hp;
  v4u ud;
  v4u w2f[7][4];
  u32 sw2;
#define W2_PIECE(s, i) do { if ((i) == 0) sw2 = w2base + (ksh[s] << 5); DMA(rw2, w2vo, sw2, (i) * 8192, w4k, (((s) + 2) % 3) * L_SLOT + (i) * 1024); } while (0)
#define W2_STAGE(s) do { W2_PIECE(s, 0); W2_PIECE(s, 1); W2_PIECE(s, 2); W2_PIECE(s, 3); } while (0)
#define LOAD_CONV_IN() do { ALD128S(ud, t16, rws, s_cp + WS_KVT, " sc1"); ALD128(hd, t16, rssm, SSM_FL); ALD64(hp, t16 - 8, rssm, SSM_FL); } while (0)
  auto w2_read = [&](int s) {
#pragma unroll
    for (int t = 0; t < 4; ++t) w2f[s][t] = trfrag(ta[t] + ((s + 2) % 3) * L_SLOT - L_SLOT, 1024);
  };
  auto write_hc = [&](int rho) {
    const bool first = (tid & 255) == 0;
    const u32 p0 = first ? 0u : hp.x, p1 = first ? 0u : hp.y;
    const u32 a0 = __builtin_amdgcn_alignbit(p1, p0, 16), a1 = __builtin_amdgcn_alignbit(hd.x, p1, 16), a2 = __builtin_amdgcn_alignbit(hd.y, hd.x, 16),
              a3 = __builtin_amdgcn_alignbit(hd.z, hd.y, 16), a4 = __builtin_amdgcn_alignbit(hd.w, hd.z, 16);
    if (rho == 0) lw128(a_hc, hd);
    if (rho == 1) lw128(a_hc + HC_STRIDE, v4u{a1, a2, a3, a4});
    if (rho == 2) lw128(a_hc + 2 * HC_STRIDE, v4u{p1, hd.x, hd.y, hd.z});
    if (rho == 3) lw128(a_hc + 3 * HC_STRIDE, v4u{a0, a1, a2, a3});
  };

  if (is_g1) {
    f16v acc[2];
#pragma unroll
    for (int i = 0; i < 16; ++i) { acc[0][i] = 0.f; acc[1][i] = 0.f; }
    v4u xf[2][2], wf[2][2][2];
#define REQ(dc, i) do { if ((dc) >= 8) W2_PIECE((dc) & 3, i); else PIECE(dc, i); } while (0)
#define STEP(st, dc, d0, nd) do { \
      constexpr int hs_ = (st) & 1, hn_ = hs_ ^ 1, sl_ = (((st) + 1) >> 1) % 3, sb_ = sl_ >> 1; constexpr u32 so_ = (sl_ & 1) * L_SLOT; \
      constexpr bool rd_ = (st) < 15, mm_ = (st) >= 0; \
      _Pragma("unroll") for (int i_ = 0; i_ < 4; ++i_) { \
        if (mm_) acc[i_ & 1] = mfma32(wf[hs_][i_ >> 1][i_ & 1], xf[hs_][i_ >> 1], acc[i_ & 1]); \
        if (rd_) { \
          if (!(i_ & 1)) xf[hn_][i_ >> 1] = lr128(xa[sb_][2 * hn_ + (i_ >> 1)] + so_); \
          wf[hn_][i_ >> 1][i_ & 1] = trfrag(wa[sb_][i_ & 1] + so_ + (32 * hn_ + 16 * (i_ >> 1)) * 256, 1024); } \
        if ((nd) == 4) { if (i_ == 0) REQ(dc, 0); if (i_ == 1) REQ(dc, 1); if (i_ == 2) REQ(dc, 2); if (i_ == 3) REQ(dc, 3); } \
        if ((nd) == 2) { if (i_ == 1) REQ(dc, d0); if (i_ == 3) REQ(dc, (d0) + 1); } \
        SB(); } } while (0)
#define ITER(c) do { STAMP_F(16 + (c)); STEP(2 * (c) + 1, (c) + 3, 0, 2); STEP(2 * (c) + 2, (c) + 3, 2, 2); } while (0)
    u64 pv; v2u stv1; u32 b1v;
    VBAR(6);
    STAMP_AT(15);
    STEP(-1, 0, 0, 0);
    STEP(0, 2, 0, 4);
    VBAR(4);
    if (w == 0) put_flag(rws, l0, WS_FKV + b * FLAG_STRIDE, tag);
    ITER(0);
    VBAR(4);
    ITER(1);
    VBAR(4);
    ITER(2);
    VBAR(4);
    ALD64(pv, fo1, rws, " sc1");
    { const srd_t rb1 = mk_srd(p.b1 + 128 * tj, 256); ALD16(b1v, (u32)(tid & 127) * 2, rb1, ""); }
    ITER(3);
    VBAR(6);
    ITER(4);
    VBAR(4);
    PIN(pv);
    wait_flags(rws, fo1, tag, pv);
    ALD64(stv1, st_off, rws, " sc1");
    LOAD_CONV_IN();
    ITER(5);
    VBAR(8);
    ITER(6);
    WAITV(11);
    PIN(stv1); PIN(b1v);
    if (w < 2) *(LAS v2u*)lp(a_st) = stv1;
    else if (w < 4) { *(LAS u32*)lp(a_st) = stv1.x; *(LAS float*)lp(a_st + (L_B1 - L_CS)) = bl(b1v); }
    LBAR();
    STAMP_F(23);
    const f2 ab = *(LAS f2*)lp(a_tok);
    f4 cs[2][2], bb[2][2];
#pragma unroll
    for (int h = 0; h < 2; ++h) { cs[0][h] = lrf4(a_cs + 8 * h * 4); bb[0][h] = lrf4(a_cs + (L_B1 - L_CS) + 8 * h * 4); }
    SB();
    STEP(15, 10, 0, 4);
    STAMP_AT(3);
#pragma unroll
    for (int qi = 0; qi < 4; ++qi) {
      const int n = qi >> 1, sp = qi & 1;
      if (qi < 3) {
#pragma unroll
        for (int h = 0; h < 2; ++h) { cs[sp ^ 1][h] = lrf4(a_cs + (16 * (qi + 1) + 8 * h) * 4); bb[sp ^ 1][h] = lrf4(a_cs + (L_B1 - L_CS) + (16 * (qi + 1) + 8 * h) * 4); }
      }
      if (qi == 1) { WAITV(13); PIN(ud); PIN(hd); PIN(hp); lw128(a_u, ud); write_hc(0); }
      if (qi == 2) { write_hc(1); write_hc(2); }
      if (qi == 3) { write_hc(3); WAITV(3); w2_read(0); w2_read(1); w2_read(2); }
      SB();
      float hv[8];
#pragma unroll
      for (int j = 0; j < 8; ++j) {
        const float a = acc[n][8 * sp + j] * ab.x + (ab.y * cs[sp][j >> 2][j & 3] + bb[sp][j >> 2][j & 3]);
        const float z = a * (a * a * (-0.10294324f) + (-2.3022082f));
        hv[j] = a * __builtin_amdgcn_rcpf(1.0f + __builtin_amdgcn_exp2f(z));
      }
      st_sc1(rws, o_h + n * 1024 + sp * 512, v4u{pk(hv[0], hv[1]), pk(hv[2], hv[3]), pk(hv[4], hv[5]), pk(hv[6], hv[7])}, s_h);
      SB();
    }
    STAMP_AT(4);
  } else {
    VBAR(0);
    if (w == 0) put_flag(rws, l0, WS_FKV + b * FLAG_STRIDE, tag);
    STAMP_AT(15);
    if (w < 4) {
      const v4u ones = {0x3f803f80u, 0x3f803f80u, 0x3f803f80u, 0x3f803f80u};
      f4 s0 = {0, 0, 0, 0};
      const u32 a = (16 * hl + 4 * (g & 1) + q4) * 128 + (((2 * w + (p4 >> 1)) ^ (4 * (g & 1) + (q4 & 2))) << 4) + 8 * (p4 & 1);
#pragma unroll
      for (int st = 0; st < 16; ++st) s0 = mfma(trfrag(a + st * 4096, 1024), ones, s0);
      st_sc1(rws, r16 == 0 && hm < 14 ? WS_CSUM + (u32)(64 * cgp + 16 * w + 4 * g) * 4 : 0x80000000u, __builtin_bit_cast(v4u, s0));
    }
    VBAR(0);
    if (w == 0) put_flag(rws, l0, WS_FST + (4 * xcd + sidx) * FLAG_STRIDE, tag);
    STAMP_AT(3);
#pragma unroll
    for (int i = 0; i < NPF; ++i) TOUCH(rw2, (u32)(lane * 128), (u32)((224 * w + 8 * sidx) * 1024) + (ksh[i] << 5), 0);
    W2_STAGE(0); W2_STAGE(1); W2_STAGE(2);
    if (b == 255) {
      if (w < 4) wait_flags(rws, WS_FKV + tid * FLAG_STRIDE, tag, ~tag);
      BAR();
      if (w == 0) put_flag(rws, l0, WS_EPOCH, tag);
    }
    wait_flags(rws, fo1, tag, ~tag);
    LOAD_CONV_IN();
    w2_read(0); w2_read(1); w2_read(2);
    WAITV(0);
    PIN(ud); PIN(hd); PIN(hp);
    lw128(a_u, ud);
    write_hc(0); write_hc(1); write_hc(2); write_hc(3);
    STAMP_AT(4);
  }
  LBAR();
  STAMP_AT(5);

  v4u hf[7];
  u32 hbm;
  auto load_h = [&](int m, int s) { if (s == 0) hbm = hb + (u32)(((m + tn) & 3) * 57344); hf[s] = ld_sc1(rws, hbm, ksh[s]); };
  u64 pv2;
  v4u uo;
  {
    auto rdA = [&](int kl, int i) {
      const v2u lo = *(volatile LAS v2u*)lp(a_A + i * 512 + kl * 64), hi = *(volatile LAS v2u*)lp(a_A + i * 512 + kl * 64 + 8);
      return v4u{lo.x, lo.y, hi.x, hi.y};
    };
    auto rdB = [&](int kl, int i) { return lr128(a_B + (7 - i) * 512 + kl * 64); };
    f4 acc[8];
    v4u A[8], B[8], A1[8], B1[8];
    A[0] = rdA(0, 0);
#pragma unroll
    for (int i = 0; i < 8; ++i) B[i] = rdB(0, i);
    A[1] = rdA(0, 1);
#pragma unroll
    for (int kh = 0; kh < 8; ++kh) {
      SB();
      if (kh + 2 < 8) A[kh + 2] = rdA(0, kh + 2);
      if (kh >= 1) { B1[kh - 1] = rdB(1, kh - 1); A1[kh - 1] = rdA(1, kh - 1); }
      if (kh == 6) { B1[7] = rdB(1, 7); A1[7] = rdA(1, 7); uo = lr128(L_U + 4080 + ni * (U_STRIDE + 4096) - t16); }
#pragma unroll
      for (int e = 0; e + kh < 8; ++e) {
        acc[kh + e] = mfma(A[kh], B[e], kh == 0 ? f4{0, 0, 0, 0} : acc[kh + e]);
        constexpr int first[8] = {0, 8, 15, 21, 26, 30, 33, 35};
        const int n = first[kh] + e;
        if (n % 3 == 1) {
          SB();
          if (n / 3 == 0) W2_PIECE(3, 0); if (n / 3 == 1) W2_PIECE(3, 1); if (n / 3 == 2) W2_PIECE(3, 2); if (n / 3 == 3) W2_PIECE(3, 3);
          if (n / 3 == 4) W2_PIECE(4, 0); if (n / 3 == 5) W2_PIECE(4, 1); if (n / 3 == 6) W2_PIECE(4, 2); if (n / 3 == 7) W2_PIECE(4, 3);
          if (n / 3 == 8) W2_PIECE(5, 0); if (n / 3 == 9) W2_PIECE(5, 1); if (n / 3 == 10) W2_PIECE(5, 2); if (n / 3 == 11) W2_PIECE(5, 3);
          SB();
        }
      }
    }
    SB();
    STAMP_AT(6);
    VBAR(12);
    if (is_g1 && w == 0) put_flag(rws, l0, WS_FH + (ti * 14 + tj) * FLAG_STRIDE, tag);
    STAMP_AT(7);
#pragma unroll
    for (int T = 7; T >= 0; --T) {
      SB();
#pragma unroll
      for (int kh = 0; kh <= T; ++kh) acc[T] = mfma(A1[kh], B1[T - kh], acc[T]);
      if (T < 7) lwf4(a_red + (T + 1) * 1024, acc[T + 1]);
      if (T == 4) { SB(); WAITV(8); w2_read(3); }
      if (T == 2) { SB(); WAITL(); W2_STAGE(6); }
    }
    lwf4(a_red, acc[0]);
    SB();
    ALD64(pv2, fo2, rws, " sc1");
    STAMP_AT(8);
  }
  const int tok = tid >> 3, chunk = tid & 7;
  LBAR();
  {
    const int t8 = tid & 255, T = t8 >> 5, c16 = (t8 >> 1) & 15, hi = t8 & 1;
    const u32 rp = L_RED + ni * 32768 + T * 1024 + (((c16 ^ (hi << 3)) + 32 * hi) << 4);
    const f4 y0 = (lrf4(rp) + lrf4(rp + 8192)) + (lrf4(rp + 16384) + lrf4(rp + 24576));
    const f4 y1 = (lrf4(rp + 256) + lrf4(rp + 8192 + 256)) + (lrf4(rp + 16384 + 256) + lrf4(rp + 24576 + 256));
    const float dc = w < 4 ? bl(ddp) : bh(ddp);
    const v4u o = {pk(y0[0] + dc * bh(uo.w), y0[1] + dc * bl(uo.w)), pk(y0[2] + dc * bh(uo.z), y0[3] + dc * bl(uo.z)),
                   pk(y1[0] + dc * bh(uo.y), y1[1] + dc * bl(uo.y)), pk(y1[2] + dc * bh(uo.x), y1[3] + dc * bl(uo.x))};
    st_sc1(rws, t16, o, s_cp + WS_YT);
    if (tid == 256) *(LAS u32*)lp(L_CNT) = 0u;
  }
  SB();
  VBAR(1);
  PIN(pv2);
  STAMP_AT(9);
  wait_flags(rws, fo2, tag, pv2);
  STAMP_AT(10);
#pragma unroll
  for (int s = 0; s < 7; ++s) load_h(0, s);
  SB();
  w2_read(4); w2_read(5); w2_read(6);
  SB();

  const u32 oidx = (u32)((64 * tm + tok) * 512 + 64 * tn + 8 * chunk);
  const u32 fo3 = WS_FY + (32 * tn + 4 * w + p4) * FLAG_STRIDE;
  v4u yv, xv, b2v; v2u stv;
  {
    u64 pv3 = 0;
    const u32 pp0 = w4k + (((lane & 48) | ((lane + 4 * hl) & 15)) << 4);
    WAITV(7);
    arrive_publish(L_CNT, rws, l0, WS_FY + cp * FLAG_STRIDE, tag);
#pragma unroll
    for (int m = 0; m < 4; ++m) {
      f4 acc[4];
#pragma unroll
      for (int s = 0; s < 7; ++s) {
#pragma unroll
        for (int t = 0; t < 4; ++t) acc[t] = mfma(w2f[s][t], hf[s], s == 0 ? f4{0, 0, 0, 0} : acc[t]);
        if (m < 3) load_h(m + 1, s);
        if (m == 2 && s == 0) pv3 = get_flag(rws, fo3);
        if (m == 3 && s == 1) {
          wait_flags(rws, fo3, tag, pv3);
          yv = ld_sc1(rws, WS_YT + (u32)((64 * tn + tok) * 4096 + (64 * tm + 8 * chunk) * 2));
        }
        SB();
      }
      if (m == 2) {
        xv = *(const GAS v4u*)(p.x + oidx);
        b2v = *(const GAS v4u*)(p.b2 + 64 * tn + 8 * chunk);
        stv = __builtin_amdgcn_raw_buffer_load_b64(rws, WS_STATS + (u32)(64 * tm + tok) * 16 + 8, 0, 16);
      }
      const u32 pm = pp0 + ((m + tn) & 3) * L_SLOT;
#pragma unroll
      for (int t = 0; t < 4; ++t) lwf4((pm ^ ((t & 1) * 128)) + t * 1024, acc[t]);
      SB();
      STAMP_F(24 + m);
    }
  }
  STAMP_AT(11);
  {
    const u32 yp = L_Y + (8 * chunk) * 128 + ((w ^ chunk) << 4) + (tok & 7) * 2;
#pragma unroll
    for (int j = 0; j < 4; ++j) {
      *(LAS u16*)lp(yp + (2 * j) * 128) = (u16)(yv[j] & 0xffff);
      *(LAS u16*)lp(yp + (2 * j + 1) * 128) = (u16)(yv[j] >> 16);
    }
  }
  LBAR();
  STAMP_AT(12);
  {
    const u32 rp = (w >> 1) * L_SLOT + (chunk >> 1) * 1024 + ((32 * (chunk & 1) + ((tok + 4 * (chunk & 3)) & 15)) << 4);
    const f4 o0 = ((lrf4(rp) + lrf4(rp + 4096)) + (lrf4(rp + 8192) + lrf4(rp + 12288))) +
                  ((lrf4(rp + 16384) + lrf4(rp + 20480)) + (lrf4(rp + 24576) + lrf4(rp + 28672)));
    const f4 o1 = ((lrf4(rp + 256) + lrf4(rp + 4352)) + (lrf4(rp + 8448) + lrf4(rp + 12544))) +
                  ((lrf4(rp + 16640) + lrf4(rp + 20736)) + (lrf4(rp + 24832) + lrf4(rp + 28928)));
    const v4u yy = lr128(L_Y + tok * 128 + ((chunk ^ w) << 4));
    const float r1 = asf(stv.y), mr = -asf(stv.x) * r1;
    float res[8];
#pragma unroll
    for (int j = 0; j < 8; ++j) {
      const float ov = (j < 4 ? o0[j & 3] : o1[j & 3]) + ((j & 1) ? bh(b2v[j >> 1]) : bl(b2v[j >> 1]));
      const float gate = ov * __builtin_amdgcn_rcpf(1.0f + __builtin_amdgcn_exp2f(ov * -1.4426950409f));
      const float xx = (j & 1) ? bh(xv[j >> 1]) : bl(xv[j >> 1]);
      const float yj = (j & 1) ? bh(yy[j >> 1]) : bl(yy[j >> 1]);
      res[j] = xx + ((xx * r1 + mr) * yj) * gate;
    }
    *(GAS v4u*)(p.out + oidx) = v4u{pk(res[0], res[1]), pk(res[2], res[3]), pk(res[4], res[5]), pk(res[6], res[7])};
  }
  asm volatile("mk_code_end:");
}
}
#if defined(__HIP_DEVICE_COMPILE__)
#pragma clang attribute pop
#endif

extern "C" void kernel_launch(void* const* d_in, const int* in_sizes, int n_in, void* d_out, int out_size, void* d_ws, size_t ws_size,
                              hipStream_t stream) {
  using namespace mk;
  Params p;
  p.x = (const u16*)d_in[0]; p.k = (const u16*)d_in[1]; p.v = (const u16*)d_in[2]; p.sk = (const u16*)d_in[3];
  p.ssm = (const u16*)d_in[4]; p.dd = (const u16*)d_in[5]; p.w1 = (const u16*)d_in[7]; p.b1 = (const u16*)d_in[8];
  p.w2 = (const u16*)d_in[9]; p.b2 = (const u16*)d_in[10];
  p.out = (u16*)d_out; p.ws = (unsigned char*)d_ws;
  (void)hipFuncSetAttribute((const void*)fused, hipFuncAttributeMaxDynamicSharedMemorySize, LDS_BYTES);
  fused<<<256, 512, LDS_BYTES, stream>>>(p);
}
```

```cpp
#include <hip/hip_runtime.h>
#include <stdint.h>
#define NPF 1

namespace mk {
typedef unsigned short u16;
typedef unsigned u32;
typedef unsigned long long u64;
typedef u32 v4u __attribute__((ext_vector_type(4)));
typedef u32 v2u __attribute__((ext_vector_type(2)));
typedef float f4 __attribute__((ext_vector_type(4)));
typedef float f2 __attribute__((ext_vector_type(2)));
typedef float f16v __attribute__((ext_vector_type(16)));
typedef short s8v __attribute__((ext_vector_type(8)));
typedef short s4v __attribute__((ext_vector_type(4)));
typedef __bf16 bf2 __attribute__((ext_vector_type(2)));
#define GAS __attribute__((address_space(1)))
#define LAS __attribute__((address_space(3)))
#define CAS __attribute__((address_space(4)))
#define DI __device__ __forceinline__
typedef __amdgpu_buffer_rsrc_t srd_t;
typedef LAS unsigned char* lptr;

constexpr int S = 2048, D = 512, F = 1792;
constexpr float EPS = 1e-5f;
constexpr u32 WS_EPOCH = 0, WS_FKV = 4096, WS_FST = 36864, WS_FH = 40960, WS_FY = 69632, WS_STATS = 131072, WS_CSUM = 196608,
              WS_KVT = 1u << 20, WS_YT = 3u << 20, WS_H = 5u << 20, WS_BYTES = 13u << 20;
constexpr u32 FLAG_STRIDE = 128;
constexpr u32 L_SLOT = 32768;
constexpr u32 L_HC = 98304, HC_STRIDE = 4160, L_U = L_HC + 8 * HC_STRIDE, U_STRIDE = 4608;
constexpr u32 L_PK = L_U + 2 * U_STRIDE, L_PV = L_PK + 72 * 128, L_PW = L_PV + 64 * 128;
constexpr u32 L_JUNK = L_PW + 1024;
constexpr u32 L_RED = L_HC, L_CNT = L_RED + 32768;
constexpr u32 L_Y = L_PK;
constexpr u32 LDS_BYTES = 163840;
static_assert(L_JUNK + 256 <= LDS_BYTES, "LDS map");
constexpr u32 SPIN_CAP = 1u << 16;

struct Params {
  const u16 *x, *k, *v, *sk, *ssm, *dd, *w1, *b1, *w2, *b2;
  u16* out;
  unsigned char* ws;
};

DI srd_t mk_srd(const void* p, u32 bytes) { return __builtin_amdgcn_make_buffer_rsrc((void*)p, (short)0, (int)bytes, 0x00020000); }
DI float asf(u32 d) { return __builtin_bit_cast(float, d); }
DI u32 asu(float f) { return __builtin_bit_cast(u32, f); }
DI bool all_lanes(bool c) { return __builtin_amdgcn_ballot_w64(!c) == 0; }
DI float bl(u32 d) { return asf(d << 16); }
DI float bh(u32 d) { return asf(d & 0xffff0000u); }
DI u32 pk(float a, float b) { f2 t = {a, b}; return __builtin_bit_cast(u32, __builtin_convertvector(t, bf2)); }
DI void split3(float v, float& hi, float& lo, float& lo2) { hi = bl(pk(v, v)); lo = bl(pk(v - hi, 0.f)); lo2 = (v - hi) - lo; }
DI f4 mfma(v4u a, v4u b, f4 c) { return __builtin_amdgcn_mfma_f32_16x16x32_bf16(__builtin_bit_cast(s8v, a), __builtin_bit_cast(s8v, b), c, 0, 0, 0); }
DI f16v mfma32(v4u a, v4u b, f16v c) { return __builtin_amdgcn_mfma_f32_32x32x16_bf16(__builtin_bit_cast(s8v, a), __builtin_bit_cast(s8v, b), c, 0, 0, 0); }
DI lptr lp(u32 a) { return (lptr)(uintptr_t)a; }
DI v2u trread(u32 a) { return __builtin_bit_cast(v2u, __builtin_amdgcn_ds_read_tr16_b64_v4i16((LAS s4v*)lp(a))); }
DI v4u trfrag(u32 a, u32 hi_off) { const v2u lo = trread(a), hi = trread(a + hi_off); return v4u{lo.x, lo.y, hi.x, hi.y}; }
DI v4u lr128(u32 a) { return *(LAS v4u*)lp(a); }
DI f4 lrf4(u32 a) { return *(LAS f4*)lp(a); }
DI void lw128(u32 a, v4u v) { *(LAS v4u*)lp(a) = v; }
DI void lwf4(u32 a, f4 v) { *(LAS f4*)lp(a) = v; }
#define DMA_(OP, FL, rs, voff, sb, SI, lb, LI) do { u32 t_; \
  asm volatile("s_add_u32 m0, %1, %2\n\ts_add_u32 %0, %3, %4\n\t" OP " %5, %6, %0 offen" FL " lds" \
               : "=&s"(t_) : "s"(lb), "i"(LI), "s"(sb), "i"(SI), "v"(voff), "s"(rs) : "memory", "scc"); } while (0)
#define DFL ""
#define DMA(rs, voff, sb, SI, lb, LI) DMA_("buffer_load_dwordx4", DFL, rs, voff, sb, SI, lb, LI)
#define SSM_FL ""
#define DMANT(rs, voff, sb, SI, lb, LI) DMA_("buffer_load_dwordx4", " nt", rs, voff, sb, SI, lb, LI)
#define TOUCH(rs, voff, sb, SI) DMA_("buffer_load_dword", "", rs, voff, sb, SI, 0u, L_JUNK)
#define ALD128(d, off, rs, FL) asm volatile("buffer_load_dwordx4 %0, %1, %2, 0 offen" FL : "=v"(d) : "v"(off), "s"(rs) : "memory")
#define ALD128S(d, off, rs, so, FL) asm volatile("buffer_load_dwordx4 %0, %1, %2, %3 offen" FL : "=v"(d) : "v"(off), "s"(rs), "s"(so) : "memory")
#define ALD64(d, off, rs, FL) asm volatile("buffer_load_dwordx2 %0, %1, %2, 0 offen" FL : "=v"(d) : "v"(off), "s"(rs) : "memory")
#define ALD16(d, off, rs, FL) asm volatile("buffer_load_ushort %0, %1, %2, 0 offen" FL : "=v"(d) : "v"(off), "s"(rs) : "memory")
#define WAITV(n) asm volatile("s_waitcnt vmcnt(" #n ")" ::: "memory")
#define PIN(a) asm volatile("" : "+v"(a)::"memory")
#define KEEP(a) asm volatile("" : "+v"(a))
#define KEEPS(a) asm volatile("" : "+s"(a))
#define WAITL() asm volatile("s_waitcnt lgkmcnt(0)" ::: "memory")
#define BAR() __builtin_amdgcn_s_barrier()
#define VBAR(n) asm volatile("s_waitcnt vmcnt(" #n ") lgkmcnt(0)\n\ts_barrier" ::: "memory")
#define LBAR() asm volatile("s_waitcnt lgkmcnt(0)\n\ts_barrier" ::: "memory")
#define SB() __builtin_amdgcn_sched_barrier(0)
DI float wave_sum(float v) {
  v += asf(__builtin_amdgcn_update_dpp(0u, asu(v), 0xB1, 0xF, 0xF, true));
  v += asf(__builtin_amdgcn_update_dpp(0u, asu(v), 0x4E, 0xF, 0xF, true));
  v += asf(__builtin_amdgcn_update_dpp(0u, asu(v), 0x141, 0xF, 0xF, true));
  v += asf(__builtin_amdgcn_update_dpp(0u, asu(v), 0x140, 0xF, 0xF, true));
  const auto a = __builtin_amdgcn_permlane16_swap(asu(v), asu(v), false, false);
  v = asf(a[0]) + asf(a[1]);
  const auto c = __builtin_amdgcn_permlane32_swap(asu(v), asu(v), false, false);
  return asf(c[0]) + asf(c[1]);
}
DI v4u ld_sc1(srd_t rs, u32 off, u32 soff = 0) { return __builtin_amdgcn_raw_buffer_load_b128(rs, off, soff, 16); }
DI void st_sc1(srd_t rs, u32 off, v4u v, u32 soff = 0) { __builtin_amdgcn_raw_buffer_store_b128(v, rs, off, soff, 16); }
DI void put_flag(srd_t rs, u32 l0, u32 off, u64 tag) { __builtin_amdgcn_raw_buffer_store_b64(__builtin_bit_cast(v2u, tag), rs, l0, off, 16); }
DI u64 get_flag(srd_t rs, u32 off) { return __builtin_bit_cast(u64, __builtin_amdgcn_raw_buffer_load_b64(rs, off, 0, 16)); }
DI void wait_flags(srd_t rs, u32 off, u64 tag, u64 v) {
  if (!all_lanes(v == tag)) {
#pragma unroll 1
    for (u32 spins = 0; spins < SPIN_CAP; ++spins) {
      u64 x;
      asm volatile("buffer_load_dwordx2 %0, %1, %2, 0 offen sc1\n\ts_waitcnt vmcnt(0)" : "=&v"(x) : "v"(off), "s"(rs) : "memory");
      if (all_lanes(x == tag)) break;
      __builtin_amdgcn_s_sleep(1);
    }
  }
  asm volatile("" ::: "memory");
}
DI void arrive_publish(u32 cnt, srd_t rs, u32 l0, u32 off, u64 tag) {
  u32 old;
  asm volatile("s_mov_b64 exec, 1\n\tds_add_rtn_u32 %0, %1, %2\n\ts_mov_b64 exec, -1\n\ts_waitcnt lgkmcnt(0)" : "=&v"(old) : "v"(cnt), "v"(1u) : "memory");
  if (__builtin_amdgcn_readfirstlane(old) == 7u) put_flag(rs, l0, off, tag);
}
#define STAMP_AT(i)
#define STAMP_F(i)
#define ZX 1
#define ZW1 1
#define ZW2 1

__global__ void __launch_bounds__(512, 2) fused(Params p) {
  const int tid = __builtin_amdgcn_workitem_id_x(), lane = tid & 63, w = __builtin_amdgcn_readfirstlane(tid >> 6);
  const int b = __builtin_amdgcn_workgroup_id_x(), xcd = b & 7, idx = b >> 3;
  const int g = lane >> 4, r16 = lane & 15, q4 = r16 >> 2, p4 = lane & 3, rr = lane >> 3;
  STAMP_AT(0);
  KEEPS(p.ws); KEEPS(p.out); KEEPS(p.w2); KEEPS(p.b2); KEEPS(p.k);
  const int cp = 32 * xcd + idx;
  u64 tag = *(const CAS u64*)(p.ws + WS_EPOCH);
  u32 ddp = *(const CAS u32*)(p.dd + 2 * cp);
  const bool is_g1 = idx < 28;
  const srd_t rx = mk_srd(p.x, S * D * 2 * ZX), rw1 = mk_srd(p.w1, D * F * 2 * ZW1);
  const u32 w1k = w * 1024;
  v4u xr;
  const int stok = 256 * xcd + 8 * idx + w;
  {
    const srd_t rk = mk_srd(p.k, S * D * 2), rv = mk_srd(p.v, S * D * 2);
    const u32 vo = (u32)(rr * 1024 + (((lane & 7) ^ (rr & 2)) << 4)), so = (u32)((64 * idx + 8 * w) * 1024 + xcd * 128);
    DMANT(rk, vo, so, 0, w1k, L_PK + 1024);
    DMANT(rv, vo, so, 0, w1k, L_PV);
    ALD128(xr, (u32)(stok * 1024 + lane * 16), rx, "");
    if (w == 0) DMANT(rk, vo + so - 8192u, 0u, 0, 0u, L_PK);
    if (w == 1) { const srd_t rsk = mk_srd(p.sk, D * 4 * 2); DMA(rsk, (u32)(lane * 16), (u32)(xcd * 512), 0, 0u, L_PW); }
    if (w == 7) {
      u64 pc; u32 len;
      asm volatile("mk_code_begin:\n\ts_getpc_b64 %0\n\ts_mov_b32 %1, mk_code_end-mk_code_begin" : "=s"(pc), "=s"(len));
      const srd_t rc = mk_srd((const void*)pc, len);
      TOUCH(rc, (u32)(lane * 128), (u32)((idx & 3) * 8192), 0);
    }
  }
  SB();
#define TNOFF (4 - (int)(((uintptr_t)p.w2 >> 7) & 7))
  const int ti = 4 * (xcd >> 1) + (idx & 3), t1 = idx >> 2, tn = (t1 + TNOFF) & 7, tj = 7 * (xcd & 1) + t1;
  const int sidx = idx & 3, hm = 4 * (xcd >> 1) + sidx, cgp = 14 * (xcd & 1) + (hm < 14 ? hm : 0);
  const u32 chsw = (u32)(((lane & 7) ^ (rr & 6)) << 4);
  const u32 xvo = (u32)((128 * ti + 8 * w + rr) * 1024 + (((lane & 7) ^ ((4 * w + g) & 7)) << 4));
  const u32 wvo = (u32)((8 * w + (is_g1 ? g : rr)) * 3584 + (is_g1 ? tj * 256 : cgp * 128)) + (is_g1 ? (u32)((r16 ^ (g << 2)) << 4) : chsw);
  const u32 w2k = w * 2048;
#define ROT1 ((t1 + 2 * sidx) & 7)
  u32 sox = (u32)(ROT1) * 128, sow = sox * 1792;
#define NEXT_CHUNK() do { sox = (sox + 128) & 1023; sow = sox * 1792; } while (0)
#define PIECE(c, i) do { \
    if ((i) == 0) DMA(rx, xvo, sox, 0, w1k, ((c) % 3) * L_SLOT); \
    if ((i) == 1) DMA(rx, xvo, sox, 65536, w1k, ((c) % 3) * L_SLOT + 8192); \
    if ((i) == 2) DMA(rw1, wvo, sow, 0, w2k, ((c) % 3) * L_SLOT + 16384); \
    if ((i) == 3) { DMA(rw1, wvo, sow, 4 * 3584, w2k, ((c) % 3) * L_SLOT + 17408); NEXT_CHUNK(); } } while (0)
#define HPIECE(c) DMA(rw1, wvo, 0u, (c) * 64 * 3584, w1k, (c) * 8192)
  BAR();
  if (is_g1) { PIECE(0, 0); PIECE(0, 1); PIECE(0, 2); PIECE(0, 3); }
  else { HPIECE(0); HPIECE(1); HPIECE(2); HPIECE(3); }
  SB();

  const srd_t rws = mk_srd(p.ws, WS_BYTES);
  const int tm = 4 * xcd + sidx;
  const int mi = w & 3, ni = w >> 2, hl = lane >> 5, l31 = lane & 31;
  u32 l0 = lane == 0 ? 0u : 0x80000000u;
  u32 a_kv = L_PK + (32 * ni + 8 * g + 4 + q4) * 128 + (((2 * mi + (p4 >> 1)) ^ (q4 & 2)) << 4) + 8 * (p4 & 1);
  u32 a_tap = L_PW + (16 * mi + r16) * 8;
  u32 o_kv = (u32)((r16 >> 1) * 8192 + (r16 & 1) * 64 + (3 - g) * 16);
  const u32 s_kv = WS_KVT + (u32)((32 * xcd + 8 * mi) * 8192 + (63 - 2 * idx - ni) * 128);
  u32 xa[2][4], wa[2][2];
#pragma unroll
  for (int ks = 0; ks < 4; ++ks) { xa[0][ks] = (32 * mi + l31) * 128 + (((2 * ks + hl) ^ ((l31 >> 1) & 7)) << 4); xa[1][ks] = xa[0][ks] + 2 * L_SLOT; }
#pragma unroll
  for (int n = 0; n < 2; ++n) {
    wa[0][n] = 16384 + (8 * hl + q4) * 256 + (((8 * ni + 4 * n + 2 * (g & 1) + (p4 >> 1)) ^ (q4 << 2)) << 4) + 8 * (p4 & 1);
    wa[1][n] = wa[0][n] + 2 * L_SLOT;
  }
  u32 w2vo = (u32)(rr * 1024) + chsw;
  const u32 w4k = w * 4096;
  u32 ta[4];
#pragma unroll
  for (int t = 0; t < 4; ++t)
    ta[t] = L_SLOT + w4k + (16 * hl + 4 * (g & 1) + q4) * 128 + (((2 * t + (p4 >> 1)) ^ (4 * (g & 1) + (q4 & 2))) << 4) + 8 * (p4 & 1);
  u32 t16 = tid * 16;
  u32 a_u = L_U + ((tid >> 2) & 1) * U_STRIDE + (tid >> 3) * 64 + (tid & 3) * 16;
  u32 a_hc = L_HC + ni * (4 * HC_STRIDE - 4096) + 32 + t16;
  u32 a_A, a_B;
  { const int rho = (15 - r16) & 3;
    a_A = L_HC + (ni * 4 + rho) * HC_STRIDE + (r16 + 1 + rho + 8 * g + 64 * mi) * 2;
    a_B = L_U + ni * U_STRIDE + (240 - 16 * r16 + 8 * g + 64 * mi) * 2; }
  KEEP(l0); KEEP(a_kv); KEEP(a_tap); KEEP(o_kv); KEEP(w2vo); KEEP(t16); KEEP(a_u); KEEP(a_hc); KEEP(a_A); KEEP(a_B);
#pragma unroll
  for (int i = 0; i < 4; ++i) { KEEP(xa[0][i]); KEEP(xa[1][i]); KEEP(ta[i]); KEEP(wa[i >> 1][i & 1]); }
  const srd_t rw2 = mk_srd(p.w2, F * D * 2 * ZW2), rssm = mk_srd(p.ssm + cp * 4096, 8192);
  u32 ksh[7];
#define ROT2 xcd
  { int t = ROT2;
#pragma unroll
    for (int s = 0; s < 7; ++s) { t = t >= 7 ? t - 7 : t; ksh[s] = (u32)t * 1024; KEEPS(ksh[s]); ++t; } }
  const u32 w2base = (u32)(224 * w * 1024 + tn * 128);
  u32 fo1, fo2;
  {
    const int m = 2 * t1 + ni;
    const u32 fkv = WS_FKV + ((31 - 4 * w - p4) * 8 + xcd) * FLAG_STRIDE;
    const u32 ft = WS_FKV + ((16 * (ti & 1) + 4 * mi + p4) * 8 + (ti >> 1)) * FLAG_STRIDE;
    const u32 fc = WS_FST + (((m >> 2) * 2 + (xcd & 1)) * 4 + (m & 3)) * FLAG_STRIDE;
    fo1 = (lane & (is_g1 ? 8 : 0)) ? fc : (lane & (is_g1 ? 4 : 0)) ? ft : fkv;
    const int tja = ((224 * w) >> 7) + p4, tjb = (224 * w + 223) >> 7;
    const u32 fh = WS_FH + ((tm >> 1) * 14 + (tja < tjb ? tja : tjb)) * FLAG_STRIDE, fk = WS_FKV + ((8 * sidx + w) * 8 + xcd) * FLAG_STRIDE;
    fo2 = p4 == 3 ? fk : fh;
  }
  u32 o_xe = WS_STATS + (u32)((128 * ti + 32 * mi + l31) * 32 + hl * 16), o_we = WS_CSUM + (u32)((128 * tj + 64 * ni + lane) * 16);
  u32 l01 = lane < 2 ? lane * 16 : 0x80000000u;
  u32 o_h = (u32)(((l31 >> 4) * 56 * 64 + r16 + 16 * hl) * 16);
  const u32 s_h = WS_H + (u32)(((8 * ti + 2 * mi) * 56 + 4 * tj + 2 * ni) * 1024);
  u32 a_red = L_RED + w * 8192 + ((lane ^ (hl << 3)) << 4);
  u32 hb = WS_H + (u32)(((4 * tm * 56 + 7 * w) * 64 + lane) * 16);
  const u32 s_cp = (u32)cp * 8192;
  KEEP(fo1); KEEP(fo2); KEEP(o_xe); KEEP(o_we); KEEP(l01); KEEP(o_h); KEEP(a_red); KEEP(hb);
  if (w == 2) lw128(L_U + hl * U_STRIDE + 4096 + l31 * 16, v4u{0, 0, 0, 0});
  if (w == 3) lw128(L_HC + rr * HC_STRIDE + (lane & 1) * 16, v4u{0, 0, 0, 0});
  SB();
  VBAR(4);
  PIN(xr);
  STAMP_AT(1);
  tag += 0x9E3779B97F4A7C15ull;
  if (is_g1) { PIECE(1, 0); PIECE(1, 1); }
  else { HPIECE(4); HPIECE(5); }
  {
    const v2u k0 = trread(a_kv), k1 = trread(a_kv + 512), k2 = trread(a_kv + 1024);
    const v2u v0 = trread(a_kv + (L_PV - L_PK - 512)), v1 = trread(a_kv + (L_PV - L_PK));
    const v2u tw = *(LAS v2u*)lp(a_tap);
    float xv8[8];
#pragma unroll
    for (int j = 0; j < 4; ++j) { xv8[2 * j] = bl(xr[j]); xv8[2 * j + 1] = bh(xr[j]); }
    const float mean = wave_sum(((xv8[0] + xv8[1]) + (xv8[2] + xv8[3])) + ((xv8[4] + xv8[5]) + (xv8[6] + xv8[7]))) * (1.0f / D);
    float q0 = 0.f, q1 = 0.f;
#pragma unroll
    for (int j = 0; j < 4; ++j) { const float d0 = xv8[2 * j] - mean, d1 = xv8[2 * j + 1] - mean; q0 += d0 * d0; q1 += d1 * d1; }
    const float var = wave_sum(q0 + q1) * (1.0f / D);
    const float r1 = __builtin_amdgcn_rsqf(var + EPS);
    const float r2 = __builtin_amdgcn_rsqf(var * r1 * r1 + EPS);
    const float kk[12] = {0.f, bh(k0.x), bl(k0.y), bh(k0.y), bl(k1.x), bh(k1.x), bl(k1.y), bh(k1.y), bl(k2.x), bh(k2.x), bl(k2.y), bh(k2.y)};
    const float vv[8] = {bl(v0.x), bh(v0.x), bl(v0.y), bh(v0.y), bl(v1.x), bh(v1.x), bl(v1.y), bh(v1.y)};
    const float w0 = bl(tw.x), w1 = bh(tw.x), w2 = bl(tw.y), w3 = bh(tw.y);
    float o[8];
#pragma unroll
    for (int j = 0; j < 8; ++j) o[j] = (kk[j + 4] * w0 + kk[j + 3] * w1 + kk[j + 2] * w2 + kk[j + 1] * w3) * vv[j];
    st_sc1(rws, o_kv, v4u{pk(o[7], o[6]), pk(o[5], o[4]), pk(o[3], o[2]), pk(o[1], o[0])}, s_kv);
    const float A = r1 * r2, iA = __builtin_amdgcn_rcpf(A);
    float mh, ml, ml2, ih, il, il2;
    split3(-mean, mh, ml, ml2); split3(iA, ih, il, il2);
    const bool l1 = lane & 1;
    st_sc1(rws, l01, v4u{l1 ? asu(A) : pk(mh, mh), l1 ? asu(iA) : pk(ml, ml), l1 ? asu(mean) : pk(ml2, mh), l1 ? asu(r1) : pk(ih, il)}, WS_STATS + (u32)stok * 32);
  }
  SB();
  if (is_g1) { PIECE(1, 2); PIECE(1, 3); }
  else { HPIECE(6); HPIECE(7); }
  if (w == 6) TOUCH(rw2, (u32)(lane * 128), (u32)((224 * tn + 8 * sidx) * 1024) + (ksh[0] << 5), 0);
  SB();
  STAMP_AT(2);
  v4u hd; v2u hp;
  v4u ud;
  v4u w2f[7][4];
  u32 sw2;
#define W2_PIECE(s, i) do { if ((i) == 0) sw2 = w2base + (ksh[s] << 5); DMA(rw2, w2vo, sw2, (i) * 8192, w4k, (((s) + 2) % 3) * L_SLOT + (i) * 1024); } while (0)
#define W2_STAGE(s) do { W2_PIECE(s, 0); W2_PIECE(s, 1); W2_PIECE(s, 2); W2_PIECE(s, 3); } while (0)
#define LOAD_SSM() do { ALD128(hd, t16, rssm, SSM_FL); ALD64(hp, t16 - 8, rssm, SSM_FL); } while (0)
#define LOAD_U() ALD128S(ud, t16, rws, s_cp + WS_KVT, " sc1")
  auto w2_read = [&](int s) {
#pragma unroll
    for (int t = 0; t < 4; ++t) w2f[s][t] = trfrag(ta[t] + ((s + 2) % 3) * L_SLOT - L_SLOT, 1024);
  };
  auto write_hc = [&](int rho) {
    const bool first = (tid & 255) == 0;
    const u32 p0 = first ? 0u : hp.x, p1 = first ? 0u : hp.y;
    const u32 a0 = __builtin_amdgcn_alignbit(p1, p0, 16), a1 = __builtin_amdgcn_alignbit(hd.x, p1, 16), a2 = __builtin_amdgcn_alignbit(hd.y, hd.x, 16),
              a3 = __builtin_amdgcn_alignbit(hd.z, hd.y, 16), a4 = __builtin_amdgcn_alignbit(hd.w, hd.z, 16);
    if (rho == 0) lw128(a_hc, hd);
    if (rho == 1) lw128(a_hc + HC_STRIDE, v4u{a1, a2, a3, a4});
    if (rho == 2) lw128(a_hc + 2 * HC_STRIDE, v4u{p1, hd.x, hd.y, hd.z});
    if (rho == 3) lw128(a_hc + 3 * HC_STRIDE, v4u{a0, a1, a2, a3});
  };

  if (is_g1) {
    f16v acc[2];
#pragma unroll
    for (int i = 0; i < 16; ++i) { acc[0][i] = 0.f; acc[1][i] = 0.f; }
    v4u xf[2][2], wf[2][2][2];
#define REQ(dc, i) do { if ((dc) >= 8) W2_PIECE((dc) & 3, i); else PIECE(dc, i); } while (0)
#define STEP(st, dc, d0, nd) do { \
      constexpr int hs_ = (st) & 1, hn_ = hs_ ^ 1, sl_ = (((st) + 1) >> 1) % 3, sb_ = sl_ >> 1; constexpr u32 so_ = (sl_ & 1) * L_SLOT; \
      constexpr bool rd_ = (st) < 15, mm_ = (st) >= 0; \
      _Pragma("unroll") for (int i_ = 0; i_ < 4; ++i_) { \
        if (mm_) acc[i_ & 1] = mfma32(wf[hs_][i_ >> 1][i_ & 1], xf[hs_][i_ >> 1], acc[i_ & 1]); \
        if (rd_) { \
          if (!(i_ & 1)) xf[hn_][i_ >> 1] = lr128(xa[sb_][2 * hn_ + (i_ >> 1)] + so_); \
          wf[hn_][i_ >> 1][i_ & 1] = trfrag(wa[sb_][i_ & 1] + so_ + (32 * hn_ + 16 * (i_ >> 1)) * 256, 1024); } \
        if ((nd) == 4) { if (i_ == 0) REQ(dc, 0); if (i_ == 1) REQ(dc, 1); if (i_ == 2) REQ(dc, 2); if (i_ == 3) REQ(dc, 3); } \
        if ((nd) == 2) { if (i_ == 1) REQ(dc, d0); if (i_ == 3) REQ(dc, (d0) + 1); } \
        SB(); } } while (0)
#define ITER(c) do { STAMP_F(16 + (c)); STEP(2 * (c) + 1, (c) + 3, 0, 2); STEP(2 * (c) + 2, (c) + 3, 2, 2); } while (0)
    u64 pv; v4u xe, we;
    VBAR(6);
    STAMP_AT(15);
    STEP(-1, 0, 0, 0);
    STEP(0, 2, 0, 4);
    VBAR(4);
    if (w == 0) put_flag(rws, l0, WS_FKV + b * FLAG_STRIDE, tag);
    ITER(0);
    VBAR(4);
    ITER(1);
    VBAR(4);
    ITER(2);
    VBAR(4);
    ITER(3);
    VBAR(4);
    ITER(4);
    ALD64(pv, fo1, rws, " sc1");
    LOAD_SSM();
    VBAR(7);
    STAMP_F(21);
    STEP(11, 8, 0, 2); STEP(12, 8, 2, 2);
    VBAR(6);
    STAMP_F(22);
    PIN(pv);
    wait_flags(rws, fo1, tag, pv);
    ALD128(xe, o_xe, rws, " sc1");
    ALD128(we, o_we, rws, " sc1");
    LOAD_U();
    STEP(13, 9, 0, 2); STEP(14, 9, 2, 2);
    LBAR();
    STAMP_F(23);
    STEP(15, 0, 0, 0);
    WAITV(5);
    PIN(xe); PIN(we); PIN(hd); PIN(hp);
    float cA[2];
    {
      v4u xb;
#pragma unroll
      for (int j = 0; j < 4; ++j) { const auto r = __builtin_amdgcn_permlane32_swap(xe[j], xe[j], false, false); xb[j] = r[0]; if (j < 2) cA[j] = asf(r[1]); }
      const v4u z = {0, 0, 0, 0};
      acc[0] = mfma32(hl ? z : we, xb, acc[0]);
      acc[1] = mfma32(hl ? we : z, xb, acc[1]);
    }
    STAMP_AT(3);
    const float k1s = -2.3022082f * cA[0], k3s = -0.10294324f * cA[0] * cA[0] * cA[0];
    const f2 k1 = {k1s, k1s}, k3 = {k3s, k3s}, iA = {cA[1], cA[1]};
#pragma unroll
    for (int qi = 0; qi < 4; ++qi) {
      const int n = qi >> 1, sp = qi & 1;
      if (qi == 0) { write_hc(0); write_hc(1); W2_PIECE(2, 0); W2_PIECE(2, 1); }
      if (qi == 1) { write_hc(2); write_hc(3); W2_PIECE(2, 2); W2_PIECE(2, 3); }
      if (qi == 2) { WAITV(10); PIN(ud); lw128(a_u, ud); w2_read(0); }
      if (qi == 3) { WAITV(7); w2_read(1); }
      SB();
      v4u o;
#pragma unroll
      for (int j = 0; j < 4; ++j) {
        const f2 a = {acc[n][8 * sp + 2 * j], acc[n][8 * sp + 2 * j + 1]};
        const f2 z = a * __builtin_elementwise_fma(a * a, k3, k1);
        const f2 e = {__builtin_amdgcn_exp2f(z.x), __builtin_amdgcn_exp2f(z.y)};
        const f2 d = __builtin_elementwise_fma(e, iA, iA);
        const f2 hv = a * f2{__builtin_amdgcn_rcpf(d.x), __builtin_amdgcn_rcpf(d.y)};
        o[j] = pk(hv.x, hv.y);
      }
      st_sc1(rws, o_h + n * 1024 + sp * 512, o, s_h);
      SB();
    }
    WAITV(3);
    w2_read(2);
    STAMP_AT(4);
  } else {
    u32 b1v;
    { const srd_t rb1 = mk_srd(p.b1 + 64 * cgp, 128); ALD16(b1v, (u32)(16 * mi + 4 * g + p4) * 2, rb1, ""); }
    VBAR(0);
    if (w == 0) put_flag(rws, l0, WS_FKV + b * FLAG_STRIDE, tag);
    STAMP_AT(15);
    if (w < 4) {
      const v4u ones = {0x3f803f80u, 0x3f803f80u, 0x3f803f80u, 0x3f803f80u};
      f4 s0 = {0, 0, 0, 0};
      const u32 a = (16 * hl + 4 * (g & 1) + q4) * 128 + (((2 * w + (p4 >> 1)) ^ (4 * (g & 1) + (q4 & 2))) << 4) + 8 * (p4 & 1);
#pragma unroll
      for (int st = 0; st < 16; ++st) s0 = mfma(trfrag(a + st * 4096, 1024), ones, s0);
      const float c = p4 == 0 ? s0[0] : p4 == 1 ? s0[1] : p4 == 2 ? s0[2] : s0[3];
      float ch, cl, cl2;
      split3(c, ch, cl, cl2);
      PIN(b1v);
      st_sc1(rws, r16 < 4 && hm < 14 ? WS_CSUM + (u32)(64 * cgp + 16 * w + 4 * g + p4) * 16 : 0x80000000u, v4u{pk(ch, cl), pk(ch, cl), pk(ch, cl2), b1v * 0x10001u});
    }
    VBAR(0);
    if (w == 0) put_flag(rws, l0, WS_FST + (4 * xcd + sidx) * FLAG_STRIDE, tag);
    STAMP_AT(3);
#pragma unroll
    for (int i = 1; i <= NPF; ++i) { u32 kk = ksh[i]; KEEPS(kk); TOUCH(rw2, (u32)(lane * 128), (u32)((224 * w + 8 * sidx) * 1024) + (kk << 5), 0); }
    LOAD_SSM();
    W2_STAGE(0); W2_STAGE(1); W2_STAGE(2);
    if (b == 255) {
      if (w < 4) wait_flags(rws, WS_FKV + tid * FLAG_STRIDE, tag, ~tag);
      BAR();
      if (w == 0) put_flag(rws, l0, WS_EPOCH, tag);
    }
    wait_flags(rws, fo1, tag, ~tag);
    LOAD_U();
    w2_read(0); w2_read(1); w2_read(2);
    WAITV(0);
    PIN(ud); PIN(hd); PIN(hp);
    lw128(a_u, ud);
    write_hc(0); write_hc(1); write_hc(2); write_hc(3);
    STAMP_AT(4);
  }
  LBAR();
  STAMP_AT(5);

  v4u hf[7];
  u32 hbm;
  auto load_h = [&](int m, int s) { if (s == 0) hbm = hb + (u32)(((m + tn) & 3) * 57344); hf[s] = ld_sc1(rws, hbm, ksh[s]); };
  u64 pv2;
  v4u uo;
  {
    auto rdA = [&](int kl, int i) {
      const v2u lo = *(volatile LAS v2u*)lp(a_A + i * 512 + kl * 64), hi = *(volatile LAS v2u*)lp(a_A + i * 512 + kl * 64 + 8);
      return v4u{lo.x, lo.y, hi.x, hi.y};
    };
    auto rdB = [&](int kl, int i) { return lr128(a_B + (7 - i) * 512 + kl * 64); };
    f4 acc[8];
    v4u A[8], B[8], A1[8], B1[8];
    A[0] = rdA(0, 0);
#pragma unroll
    for (int i = 0; i < 8; ++i) B[i] = rdB(0, i);
    A[1] = rdA(0, 1);
#pragma unroll
    for (int kh = 0; kh < 8; ++kh) {
      SB();
      if (kh + 2 < 8) A[kh + 2] = rdA(0, kh + 2);
      if (kh >= 1) { B1[kh - 1] = rdB(1, kh - 1); A1[kh - 1] = rdA(1, kh - 1); }
      if (kh == 6) { B1[7] = rdB(1, 7); A1[7] = rdA(1, 7); uo = lr128(L_U + 4080 + ni * (U_STRIDE + 4096) - t16); }
#pragma unroll
      for (int e = 0; e + kh < 8; ++e) {
        acc[kh + e] = mfma(A[kh], B[e], kh == 0 ? f4{0, 0, 0, 0} : acc[kh + e]);
        constexpr int first[8] = {0, 8, 15, 21, 26, 30, 33, 35};
        const int n = first[kh] + e;
        if (n % 3 == 1) {
          SB();
          if (n / 3 == 0) W2_PIECE(3, 0); if (n / 3 == 1) W2_PIECE(3, 1); if (n / 3 == 2) W2_PIECE(3, 2); if (n / 3 == 3) W2_PIECE(3, 3);
          if (n / 3 == 4) W2_PIECE(4, 0); if (n / 3 == 5) W2_PIECE(4, 1); if (n / 3 == 6) W2_PIECE(4, 2); if (n / 3 == 7) W2_PIECE(4, 3);
          if (n / 3 == 8) W2_PIECE(5, 0); if (n / 3 == 9) W2_PIECE(5, 1); if (n / 3 == 10) W2_PIECE(5, 2); if (n / 3 == 11) W2_PIECE(5, 3);
          SB();
        }
      }
    }
    SB();
    STAMP_AT(6);
    VBAR(12);
    if (is_g1 && w == 0) put_flag(rws, l0, WS_FH + (ti * 14 + tj) * FLAG_STRIDE, tag);
    STAMP_AT(7);
#pragma unroll
    for (int T = 7; T >= 0; --T) {
      SB();
#pragma unroll
      for (int kh = 0; kh <= T; ++kh) acc[T] = mfma(A1[kh], B1[T - kh], acc[T]);
      if (T < 7) lwf4(a_red + (T + 1) * 1024, acc[T + 1]);
      if (T == 4) { SB(); WAITV(8); w2_read(3); }
      if (T == 3) ALD64(pv2, fo2, rws, " sc1");
      if (T == 2) { SB(); WAITL(); W2_STAGE(6); }
    }
    lwf4(a_red, acc[0]);
    SB();
    STAMP_AT(8);
    WAITV(4);
    STAMP_F(29);
    PIN(pv2);
    wait_flags(rws, fo2, tag, pv2);
    STAMP_AT(9);
#pragma unroll
    for (int s = 0; s < 7; ++s) load_h(0, s);
    SB();
    STAMP_F(30);
    w2_read(4); w2_read(5);
  }
  const int tok = tid >> 3, chunk = tid & 7;
  LBAR();
  STAMP_F(31);
  {
    const int t8 = tid & 255, T = t8 >> 5, c16 = (t8 >> 1) & 15, hi = t8 & 1;
    const u32 rp = L_RED + ni * 32768 + T * 1024 + (((c16 ^ (hi << 3)) + 32 * hi) << 4);
    const f4 y0 = (lrf4(rp) + lrf4(rp + 8192)) + (lrf4(rp + 16384) + lrf4(rp + 24576));
    const f4 y1 = (lrf4(rp + 256) + lrf4(rp + 8192 + 256)) + (lrf4(rp + 16384 + 256) + lrf4(rp + 24576 + 256));
    const float dc = w < 4 ? bl(ddp) : bh(ddp);
    const v4u o = {pk(y0[0] + dc * bh(uo.w), y0[1] + dc * bl(uo.w)), pk(y0[2] + dc * bh(uo.z), y0[3] + dc * bl(uo.z)),
                   pk(y1[0] + dc * bh(uo.y), y1[1] + dc * bl(uo.y)), pk(y1[2] + dc * bh(uo.x), y1[3] + dc * bl(uo.x))};
    st_sc1(rws, t16, o, s_cp + WS_YT);
    if (tid == 256) *(LAS u32*)lp(L_CNT) = 0u;
  }
  SB();
  STAMP_F(32);
  VBAR(8);
  w2_read(6);
  STAMP_AT(10);

  const u32 oidx = (u32)((64 * tm + tok) * 512 + 64 * tn + 8 * chunk);
  const u32 fo3 = WS_FY + (32 * tn + 4 * w + p4) * FLAG_STRIDE;
  v4u yv, xv, b2v; v2u stv;
  {
    u64 pv3 = 0;
    const u32 pp0 = w4k + (((lane & 48) | ((lane + 4 * hl) & 15)) << 4);
#pragma unroll
    for (int m = 0; m < 4; ++m) {
      f4 acc[4];
#pragma unroll
      for (int s = 0; s < 7; ++s) {
#pragma unroll
        for (int t = 0; t < 4; ++t) acc[t] = mfma(w2f[s][t], hf[s], s == 0 ? f4{0, 0, 0, 0} : acc[t]);
        if (m < 3) load_h(m + 1, s);
        if (m == 2 && s == 0) pv3 = get_flag(rws, fo3);
        if (m == 3 && s == 1) {
          wait_flags(rws, fo3, tag, pv3);
          yv = ld_sc1(rws, WS_YT + (u32)((64 * tn + tok) * 4096 + (64 * tm + 8 * chunk) * 2));
        }
        SB();
      }
      if (m == 0) {
        WAITV(7);
        arrive_publish(L_CNT, rws, l0, WS_FY + cp * FLAG_STRIDE, tag);
      }
      if (m == 2) {
        xv = *(const GAS v4u*)(p.x + oidx);
        b2v = *(const GAS v4u*)(p.b2 + 64 * tn + 8 * chunk);
        stv = __builtin_amdgcn_raw_buffer_load_b64(rws, WS_STATS + (u32)(64 * tm + tok) * 32 + 24, 0, 16);
      }
      const u32 pm = pp0 + ((m + tn) & 3) * L_SLOT;
#pragma unroll
      for (int t = 0; t < 4; ++t) lwf4((pm ^ ((t & 1) * 128)) + t * 1024, acc[t]);
      SB();
      STAMP_F(24 + m);
    }
  }
  STAMP_AT(11);
  {
    const u32 yp = L_Y + (8 * chunk) * 128 + ((w ^ chunk) << 4) + (tok & 7) * 2;
#pragma unroll
    for (int j = 0; j < 4; ++j) {
      *(LAS u16*)lp(yp + (2 * j) * 128) = (u16)(yv[j] & 0xffff);
      *(LAS u16*)lp(yp + (2 * j + 1) * 128) = (u16)(yv[j] >> 16);
    }
  }
  LBAR();
  STAMP_AT(12);
  {
    const u32 rp = (w >> 1) * L_SLOT + (chunk >> 1) * 1024 + ((32 * (chunk & 1) + ((tok + 4 * (chunk & 3)) & 15)) << 4);
    const f4 o0 = ((lrf4(rp) + lrf4(rp + 4096)) + (lrf4(rp + 8192) + lrf4(rp + 12288))) +
                  ((lrf4(rp + 16384) + lrf4(rp + 20480)) + (lrf4(rp + 24576) + lrf4(rp + 28672)));
    const f4 o1 = ((lrf4(rp + 256) + lrf4(rp + 4352)) + (lrf4(rp + 8448) + lrf4(rp + 12544))) +
                  ((lrf4(rp + 16640) + lrf4(rp + 20736)) + (lrf4(rp + 24832) + lrf4(rp + 28928)));
    const v4u yy = lr128(L_Y + tok * 128 + ((chunk ^ w) << 4));
    const float r1 = asf(stv.y), mr = -asf(stv.x) * r1;
    float res[8];
#pragma unroll
    for (int j = 0; j < 8; ++j) {
      const float ov = (j < 4 ? o0[j & 3] : o1[j & 3]) + ((j & 1) ? bh(b2v[j >> 1]) : bl(b2v[j >> 1]));
      const float gate = ov * __builtin_amdgcn_rcpf(1.0f + __builtin_amdgcn_exp2f(ov * -1.4426950409f));
      const float xx = (j & 1) ? bh(xv[j >> 1]) : bl(xv[j >> 1]);
      const float yj = (j & 1) ? bh(yy[j >> 1]) : bl(yy[j >> 1]);
      res[j] = xx + ((xx * r1 + mr) * yj) * gate;
    }
    *(GAS v4u*)(p.out + oidx) = v4u{pk(res[0], res[1]), pk(res[2], res[3]), pk(res[4], res[5]), pk(res[6], res[7])};
  }
  asm volatile("mk_code_end:");
}
}

extern "C" void kernel_launch(void* const* d_in, const int* in_sizes, int n_in, void* d_out, int out_size, void* d_ws, size_t ws_size,
                              hipStream_t stream) {
  using namespace mk;
  Params p;
  p.x = (const u16*)d_in[0]; p.k = (const u16*)d_in[1]; p.v = (const u16*)d_in[2]; p.sk = (const u16*)d_in[3];
  p.ssm = (const u16*)d_in[4]; p.dd = (const u16*)d_in[5]; p.w1 = (const u16*)d_in[7]; p.b1 = (const u16*)d_in[8];
  p.w2 = (const u16*)d_in[9]; p.b2 = (const u16*)d_in[10];
  p.out = (u16*)d_out; p.ws = (unsigned char*)d_ws;
  (void)hipFuncSetAttribute((const void*)fused, hipFuncAttributeMaxDynamicSharedMemorySize, LDS_BYTES);
  fused<<<256, 512, LDS_BYTES, stream>>>(p);
}
```

```cpp
#include <hip/hip_runtime.h>
#include <stdint.h>
#define OUT_AUX 16
#include <type_traits>
#include <utility>

namespace mk {
typedef unsigned short u16;
typedef unsigned u32;
typedef unsigned long long u64;
typedef u32 v4u __attribute__((ext_vector_type(4)));
typedef u32 v2u __attribute__((ext_vector_type(2)));
typedef float f4 __attribute__((ext_vector_type(4)));
typedef float f2 __attribute__((ext_vector_type(2)));
typedef float f16v __attribute__((ext_vector_type(16)));
typedef short s8v __attribute__((ext_vector_type(8)));
typedef short s4v __attribute__((ext_vector_type(4)));
typedef __bf16 bf2 __attribute__((ext_vector_type(2)));
#define GAS __attribute__((address_space(1)))
#define LAS __attribute__((address_space(3)))
#define CAS __attribute__((address_space(4)))
#define DI __device__ __forceinline__
typedef __amdgpu_buffer_rsrc_t srd_t;
typedef LAS unsigned char* lptr;

constexpr int S = 2048, D = 512, F = 1792;
constexpr float EPS = 1e-5f;
constexpr u32 WS_EPOCH = 0, WS_FKV = 4096, WS_FST = 36864, WS_FH = 40960, WS_FY = 69632, WS_STATS = 131072, WS_CSUM = 196608,
              WS_KVT = 1u << 20, WS_YT = 3u << 20, WS_H = 5u << 20, WS_BYTES = 13u << 20;
constexpr u32 FLAG_STRIDE = 128;
constexpr u32 L_SLOT = 32768;
constexpr u32 L_HC = 98304, HC_STRIDE = 4160, L_U = L_HC + 8 * HC_STRIDE, U_STRIDE = 4608;
constexpr u32 L_PK = L_U + 2 * U_STRIDE, L_PV = L_PK + 72 * 128, L_PW = L_PV + 64 * 128;
constexpr u32 L_JUNK = L_PW + 1024, L_CNT0 = L_JUNK + 256;
constexpr u32 L_Y = L_PK, L_X = L_PK;
constexpr u32 LDS_BYTES = 163840;
static_assert(L_JUNK + 256 <= LDS_BYTES, "LDS map");
constexpr u32 SPIN_CAP = 1u << 16;

struct Params {
  const u16 *x, *k, *v, *sk, *ssm, *dd, *w1, *b1, *w2, *b2;
  u16* out;
  unsigned char* ws;
};

DI srd_t mk_srd(const void* p, u32 bytes) { return __builtin_amdgcn_make_buffer_rsrc((void*)p, (short)0, (int)bytes, 0x00020000); }
DI float asf(u32 d) { return __builtin_bit_cast(float, d); }
DI u32 asu(float f) { return __builtin_bit_cast(u32, f); }
DI bool all_lanes(bool c) { return __builtin_amdgcn_ballot_w64(!c) == 0; }
DI float bl(u32 d) { return asf(d << 16); }
DI float bh(u32 d) { return asf(d & 0xffff0000u); }
DI u32 pk(float a, float b) { f2 t = {a, b}; return __builtin_bit_cast(u32, __builtin_convertvector(t, bf2)); }
DI float rest(float v) { return v - bh(asu(v)); }
DI u32 pkt(float lo, float hi) { return __builtin_amdgcn_perm(asu(hi), asu(lo), 0x07060302u); }
DI f4 mfma(v4u a, v4u b, f4 c) { return __builtin_amdgcn_mfma_f32_16x16x32_bf16(__builtin_bit_cast(s8v, a), __builtin_bit_cast(s8v, b), c, 0, 0, 0); }
DI f16v mfma32(v4u a, v4u b, f16v c) { return __builtin_amdgcn_mfma_f32_32x32x16_bf16(__builtin_bit_cast(s8v, a), __builtin_bit_cast(s8v, b), c, 0, 0, 0); }
DI lptr lp(u32 a) { return (lptr)(uintptr_t)a; }
DI v2u trread(u32 a) { return __builtin_bit_cast(v2u, __builtin_amdgcn_ds_read_tr16_b64_v4i16((LAS s4v*)lp(a))); }
DI v4u trfrag(u32 a, u32 hi_off) { const v2u lo = trread(a), hi = trread(a + hi_off); return v4u{lo.x, lo.y, hi.x, hi.y}; }
DI v4u lr128(u32 a) { return *(LAS v4u*)lp(a); }
DI f4 lrf4(u32 a) { return *(LAS f4*)lp(a); }
DI void lw128(u32 a, v4u v) { *(LAS v4u*)lp(a) = v; }
DI void lwf4(u32 a, f4 v) { *(LAS f4*)lp(a) = v; }
#define DMA_(OP, FL, rs, voff, sb, SI, lb, LI) do { u32 t_; \
  asm volatile("s_add_u32 m0, %1, %2\n\ts_add_u32 %0, %3, %4\n\t" OP " %5, %6, %0 offen" FL " lds" \
               : "=&s"(t_) : "s"(lb), "i"(LI), "s"(sb), "i"(SI), "v"(voff), "s"(rs) : "memory", "scc"); } while (0)
#define DFL ""
#define DMA(rs, voff, sb, SI, lb, LI) DMA_("buffer_load_dwordx4", DFL, rs, voff, sb, SI, lb, LI)
#define SSM_FL ""
#define DMANT(rs, voff, sb, SI, lb, LI) DMA_("buffer_load_dwordx4", " nt", rs, voff, sb, SI, lb, LI)
#define TOUCH(rs, voff, sb, SI) DMA_("buffer_load_dword", "", rs, voff, sb, SI, 0u, L_JUNK)
#define ALD128(d, off, rs, FL) asm volatile("buffer_load_dwordx4 %0, %1, %2, 0 offen" FL : "=v"(d) : "v"(off), "s"(rs) : "memory")
#define ALD128S(d, off, rs, so, FL) asm volatile("buffer_load_dwordx4 %0, %1, %2, %3 offen" FL : "=v"(d) : "v"(off), "s"(rs), "s"(so) : "memory")
#define ALD64(d, off, rs, FL) asm volatile("buffer_load_dwordx2 %0, %1, %2, 0 offen" FL : "=v"(d) : "v"(off), "s"(rs) : "memory")
#define ALD16(d, off, rs, FL) asm volatile("buffer_load_ushort %0, %1, %2, 0 offen" FL : "=v"(d) : "v"(off), "s"(rs) : "memory")
#define WAITV(n) asm volatile("s_waitcnt vmcnt(" #n ")" ::: "memory")
#define PIN(a) asm volatile("" : "+v"(a)::"memory")
#define KEEP(a) asm volatile("" : "+v"(a))
#define KEEPS(a) asm volatile("" : "+s"(a))
#define WAITL() asm volatile("s_waitcnt lgkmcnt(0)" ::: "memory")
#define BAR() __builtin_amdgcn_s_barrier()
#define VBAR(n) asm volatile("s_waitcnt vmcnt(" #n ") lgkmcnt(0)\n\ts_barrier" ::: "memory")
#define LBAR() asm volatile("s_waitcnt lgkmcnt(0)\n\ts_barrier" ::: "memory")
#define SB() __builtin_amdgcn_sched_barrier(0)
DI float wave_sum(float v) {
  v += asf(__builtin_amdgcn_update_dpp(0u, asu(v), 0xB1, 0xF, 0xF, true));
  v += asf(__builtin_amdgcn_update_dpp(0u, asu(v), 0x4E, 0xF, 0xF, true));
  v += asf(__builtin_amdgcn_update_dpp(0u, asu(v), 0x141, 0xF, 0xF, true));
  v += asf(__builtin_amdgcn_update_dpp(0u, asu(v), 0x140, 0xF, 0xF, true));
  const auto a = __builtin_amdgcn_permlane16_swap(asu(v), asu(v), false, false);
  v = asf(a[0]) + asf(a[1]);
  const auto c = __builtin_amdgcn_permlane32_swap(asu(v), asu(v), false, false);
  return asf(c[0]) + asf(c[1]);
}
DI v4u ld_sc1(srd_t rs, u32 off, u32 soff = 0) { return __builtin_amdgcn_raw_buffer_load_b128(rs, off, soff, 16); }
template <int IMM = 0> DI void st_sc1(srd_t rs, u32 off, v4u v, u32 soff = 0) {
  asm volatile("buffer_store_dwordx4 %0, %1, %2, %3 offen offset:%4 sc1\n\ts_nop 1" ::"v"(v), "v"(off), "s"(rs), "s"(soff), "i"(IMM) : "memory");
}
DI void put_flag(srd_t rs, u32 l0, u32 off, u64 tag) { __builtin_amdgcn_raw_buffer_store_b64(__builtin_bit_cast(v2u, tag), rs, l0, off, 16); }
DI u64 get_flag(srd_t rs, u32 off) { return __builtin_bit_cast(u64, __builtin_amdgcn_raw_buffer_load_b64(rs, off, 0, 16)); }
DI void wait_flags(srd_t rs, u32 off, u64 tag, u64 v) {
  if (!all_lanes(v == tag)) {
#pragma unroll 1
    for (u32 spins = 0; spins < SPIN_CAP; ++spins) {
      u64 x;
      asm volatile("buffer_load_dwordx2 %0, %1, %2, 0 offen sc1\n\ts_waitcnt vmcnt(0)" : "=&v"(x) : "v"(off), "s"(rs) : "memory");
      if (all_lanes(x == tag)) break;
      __builtin_amdgcn_s_sleep(1);
    }
  }
  asm volatile("" ::: "memory");
}
DI void arrive_publish(u32 cnt, srd_t rs, u32 l0, u32 off, u64 tag) {
  u32 old;
  asm volatile("s_mov_b64 exec, 1\n\tds_add_rtn_u32 %0, %1, %2\n\ts_mov_b64 exec, -1\n\ts_waitcnt lgkmcnt(0)" : "=&v"(old) : "v"(cnt), "v"(1u) : "memory");
  if (__builtin_amdgcn_readfirstlane(old) == 3u) put_flag(rs, l0, off, tag);
}
#define STAMP_AT(i)
#define STAMP_F(i)
struct ConvList { int kh[144], e[144]; bool newA[144], newB[144]; };
constexpr int KEEP_MASK[2] = {0x99, 0x66};
constexpr int TS[2][4] = {{0, 3, 4, 7}, {1, 2, 5, 6}};
constexpr ConvList make_list(int role) {
  ConvList L = {};
  int n = 0;
  for (int kl = 0; kl < 4; ++kl) {
    bool seen[16] = {};
    for (int part = 0; part < (kl == 3 ? 2 : 1); ++part) {
      const int mask = kl < 3 ? 0xff : KEEP_MASK[part ? role : role ^ 1];
      for (int kh = 0; kh < 8; ++kh)
        for (int T = 7; T >= kh; --T) {
          if (!((mask >> T) & 1)) continue;
          const int e = T - kh;
          L.kh[n] = kh; L.e[n] = e; L.newA[n] = !seen[kh]; L.newB[n] = !seen[8 + e];
          seen[kh] = seen[8 + e] = true;
          ++n;
        }
    }
  }
  return L;
}
template <class F, int... I> DI void static_for_(F&& f, std::integer_sequence<int, I...>) { (f(std::integral_constant<int, I>{}), ...); }
template <int N, class F> DI void static_for(F&& f) { static_for_(f, std::make_integer_sequence<int, N>{}); }
#define CLA 10
#define NPF 0
#define ZX 1
#define ZW1 1
#define ZW2 1

__global__ void __launch_bounds__(512, 2) fused(Params p) {
  const int tid = __builtin_amdgcn_workitem_id_x(), lane = tid & 63, w = __builtin_amdgcn_readfirstlane(tid >> 6);
  const int b = __builtin_amdgcn_workgroup_id_x(), xcd = b & 7, idx = b >> 3;
  const int g = lane >> 4, r16 = lane & 15, q4 = r16 >> 2, p4 = lane & 3, rr = lane >> 3;
  STAMP_AT(0);
  KEEPS(p.ws); KEEPS(p.out); KEEPS(p.w2); KEEPS(p.b2); KEEPS(p.k);
  const int cp = 32 * xcd + idx;
  u64 tag = *(const CAS u64*)(p.ws + WS_EPOCH);
  u32 ddp = *(const CAS u32*)(p.dd + 2 * cp);
  const bool is_g1 = idx < 28;
  const srd_t rx = mk_srd(p.x, S * D * 2 * ZX), rw1 = mk_srd(p.w1, D * F * 2 * ZW1);
  const u32 w1k = w * 1024;
  v4u xr;
  const int stok = 256 * xcd + 8 * idx + w;
  {
    const srd_t rk = mk_srd(p.k, S * D * 2), rv = mk_srd(p.v, S * D * 2);
    const u32 vo = (u32)(rr * 1024 + (((lane & 7) ^ (rr & 2)) << 4)), so = (u32)((64 * idx + 8 * w) * 1024 + xcd * 128);
    DMANT(rk, vo, so, 0, w1k, L_PK + 1024);
    DMANT(rv, vo, so, 0, w1k, L_PV);
    ALD128(xr, (u32)(stok * 1024 + lane * 16), rx, "");
    if (w == 0) DMANT(rk, vo + so - 8192u, 0u, 0, 0u, L_PK);
    if (w == 1) { const srd_t rsk = mk_srd(p.sk, D * 4 * 2); DMA(rsk, (u32)(lane * 16), (u32)(xcd * 512), 0, 0u, L_PW); }
    if (w == 7) {
      u64 pc; u32 len;
      asm volatile("mk_code_begin:\n\ts_getpc_b64 %0\n\ts_mov_b32 %1, mk_code_end-mk_code_begin" : "=s"(pc), "=s"(len));
      const srd_t rc = mk_srd((const void*)pc, len);
      TOUCH(rc, (u32)(lane * 128), (u32)((idx & 3) * 8192), 0);
    }
  }
  SB();
#define TNOFF (4 - (int)(((uintptr_t)p.w2 >> 7) & 7))
  const int ti = 4 * (xcd >> 1) + (idx & 3), t1 = idx >> 2, tn = (t1 + TNOFF) & 7, tj = 7 * (xcd & 1) + t1;
  const int sidx = idx & 3, hm = 4 * (xcd >> 1) + sidx, cgp = 14 * (xcd & 1) + (hm < 14 ? hm : 0);
  const u32 chsw = (u32)(((lane & 7) ^ (rr & 6)) << 4);
  const u32 xvo = (u32)((128 * ti + 8 * w + rr) * 1024 + (((lane & 7) ^ ((4 * w + g) & 7)) << 4));
  const u32 wvo = (u32)((8 * w + (is_g1 ? g : rr)) * 3584 + (is_g1 ? tj * 256 : cgp * 128)) + (is_g1 ? (u32)((r16 ^ (g << 2)) << 4) : chsw);
  const u32 w2k = w * 2048;
#define ROT1 ((t1 + 2 * sidx) & 7)
  u32 sox = (u32)(ROT1) * 128, sow = sox * 1792;
#define NEXT_CHUNK() do { sox = (sox + 128) & 1023; sow = sox * 1792; } while (0)
#define PIECE(c, i) do { \
    if ((i) == 0) DMA(rx, xvo, sox, 0, w1k, ((c) % 3) * L_SLOT); \
    if ((i) == 1) DMA(rx, xvo, sox, 65536, w1k, ((c) % 3) * L_SLOT + 8192); \
    if ((i) == 2) DMA(rw1, wvo, sow, 0, w2k, ((c) % 3) * L_SLOT + 16384); \
    if ((i) == 3) { DMA(rw1, wvo, sow, 4 * 3584, w2k, ((c) % 3) * L_SLOT + 17408); NEXT_CHUNK(); } } while (0)
#define HPIECE(c) DMA(rw1, wvo, 0u, (c) * 64 * 3584, w1k, (c) * 8192)
  BAR();
  if (is_g1) { PIECE(0, 0); PIECE(0, 1); PIECE(0, 2); PIECE(0, 3); }
  else { HPIECE(0); HPIECE(1); HPIECE(2); HPIECE(3); }
  SB();

  const srd_t rws = mk_srd(p.ws, WS_BYTES);
  const int tm = 4 * xcd + sidx;
  const int mi = w & 3, ni = w >> 2, hl = lane >> 5, l31 = lane & 31;
  u32 l0 = lane == 0 ? 0u : 0x80000000u;
  u32 a_kv = L_PK + (32 * ni + 8 * g + 4 + q4) * 128 + (((2 * mi + (p4 >> 1)) ^ (q4 & 2)) << 4) + 8 * (p4 & 1);
  u32 a_tap = L_PW + (16 * mi + r16) * 8;
  u32 o_kv = (u32)((r16 >> 1) * 8192 + (r16 & 1) * 64 + (3 - g) * 16);
  u32 s_st = WS_STATS + (u32)stok * 32;
  u32 s_kv = WS_KVT + (u32)((32 * xcd + 8 * mi) * 8192 + (63 - 2 * idx - ni) * 128);
  u32 xa[2][4], wa[2][2];
#pragma unroll
  for (int ks = 0; ks < 4; ++ks) { xa[0][ks] = (32 * mi + l31) * 128 + (((2 * ks + hl) ^ ((l31 >> 1) & 7)) << 4); xa[1][ks] = xa[0][ks] + 2 * L_SLOT; }
#pragma unroll
  for (int n = 0; n < 2; ++n) {
    wa[0][n] = 16384 + (8 * hl + q4) * 256 + (((8 * ni + 4 * n + 2 * (g & 1) + (p4 >> 1)) ^ (q4 << 2)) << 4) + 8 * (p4 & 1);
    wa[1][n] = wa[0][n] + 2 * L_SLOT;
  }
  constexpr u32 TR_HI = 1024;
  const int trow = 16 * hl + 4 * (g & 1) + q4;
  u32 w2vo = (u32)(rr * 1024) + chsw;
  const u32 w4k = w * 4096;
  u32 ta[4];
#pragma unroll
  for (int t = 0; t < 4; ++t)
    ta[t] = L_SLOT + w4k + trow * 128 + (((2 * t + (p4 >> 1)) ^ (trow & 6)) << 4) + 8 * (p4 & 1);
  u32 t16 = tid * 16;
  u32 a_u = L_U + ((tid >> 2) & 1) * U_STRIDE + (tid >> 3) * 64 + (tid & 3) * 16;
  u32 a_hc = L_HC + ni * (4 * HC_STRIDE - 4096) + 32 + t16;
  u32 a_A, a_B;
  { const int rho = (15 - r16) & 3;
    a_A = L_HC + ((mi >> 1) * 4 + rho) * HC_STRIDE + (r16 + 1 + rho + 8 * g) * 2 + (mi & 1) * 256;
    a_B = L_U + (mi >> 1) * U_STRIDE + (240 - 16 * r16 + 8 * g) * 2 + (mi & 1) * 256; }
  KEEP(l0); KEEP(a_kv); KEEP(a_tap); KEEP(o_kv); KEEP(w2vo); KEEP(t16); KEEP(a_u); KEEP(a_hc); KEEP(a_A); KEEP(a_B);
#pragma unroll
  for (int i = 0; i < 4; ++i) { KEEP(xa[0][i]); KEEP(xa[1][i]); KEEP(ta[i]); KEEP(wa[i >> 1][i & 1]); }
  const srd_t rw2 = mk_srd(p.w2, F * D * 2 * ZW2), rssm = mk_srd(p.ssm + cp * 4096, 8192);
  u32 ksh[7];
#define ROT2 xcd
  { int t = ROT2;
#pragma unroll
    for (int s = 0; s < 7; ++s) { t = t >= 7 ? t - 7 : t; ksh[s] = (u32)t * 1024; KEEPS(ksh[s]); ++t; } }
  u32 w2base = (u32)(224 * w * 1024 + tn * 128);
  u32 fo1, fo2;
  {
    const int m = 2 * t1 + ni;
    const u32 fkv = WS_FKV + ((31 - 4 * w - p4) * 8 + xcd) * FLAG_STRIDE;
    const u32 ft = WS_FKV + ((16 * (ti & 1) + 4 * mi + p4) * 8 + (ti >> 1)) * FLAG_STRIDE;
    const u32 fc = WS_FST + (((m >> 2) * 2 + (xcd & 1)) * 4 + (m & 3)) * FLAG_STRIDE;
    const u32 fk = WS_FKV + ((8 * sidx + w) * 8 + xcd) * FLAG_STRIDE;
    fo1 = (lane & 16) ? fk : (lane & (is_g1 ? 8 : 0)) ? fc : (lane & (is_g1 ? 4 : 0)) ? ft : fkv;
    const int tja = ((224 * w) >> 7) + p4, tjb = (224 * w + 223) >> 7;
    fo2 = WS_FH + ((tm >> 1) * 14 + (tja < tjb ? tja : tjb)) * FLAG_STRIDE;
  }
  u32 o_xe = WS_STATS + (u32)((128 * ti + 32 * mi + l31) * 32 + hl * 16), o_we = WS_CSUM + (u32)((128 * tj + 64 * ni + lane) * 16);
  u32 o_out = (u32)((64 * tm + (tid >> 3)) * 1024 + 128 * tn + (tid & 7) * 16);
  KEEP(o_out);
  u32 l01 = lane < 2 ? lane * 16 : 0x80000000u;
  u32 o_h = (u32)(((l31 >> 4) * 56 * 64 + r16 + 16 * hl) * 16);
  u32 s_h = WS_H + (u32)(((8 * ti + 2 * mi) * 56 + 4 * tj + 2 * ni) * 1024);
  u32 hb = WS_H + (u32)(((4 * tm * 56 + 7 * w) * 64 + lane) * 16);
  u32 s_cp = (u32)cp * 8192, s_t0 = (u32)((224 * tn + 8 * sidx) * 1024) + (ksh[0] << 5);
  u32 f_kv = WS_FKV + b * FLAG_STRIDE, f_h = WS_FH + (ti * 14 + tj) * FLAG_STRIDE;
  KEEPS(s_st); KEEPS(s_kv); KEEPS(w2base); KEEPS(s_h); KEEPS(s_cp); KEEPS(s_t0); KEEPS(f_kv); KEEPS(f_h);
  KEEP(fo1); KEEP(fo2); KEEP(o_xe); KEEP(o_we); KEEP(l01); KEEP(o_h); KEEP(hb);
  if (w == 2) lw128(L_U + hl * U_STRIDE + 4096 + l31 * 16, v4u{0, 0, 0, 0});
  if (w == 3) lw128(L_CNT0 + (lane & 1) * 16, v4u{0, 0, 0, 0});
  SB();
  VBAR(4);
  PIN(xr);
  STAMP_AT(1);
  tag += 0x9E3779B97F4A7C15ull;
  if (is_g1) { PIECE(1, 0); PIECE(1, 1); }
  else { HPIECE(4); HPIECE(5); }
  {
    const v2u k0 = trread(a_kv), k1 = trread(a_kv + 512), k2 = trread(a_kv + 1024);
    const v2u v0 = trread(a_kv + (L_PV - L_PK - 512)), v1 = trread(a_kv + (L_PV - L_PK));
    const v2u tw = *(LAS v2u*)lp(a_tap);
    SB();
    float xv8[8];
#pragma unroll
    for (int j = 0; j < 4; ++j) { xv8[2 * j] = bl(xr[j]); xv8[2 * j + 1] = bh(xr[j]); }
    const float mean = wave_sum(((xv8[0] + xv8[1]) + (xv8[2] + xv8[3])) + ((xv8[4] + xv8[5]) + (xv8[6] + xv8[7]))) * (1.0f / D);
    float q0 = 0.f, q1 = 0.f;
#pragma unroll
    for (int j = 0; j < 4; ++j) { const float d0 = xv8[2 * j] - mean, d1 = xv8[2 * j + 1] - mean; q0 += d0 * d0; q1 += d1 * d1; }
    const float var = wave_sum(q0 + q1) * (1.0f / D);
    const float r1 = __builtin_amdgcn_rsqf(var + EPS);
    const float r2 = __builtin_amdgcn_rsqf(var * r1 * r1 + EPS);
    const float kk[12] = {0.f, bh(k0.x), bl(k0.y), bh(k0.y), bl(k1.x), bh(k1.x), bl(k1.y), bh(k1.y), bl(k2.x), bh(k2.x), bl(k2.y), bh(k2.y)};
    const float vv[8] = {bl(v0.x), bh(v0.x), bl(v0.y), bh(v0.y), bl(v1.x), bh(v1.x), bl(v1.y), bh(v1.y)};
    const float w0 = bl(tw.x), w1 = bh(tw.x), w2 = bl(tw.y), w3 = bh(tw.y);
    float o[8];
#pragma unroll
    for (int j = 0; j < 8; ++j) o[j] = (kk[j + 4] * w0 + kk[j + 3] * w1 + kk[j + 2] * w2 + kk[j + 1] * w3) * vv[j];
    st_sc1(rws, o_kv, v4u{pk(o[7], o[6]), pk(o[5], o[4]), pk(o[3], o[2]), pk(o[1], o[0])}, s_kv);
    const float A = r1 * r2, m0 = -mean, m1 = rest(m0), m2 = rest(m1);
    float iA = __builtin_amdgcn_rcpf(A);
    u32 d0 = pkt(m0, m0), d1 = pkt(m1, m1), d2 = pkt(m2, m0), d3 = pkt(iA, rest(iA));
    KEEP(d0); KEEP(d1); KEEP(d2); KEEP(d3);
    const bool l1 = lane & 1;
    st_sc1(rws, l01, v4u{l1 ? asu(A) : d0, l1 ? asu(iA) : d1, l1 ? asu(mean) : d2, l1 ? asu(r1) : d3}, s_st);
  }
  SB();
  if (is_g1) { PIECE(1, 2); PIECE(1, 3); }
  else { HPIECE(6); HPIECE(7); }
  if (w == 6) TOUCH(rw2, (u32)(lane * 128), s_t0, 0);
  SB();
  STAMP_AT(2);
  v4u hd; v2u hp;
  v4u ud;
  v4u w2f[7][4];
  u32 sw2;
#define W2SLOT(s) ((s) == 0 ? 3 : ((s) + 1) % 3)
#define W2_PIECE(s, i) do { if ((i) == 0) sw2 = w2base + (ksh[s] << 5); DMA(rw2, w2vo, sw2, (i) * 8192, w4k, W2SLOT(s) * L_SLOT + (i) * 1024); } while (0)
#define W2_STAGE(s) do { W2_PIECE(s, 0); W2_PIECE(s, 1); W2_PIECE(s, 2); W2_PIECE(s, 3); } while (0)
  v4u xv, b2v; v2u stv;
#define LOAD_SSM() do { ALD128(hd, t16, rssm, SSM_FL); ALD64(hp, t16 - 8, rssm, SSM_FL); ALD128(xv, o_out, rx, ""); \
    const srd_t rb2 = mk_srd(p.b2, D * 2); ALD128(b2v, o_out & 1023u, rb2, ""); } while (0)
#define LOAD_U() do { ALD128S(ud, t16, rws, s_cp + WS_KVT, " sc1"); ALD64(stv, WS_STATS + 24 + (o_out >> 10) * 32, rws, " sc1"); } while (0)
  auto w2_read = [&](int s) {
#pragma unroll
    for (int t = 0; t < 4; ++t) w2f[s][t] = trfrag(ta[t] + W2SLOT(s) * L_SLOT - L_SLOT, TR_HI);
  };
  auto write_hc = [&](int rho) {
    const bool first = (tid & 255) == 0;
    const u32 p0 = first ? 0u : hp.x, p1 = first ? 0u : hp.y;
    const u32 a0 = __builtin_amdgcn_alignbit(p1, p0, 16), a1 = __builtin_amdgcn_alignbit(hd.x, p1, 16), a2 = __builtin_amdgcn_alignbit(hd.y, hd.x, 16),
              a3 = __builtin_amdgcn_alignbit(hd.z, hd.y, 16), a4 = __builtin_amdgcn_alignbit(hd.w, hd.z, 16);
    if (rho == 0) lw128(a_hc, hd);
    if (rho == 1) lw128(a_hc + HC_STRIDE, v4u{a1, a2, a3, a4});
    if (rho == 2) lw128(a_hc + 2 * HC_STRIDE, v4u{p1, hd.x, hd.y, hd.z});
    if (rho == 3) lw128(a_hc + 3 * HC_STRIDE, v4u{a0, a1, a2, a3});
  };

#define CONV_SETUP() do { PIN(ud); PIN(hd); PIN(hp); lw128(a_u, ud); write_hc(0); write_hc(1); write_hc(2); write_hc(3); \
    if (w == 4) lw128(L_HC + rr * HC_STRIDE + (lane & 1) * 16, v4u{0, 0, 0, 0}); } while (0)

  if (is_g1) {
    f16v acc[2];
#pragma unroll
    for (int i = 0; i < 16; ++i) { acc[0][i] = 0.f; acc[1][i] = 0.f; }
    v4u xf[2][2], wf[2][2][2];
#define REQ(dc, i) do { if ((dc) >= 8) W2_PIECE((dc) & 3, i); else PIECE(dc, i); } while (0)
#define STEP(st, dc, d0, nd) do { \
      constexpr int hs_ = (st) & 1, hn_ = hs_ ^ 1, sl_ = (((st) + 1) >> 1) % 3, sb_ = sl_ >> 1; constexpr u32 so_ = (sl_ & 1) * L_SLOT; \
      constexpr bool rd_ = (st) < 15, mm_ = (st) >= 0; \
      _Pragma("unroll") for (int i_ = 0; i_ < 4; ++i_) { \
        if (mm_) acc[i_ & 1] = mfma32(wf[hs_][i_ >> 1][i_ & 1], xf[hs_][i_ >> 1], acc[i_ & 1]); \
        if (rd_) { \
          if (!(i_ & 1)) xf[hn_][i_ >> 1] = lr128(xa[sb_][2 * hn_ + (i_ >> 1)] + so_); \
          wf[hn_][i_ >> 1][i_ & 1] = trfrag(wa[sb_][i_ & 1] + so_ + (32 * hn_ + 16 * (i_ >> 1)) * 256, 1024); } \
        if ((nd) == 4) { if (i_ == 0) REQ(dc, 0); if (i_ == 1) REQ(dc, 1); if (i_ == 2) REQ(dc, 2); if (i_ == 3) REQ(dc, 3); } \
        if ((nd) == 2) { if (i_ == 1) REQ(dc, d0); if (i_ == 3) REQ(dc, (d0) + 1); } \
        SB(); } } while (0)
#define ITER(c) do { STAMP_F(16 + (c)); STEP(2 * (c) + 1, (c) + 3, 0, 2); STEP(2 * (c) + 2, (c) + 3, 2, 2); } while (0)
    u64 pv; v4u xe, we;
    VBAR(6);
    STAMP_AT(15);
    STEP(-1, 0, 0, 0);
    STEP(0, 2, 0, 4);
    VBAR(4);
    if (w == 0) put_flag(rws, l0, f_kv, tag);
    ITER(0); W2_PIECE(0, 0);
    VBAR(5);
    ITER(1); W2_PIECE(0, 1);
    VBAR(6);
    ITER(2); W2_PIECE(0, 2);
    VBAR(6);
    ITER(3); W2_PIECE(0, 3);
    VBAR(6);
    ITER(4);
    ALD64(pv, fo1, rws, " sc1");
    LOAD_SSM();
    VBAR(10);
    STAMP_F(21);
    STEP(11, 9, 0, 2); STEP(12, 9, 2, 2);
    VBAR(8);
    STAMP_F(22);
    PIN(pv);
    wait_flags(rws, fo1, tag, pv);
    ALD128(xe, o_xe, rws, " sc1");
    ALD128(we, o_we, rws, " sc1");
    LOAD_U();
    STEP(13, 10, 0, 2); w2_read(0); STEP(14, 10, 2, 2);
    LBAR();
    STAMP_F(23);
    STEP(15, 0, 0, 0);
    WAITV(6);
    PIN(xe); PIN(we);
    float cA[2];
    {
      v4u xb;
#pragma unroll
      for (int j = 0; j < 4; ++j) { const auto r = __builtin_amdgcn_permlane32_swap(xe[j], xe[j], false, false); xb[j] = r[0]; if (j < 2) cA[j] = asf(r[1]); }
      const v4u z = {0, 0, 0, 0};
      acc[0] = mfma32(hl ? z : we, xb, acc[0]);
      acc[1] = mfma32(hl ? we : z, xb, acc[1]);
    }
    STAMP_AT(3);
    if (ni) { WAITV(5); CONV_SETUP(); SB(); }
    const float k1s = -2.3022082f * cA[0], k3s = -0.10294324f * cA[0] * cA[0] * cA[0];
    const f2 k1 = {k1s, k1s}, k3 = {k3s, k3s}, iA = {cA[1], cA[1]};
#pragma unroll
    for (int qi = 0; qi < 4; ++qi) {
      const int n = qi >> 1, sp = qi & 1;
      if (qi == 0) W2_PIECE(3, 0);
      if (qi == 1) W2_PIECE(3, 1);
      if (qi == 2) W2_PIECE(3, 2);
      if (qi == 3) W2_PIECE(3, 3);
      v4u o;
#pragma unroll
      for (int j = 0; j < 4; ++j) {
        const f2 a = {acc[n][8 * sp + 2 * j], acc[n][8 * sp + 2 * j + 1]};
        const f2 z = a * __builtin_elementwise_fma(a * a, k3, k1);
        const f2 e = {__builtin_amdgcn_exp2f(z.x), __builtin_amdgcn_exp2f(z.y)};
        const f2 d = __builtin_elementwise_fma(e, iA, iA);
        const f2 hv = a * f2{__builtin_amdgcn_rcpf(d.x), __builtin_amdgcn_rcpf(d.y)};
        o[j] = pk(hv.x, hv.y);
      }
      if (qi == 0) st_sc1<0>(rws, o_h, o, s_h);
      if (qi == 1) st_sc1<512>(rws, o_h, o, s_h);
      if (qi == 2) st_sc1<1024>(rws, o_h, o, s_h);
      if (qi == 3) st_sc1<1536>(rws, o_h, o, s_h);
      SB();
    }
    STAMP_AT(4);
    if (!ni) { WAITV(8); CONV_SETUP(); }
  } else {
    u32 b1v;
    { const srd_t rb1 = mk_srd(p.b1 + 64 * cgp, 128); ALD16(b1v, (u32)(16 * mi + 4 * g + p4) * 2, rb1, ""); }
    VBAR(0);
    if (w == 0) put_flag(rws, l0, f_kv, tag);
    STAMP_AT(15);
    if (w < 4) {
      const v4u ones = {0x3f803f80u, 0x3f803f80u, 0x3f803f80u, 0x3f803f80u};
      f4 s0 = {0, 0, 0, 0};
      const u32 a = trow * 128 + (((2 * w + (p4 >> 1)) ^ (trow & 6)) << 4) + 8 * (p4 & 1);
#pragma unroll
      for (int st = 0; st < 16; ++st) s0 = mfma(trfrag(a + st * 4096, TR_HI), ones, s0);
      const float ca = (p4 & 1) ? s0[1] : s0[0], cb = (p4 & 1) ? s0[3] : s0[2], c = (p4 & 2) ? cb : ca;
      const float c1 = rest(c), c2 = rest(c1);
      PIN(b1v);
      st_sc1(rws, r16 < 4 && hm < 14 ? WS_CSUM + (u32)(64 * cgp + 16 * w + 4 * g + p4) * 16 : 0x80000000u, v4u{pkt(c, c1), pkt(c, c1), pkt(c, c2), b1v * 0x10001u});
    }
    VBAR(0);
    if (w == 0) put_flag(rws, l0, WS_FST + (4 * xcd + sidx) * FLAG_STRIDE, tag);
    STAMP_AT(3);
#pragma unroll
    for (int i = 1; i <= NPF; ++i) { u32 kk = ksh[i]; KEEPS(kk); TOUCH(rw2, (u32)(lane * 128), (u32)((224 * w + 8 * sidx) * 1024) + (kk << 5), 0); }
    LOAD_SSM();
    W2_STAGE(0); W2_STAGE(1); W2_STAGE(2); W2_STAGE(3);
    if (b == 255) {
      if (w < 4) wait_flags(rws, WS_FKV + tid * FLAG_STRIDE, tag, ~tag);
      BAR();
      if (w == 0) put_flag(rws, l0, WS_EPOCH, tag);
    }
    wait_flags(rws, fo1, tag, ~tag);
    LOAD_U();
    w2_read(0);
    STAMP_AT(4);
    VBAR(0);
    CONV_SETUP();
  }
  v4u hf[7];
  u32 hbm;
#define MROT (2 * tn)
  const int mrot = MROT;
  auto load_h = [&](int m, int s) { if (s == 0) hbm = hb + (u32)(((m + mrot) & 3) * 57344); hf[s] = ld_sc1(rws, hbm, ksh[s]); };
  if (!ni) {
    WAITV(0);
    w2_read(1); w2_read(2); w2_read(3);
    v4u AD;
    { const u32 dv = (mi & 2) ? ddp >> 16 : ddp & 0xffffu; const int pos = 15 - r16 - 8 * g;
#pragma unroll
      for (int j = 0; j < 4; ++j) AD[j] = (pos >> 1) == j ? dv << ((pos & 1) * 16) : 0u; }
    u32 a_x = L_X + w4k + lane * 16, a_B0 = a_B - (mi & 1) * 256;
    u32 o_y = r16 * 32 + hl * 16 + (g & 1) * ((mi & 1) ? 512u : 1536u), s_y = s_cp + WS_YT + (mi >> 1) * 4096 + (mi & 1) * 512;
    KEEP(a_x); KEEP(a_B0); KEEP(o_y); KEEPS(s_y);
    LBAR();
    STAMP_AT(5);
    f4 acc[4];
    u64 pv2;
    auto conv = [&](auto ROLE) {
      constexpr int role = decltype(ROLE)::value;
      constexpr ConvList CL = make_list(role);
      f4 ac[8];
      v4u fa[4][8], fb[4][8], fd[4];
      auto rd = [&](auto N) {
        constexpr int n = decltype(N)::value, kl = n / 36;
        if constexpr (n < 144) {
          if constexpr (CL.newA[n]) {
            const v2u lo = *(volatile LAS v2u*)lp(a_A + CL.kh[n] * 512 + kl * 64), hi = *(volatile LAS v2u*)lp(a_A + CL.kh[n] * 512 + kl * 64 + 8);
            fa[kl][CL.kh[n]] = v4u{lo.x, lo.y, hi.x, hi.y};
          }
          if constexpr (CL.newB[n]) fb[kl][CL.e[n]] = lr128(a_B + (7 - CL.e[n]) * 512 + kl * 64);
        } else if constexpr (n < 148) fd[n - 144] = lr128(a_B0 + (7 - TS[role][n - 144]) * 512);
      };
      static_for<CLA>(rd);
      static_for<144>([&](auto N) {
        constexpr int n = decltype(N)::value, kl = n / 36, T = CL.kh[n] + CL.e[n];
        ac[T] = mfma(fa[kl][CL.kh[n]], fb[kl][CL.e[n]], n < 8 ? f4{0, 0, 0, 0} : ac[T]);
        rd(std::integral_constant<int, n + CLA>{});
        if constexpr (n >= 9 && n < 9 + 12 * 5 && (n - 9) % 5 == 0) { constexpr int q = (n - 9) / 5; W2_PIECE(4 + q / 4, q % 4); }
        if constexpr (n == 100) ALD64(pv2, fo2, rws, " sc1");
        if constexpr (n >= 128 && n < 136 && (n & 1) == 0) lwf4(a_x + ((n - 128) / 2) * 1024, ac[TS[role ^ 1][(n - 128) / 2]]);
        if constexpr (n == 136) *(volatile LAS u32*)lp(L_CNT0 + 16 + w * 4) = 1u;
        SB();
      });
#pragma unroll
      for (int j = 0; j < 4; ++j) acc[j] = mfma(AD, fd[j], ac[TS[role][j]]);
    };
    if (mi & 1) conv(std::integral_constant<int, 1>{}); else conv(std::integral_constant<int, 0>{});
    STAMP_AT(8);
#pragma unroll 1
    for (u32 spins = 0; spins < SPIN_CAP && *(volatile LAS u32*)lp(L_CNT0 + 16 + (w ^ 1) * 4) == 0u; ++spins) __builtin_amdgcn_s_sleep(1);
    asm volatile("" ::: "memory");
#pragma unroll
    for (int j = 0; j < 4; ++j) acc[j] += lrf4(a_x + ((mi & 1) ? -4096 : 4096) + j * 1024);
#pragma unroll
    for (int j = 0; j < 4; j += 2) {
      const auto r0 = __builtin_amdgcn_permlane16_swap(pk(acc[j][0], acc[j][1]), pk(acc[j + 1][0], acc[j + 1][1]), false, false);
      const auto r1 = __builtin_amdgcn_permlane16_swap(pk(acc[j][2], acc[j][3]), pk(acc[j + 1][2], acc[j + 1][3]), false, false);
      if (j == 0) st_sc1<0>(rws, o_y, v4u{r0[0], r1[0], r0[1], r1[1]}, s_y);
      else st_sc1<2048>(rws, o_y, v4u{r0[0], r1[0], r0[1], r1[1]}, s_y);
    }
    WAITV(2);
    PIN(pv2);
    wait_flags(rws, fo2, tag, pv2);
  } else {
    LBAR();
    STAMP_AT(5);
    WAITV(0);
    if (is_g1) arrive_publish(L_CNT0, rws, l0, f_h, tag);
    w2_read(1); w2_read(2); w2_read(3);
    WAITL();
    W2_STAGE(4); W2_STAGE(5); W2_STAGE(6);
    STAMP_AT(8);
    wait_flags(rws, fo2, tag, ~tag);
  }
  STAMP_AT(9);
#pragma unroll
  for (int s = 0; s < 7; ++s) load_h(0, s);
  SB();
  w2_read(4); w2_read(5); w2_read(6);
  if (!ni) {
    WAITV(7);
    arrive_publish(L_CNT0 + 4, rws, l0, WS_FY + cp * FLAG_STRIDE, tag);
  }
  STAMP_AT(10);
  const int tok = tid >> 3, chunk = tid & 7;

  const u32 fo3 = WS_FY + (32 * tn + 4 * w + p4) * FLAG_STRIDE;
  v4u yv;
  PIN(xv); PIN(b2v); PIN(stv);
  {
    u64 pv3 = 0;
    const u32 pp0 = w4k + (((lane & 48) | ((lane + 4 * hl) & 15)) << 4);
    f4 acc[2][4];
#pragma unroll
    for (int m = 0; m < 4; ++m) {
#pragma unroll
      for (int s = 0; s < 7; ++s) {
#pragma unroll
        for (int t = 0; t < 4; ++t) acc[m & 1][t] = mfma(w2f[s][t], hf[s], s == 0 ? f4{0, 0, 0, 0} : acc[m & 1][t]);
        if (m < 3) load_h(m + 1, s);
        if (m > 0 && s >= 1 && s <= 4) lwf4(((pp0 + (m - 1) * L_SLOT) ^ (((s - 1) & 1) * 128)) + (s - 1) * 1024, acc[(m - 1) & 1][s - 1]);
        if (m == 2 && s == 0) pv3 = get_flag(rws, fo3);
        if (m == 3 && s == 1) {
          STAMP_F(28);
          wait_flags(rws, fo3, tag, pv3);
          STAMP_F(29);
          yv = ld_sc1(rws, WS_YT + (u32)((64 * tn + tok) * 4096 + (64 * tm + 8 * chunk) * 2));
        }
        SB();
      }
      STAMP_F(24 + m);
    }
#pragma unroll 1
    for (u32 spins = 0; spins < SPIN_CAP && *(volatile LAS u32*)lp(L_CNT0 + 4) < 4u; ++spins) __builtin_amdgcn_s_sleep(1);
#pragma unroll
    for (int t = 0; t < 4; ++t) lwf4(((pp0 + 3 * L_SLOT) ^ ((t & 1) * 128)) + t * 1024, acc[1][t]);
  }
  STAMP_AT(11);
  {
    const u32 yp = L_Y + (8 * chunk) * 128 + ((w ^ chunk) << 4) + (tok & 7) * 2;
#pragma unroll
    for (int j = 0; j < 4; ++j) {
      *(LAS u16*)lp(yp + (2 * j) * 128) = (u16)(yv[j] & 0xffff);
      *(LAS u16*)lp(yp + (2 * j + 1) * 128) = (u16)(yv[j] >> 16);
    }
  }
  LBAR();
  STAMP_AT(12);
  {
    const u32 rp = (((w >> 1) - mrot) & 3) * L_SLOT + (chunk >> 1) * 1024 + ((32 * (chunk & 1) + ((tok + 4 * (chunk & 3)) & 15)) << 4);
    const f4 o0 = ((lrf4(rp) + lrf4(rp + 4096)) + (lrf4(rp + 8192) + lrf4(rp + 12288))) +
                  ((lrf4(rp + 16384) + lrf4(rp + 20480)) + (lrf4(rp + 24576) + lrf4(rp + 28672)));
    const f4 o1 = ((lrf4(rp + 256) + lrf4(rp + 4352)) + (lrf4(rp + 8448) + lrf4(rp + 12544))) +
                  ((lrf4(rp + 16640) + lrf4(rp + 20736)) + (lrf4(rp + 24832) + lrf4(rp + 28928)));
    const v4u yy = lr128(L_Y + tok * 128 + ((chunk ^ w) << 4));
    const float r1 = asf(stv.y), mr = -asf(stv.x) * r1;
    float res[8];
#pragma unroll
    for (int j = 0; j < 8; ++j) {
      const float ov = (j < 4 ? o0[j & 3] : o1[j & 3]) + ((j & 1) ? bh(b2v[j >> 1]) : bl(b2v[j >> 1]));
      const float gate = ov * __builtin_amdgcn_rcpf(1.0f + __builtin_amdgcn_exp2f(ov * -1.4426950409f));
      const float xx = (j & 1) ? bh(xv[j >> 1]) : bl(xv[j >> 1]);
      const float yj = (j & 1) ? bh(yy[j >> 1]) : bl(yy[j >> 1]);
      res[j] = xx + ((xx * r1 + mr) * yj) * gate;
    }
    __builtin_amdgcn_raw_buffer_store_b128(v4u{pk(res[0], res[1]), pk(res[2], res[3]), pk(res[4], res[5]), pk(res[6], res[7])}, mk_srd(p.out, S * D * 2), o_out, 0, OUT_AUX);
  }
  asm volatile("mk_code_end:");
}
}

extern "C" void kernel_launch(void* const* d_in, const int* in_sizes, int n_in, void* d_out, int out_size, void* d_ws, size_t ws_size,
                              hipStream_t stream) {
  using namespace mk;
  Params p;
  p.x = (const u16*)d_in[0]; p.k = (const u16*)d_in[1]; p.v = (const u16*)d_in[2]; p.sk = (const u16*)d_in[3];
  p.ssm = (const u16*)d_in[4]; p.dd = (const u16*)d_in[5]; p.w1 = (const u16*)d_in[7]; p.b1 = (const u16*)d_in[8];
  p.w2 = (const u16*)d_in[9]; p.b2 = (const u16*)d_in[10];
  p.out = (u16*)d_out; p.ws = (unsigned char*)d_ws;
  (void)hipFuncSetAttribute((const void*)fused, hipFuncAttributeMaxDynamicSharedMemorySize, LDS_BYTES);
  fused<<<256, 512, LDS_BYTES, stream>>>(p);
}
```

```cpp
#include <hip/hip_runtime.h>
#include <stdint.h>
#define PROBE 0
#define PCYC 0
#define ROT1 xcd
#define OUT_AUX 16
#include <type_traits>
#include <utility>

namespace mk {
typedef unsigned short u16;
typedef unsigned u32;
typedef unsigned long long u64;
typedef u32 v4u __attribute__((ext_vector_type(4)));
typedef u32 v2u __attribute__((ext_vector_type(2)));
typedef float f4 __attribute__((ext_vector_type(4)));
typedef float f2 __attribute__((ext_vector_type(2)));
typedef float f16v __attribute__((ext_vector_type(16)));
typedef short s8v __attribute__((ext_vector_type(8)));
typedef short s4v __attribute__((ext_vector_type(4)));
typedef __bf16 bf2 __attribute__((ext_vector_type(2)));
#define GAS __attribute__((address_space(1)))
#define LAS __attribute__((address_space(3)))
#define CAS __attribute__((address_space(4)))
#define DI __device__ __forceinline__
typedef __amdgpu_buffer_rsrc_t srd_t;
typedef LAS unsigned char* lptr;

constexpr int S = 2048, D = 512, F = 1792;
constexpr float EPS = 1e-5f;
constexpr u32 WS_EPOCH = 0, WS_FKV = 4096, WS_FST = 36864, WS_FH = 40960, WS_FY = 69632, WS_STATS = 131072, WS_CSUM = 196608,
              WS_KVT = 1u << 20, WS_YT = 3u << 20, WS_H = 5u << 20, WS_BYTES = 13u << 20;
constexpr u32 FLAG_STRIDE = 128;
constexpr u32 L_SLOT = 32768;
constexpr u32 L_HC = 98304, HC_STRIDE = 4160, L_U = L_HC + 8 * HC_STRIDE, U_STRIDE = 4608;
constexpr u32 L_PK = L_U + 2 * U_STRIDE, L_PV = L_PK + 72 * 128, L_PW = L_PV + 64 * 128;
constexpr u32 L_JUNK = L_PW + 1024, L_CNT0 = L_JUNK + 256;
constexpr u32 L_Y = L_PK, L_X = L_PK;
constexpr u32 LDS_BYTES = 163840;
static_assert(L_JUNK + 256 <= LDS_BYTES, "LDS map");
constexpr u32 SPIN_CAP = 1u << 16;

struct Params {
  const u16 *x, *k, *v, *sk, *ssm, *dd, *w1, *b1, *w2, *b2;
  u16* out;
  unsigned char* ws;
};

DI srd_t mk_srd(const void* p, u32 bytes) { return __builtin_amdgcn_make_buffer_rsrc((void*)p, (short)0, (int)bytes, 0x00020000); }
DI float asf(u32 d) { return __builtin_bit_cast(float, d); }
DI u32 asu(float f) { return __builtin_bit_cast(u32, f); }
DI bool all_lanes(bool c) { return __builtin_amdgcn_ballot_w64(!c) == 0; }
DI float bl(u32 d) { return asf(d << 16); }
DI float bh(u32 d) { return asf(d & 0xffff0000u); }
DI u32 pk(float a, float b) { f2 t = {a, b}; return __builtin_bit_cast(u32, __builtin_convertvector(t, bf2)); }
DI float rest(float v) { return v - bh(asu(v)); }
DI u32 pkt(float lo, float hi) { return __builtin_amdgcn_perm(asu(hi), asu(lo), 0x07060302u); }
DI f4 mfma(v4u a, v4u b, f4 c) { return __builtin_amdgcn_mfma_f32_16x16x32_bf16(__builtin_bit_cast(s8v, a), __builtin_bit_cast(s8v, b), c, 0, 0, 0); }
DI f16v mfma32(v4u a, v4u b, f16v c) { return __builtin_amdgcn_mfma_f32_32x32x16_bf16(__builtin_bit_cast(s8v, a), __builtin_bit_cast(s8v, b), c, 0, 0, 0); }
DI lptr lp(u32 a) { return (lptr)(uintptr_t)a; }
DI v2u trread(u32 a) { return __builtin_bit_cast(v2u, __builtin_amdgcn_ds_read_tr16_b64_v4i16((LAS s4v*)lp(a))); }
DI v4u trfrag(u32 a, u32 hi_off) { const v2u lo = trread(a), hi = trread(a + hi_off); return v4u{lo.x, lo.y, hi.x, hi.y}; }
DI v4u lr128(u32 a) { return *(LAS v4u*)lp(a); }
DI f4 lrf4(u32 a) { return *(LAS f4*)lp(a); }
DI void lw128(u32 a, v4u v) { *(LAS v4u*)lp(a) = v; }
DI void lwf4(u32 a, f4 v) { *(LAS f4*)lp(a) = v; }
#define DMA_(OP, FL, rs, voff, sb, SI, lb, LI) do { u32 t_; \
  asm volatile("s_add_u32 m0, %1, %2\n\ts_add_u32 %0, %3, %4\n\t" OP " %5, %6, %0 offen" FL " lds" \
               : "=&s"(t_) : "s"(lb), "i"(LI), "s"(sb), "i"(SI), "v"(voff), "s"(rs) : "memory", "scc"); } while (0)
#define DFL ""
#define DMA(rs, voff, sb, SI, lb, LI) DMA_("buffer_load_dwordx4", DFL, rs, voff, sb, SI, lb, LI)
#define SSM_FL ""
#define DMANT(rs, voff, sb, SI, lb, LI) DMA_("buffer_load_dwordx4", " nt", rs, voff, sb, SI, lb, LI)
#define TOUCH(rs, voff, sb, SI) DMA_("buffer_load_dword", "", rs, voff, sb, SI, 0u, L_JUNK)
#define ALD128(d, off, rs, FL) asm volatile("buffer_load_dwordx4 %0, %1, %2, 0 offen" FL : "=v"(d) : "v"(off), "s"(rs) : "memory")
#define ALD128S(d, off, rs, so, FL) asm volatile("buffer_load_dwordx4 %0, %1, %2, %3 offen" FL : "=v"(d) : "v"(off), "s"(rs), "s"(so) : "memory")
#define ALD64(d, off, rs, FL) asm volatile("buffer_load_dwordx2 %0, %1, %2, 0 offen" FL : "=v"(d) : "v"(off), "s"(rs) : "memory")
#define ALD16(d, off, rs, FL) asm volatile("buffer_load_ushort %0, %1, %2, 0 offen" FL : "=v"(d) : "v"(off), "s"(rs) : "memory")
#define WAITV(n) asm volatile("s_waitcnt vmcnt(" #n ")" ::: "memory")
#define PIN(a) asm volatile("" : "+v"(a)::"memory")
#define KEEP(a) asm volatile("" : "+v"(a))
#define KEEPS(a) asm volatile("" : "+s"(a))
#define WAITL() asm volatile("s_waitcnt lgkmcnt(0)" ::: "memory")
#define BAR() __builtin_amdgcn_s_barrier()
#define VBAR(n) asm volatile("s_waitcnt vmcnt(" #n ") lgkmcnt(0)\n\ts_barrier" ::: "memory")
#define LBAR() asm volatile("s_waitcnt lgkmcnt(0)\n\ts_barrier" ::: "memory")
#define SB() __builtin_amdgcn_sched_barrier(0)
DI float wave_sum(float v) {
  v += asf(__builtin_amdgcn_update_dpp(0u, asu(v), 0xB1, 0xF, 0xF, true));
  v += asf(__builtin_amdgcn_update_dpp(0u, asu(v), 0x4E, 0xF, 0xF, true));
  v += asf(__builtin_amdgcn_update_dpp(0u, asu(v), 0x141, 0xF, 0xF, true));
  v += asf(__builtin_amdgcn_update_dpp(0u, asu(v), 0x140, 0xF, 0xF, true));
  const auto a = __builtin_amdgcn_permlane16_swap(asu(v), asu(v), false, false);
  v = asf(a[0]) + asf(a[1]);
  const auto c = __builtin_amdgcn_permlane32_swap(asu(v), asu(v), false, false);
  return asf(c[0]) + asf(c[1]);
}
DI v4u ld_sc1(srd_t rs, u32 off, u32 soff = 0) { return __builtin_amdgcn_raw_buffer_load_b128(rs, off, soff, 16); }
template <int IMM = 0> DI void st_sc1(srd_t rs, u32 off, v4u v, u32 soff = 0) {
  asm volatile("buffer_store_dwordx4 %0, %1, %2, %3 offen offset:%4 sc1\n\ts_nop 1" ::"v"(v), "v"(off), "s"(rs), "s"(soff), "i"(IMM) : "memory");
}
DI void put_flag(srd_t rs, u32 l0, u32 off, u64 tag) { __builtin_amdgcn_raw_buffer_store_b64(__builtin_bit_cast(v2u, tag), rs, l0, off, 16); }
DI u64 get_flag(srd_t rs, u32 off) { return __builtin_bit_cast(u64, __builtin_amdgcn_raw_buffer_load_b64(rs, off, 0, 16)); }
DI void wait_flags(srd_t rs, u32 off, u64 tag, u64 v) {
  if (!all_lanes(v == tag)) {
#pragma unroll 1
    for (u32 spins = 0; spins < SPIN_CAP; ++spins) {
      u64 x;
      asm volatile("buffer_load_dwordx2 %0, %1, %2, 0 offen sc1\n\ts_waitcnt vmcnt(0)" : "=&v"(x) : "v"(off), "s"(rs) : "memory");
      if (all_lanes(x == tag)) break;
      __builtin_amdgcn_s_sleep(1);
    }
  }
  asm volatile("" ::: "memory");
}
DI void arrive_publish(u32 cnt, srd_t rs, u32 l0, u32 off, u64 tag) {
  u32 old;
  asm volatile("s_mov_b64 exec, 1\n\tds_add_rtn_u32 %0, %1, %2\n\ts_mov_b64 exec, -1\n\ts_waitcnt lgkmcnt(0)" : "=&v"(old) : "v"(cnt), "v"(1u) : "memory");
  if (__builtin_amdgcn_readfirstlane(old) == 3u) put_flag(rs, l0, off, tag);
}
#define STAMP_AT(i) do { if ((i) == 0) { prt0 = __builtin_amdgcn_s_memrealtime(); pc0 = __builtin_amdgcn_s_memtime(); __builtin_amdgcn_s_waitcnt(0xC07F); } \
   if ((i) == PROBE && (i) != 0) { SB(); pd = PCYC ? (__builtin_amdgcn_s_memtime() - pc0) / 20 : __builtin_amdgcn_s_memrealtime() - prt0; __builtin_amdgcn_s_waitcnt(0xC07F); SB(); } } while (0)
#define STAMP_F(i)
struct ConvList { int kh[144], e[144]; bool newA[144], newB[144]; };
constexpr int KEEP_MASK[2] = {0x99, 0x66};
constexpr int TS[2][4] = {{0, 3, 4, 7}, {1, 2, 5, 6}};
constexpr ConvList make_list(int role) {
  ConvList L = {};
  int n = 0;
  for (int kl = 0; kl < 4; ++kl) {
    bool seen[16] = {};
    for (int part = 0; part < (kl == 3 ? 2 : 1); ++part) {
      const int mask = kl < 3 ? 0xff : KEEP_MASK[part ? role : role ^ 1];
      for (int kh = 0; kh < 8; ++kh)
        for (int T = 7; T >= kh; --T) {
          if (!((mask >> T) & 1)) continue;
          const int e = T - kh;
          L.kh[n] = kh; L.e[n] = e; L.newA[n] = !seen[kh]; L.newB[n] = !seen[8 + e];
          seen[kh] = seen[8 + e] = true;
          ++n;
        }
    }
  }
  return L;
}
template <class F, int... I> DI void static_for_(F&& f, std::integer_sequence<int, I...>) { (f(std::integral_constant<int, I>{}), ...); }
template <int N, class F> DI void static_for(F&& f) { static_for_(f, std::make_integer_sequence<int, N>{}); }
#define CLA 10
#define NPF 0
#define ZX 1
#define ZW1 1
#define ZW2 1

__global__ void __launch_bounds__(512, 2) fused(Params p) {
  const int tid = __builtin_amdgcn_workitem_id_x(), lane = tid & 63, w = __builtin_amdgcn_readfirstlane(tid >> 6);
  const int b = __builtin_amdgcn_workgroup_id_x(), xcd = b & 7, idx = b >> 3;
  const int g = lane >> 4, r16 = lane & 15, q4 = r16 >> 2, p4 = lane & 3, rr = lane >> 3;
  u64 prt0 = 0, pc0 = 0, pd = 0;
  STAMP_AT(0);
  KEEPS(p.ws); KEEPS(p.out); KEEPS(p.w2); KEEPS(p.b2); KEEPS(p.k);
  const int cp = 32 * xcd + idx;
  u64 tag = *(const CAS u64*)(p.ws + WS_EPOCH);
  u32 ddp = *(const CAS u32*)(p.dd + 2 * cp);
  const bool is_g1 = idx < 28;
  const srd_t rx = mk_srd(p.x, S * D * 2 * ZX), rw1 = mk_srd(p.w1, D * F * 2 * ZW1);
  const u32 w1k = w * 1024;
  v4u xr;
  const int stok = 256 * xcd + 8 * idx + w;
  {
    const srd_t rk = mk_srd(p.k, S * D * 2), rv = mk_srd(p.v, S * D * 2);
    const u32 vo = (u32)(rr * 1024 + (((lane & 7) ^ (rr & 2)) << 4)), so = (u32)((64 * idx + 8 * w) * 1024 + xcd * 128);
    DMANT(rk, vo, so, 0, w1k, L_PK + 1024);
    DMANT(rv, vo, so, 0, w1k, L_PV);
    ALD128(xr, (u32)(stok * 1024 + lane * 16), rx, "");
    if (w == 0) DMANT(rk, vo + so - 8192u, 0u, 0, 0u, L_PK);
    if (w == 1) { const srd_t rsk = mk_srd(p.sk, D * 4 * 2); DMA(rsk, (u32)(lane * 16), (u32)(xcd * 512), 0, 0u, L_PW); }
    if (w == 7) {
      u64 pc; u32 len;
      asm volatile("mk_code_begin:\n\ts_getpc_b64 %0\n\ts_mov_b32 %1, mk_code_end-mk_code_begin" : "=s"(pc), "=s"(len));
      const srd_t rc = mk_srd((const void*)pc, len);
      TOUCH(rc, (u32)(lane * 128), (u32)((idx & 3) * 8192), 0);
    }
  }
  SB();
#define TNOFF (4 - (int)(((uintptr_t)p.w2 >> 7) & 7))
  const int ti = 4 * (xcd >> 1) + (idx & 3), t1 = idx >> 2, tn = (t1 + TNOFF) & 7, tj = 7 * (xcd & 1) + t1;
  const int sidx = idx & 3, hm = 4 * (xcd >> 1) + sidx, cgp = 14 * (xcd & 1) + (hm < 14 ? hm : 0);
  const u32 chsw = (u32)(((lane & 7) ^ (rr & 6)) << 4);
  const u32 xvo = (u32)((128 * ti + 8 * w + rr) * 1024 + (((lane & 7) ^ ((4 * w + g) & 7)) << 4));
  const u32 wvo = (u32)((8 * w + (is_g1 ? g : rr)) * 3584 + (is_g1 ? tj * 256 : cgp * 128)) + (is_g1 ? (u32)((r16 ^ (g << 2)) << 4) : chsw);
  const u32 w2k = w * 2048;
  u32 sox = (u32)(ROT1) * 128, sow = sox * 1792;
#define NEXT_CHUNK() do { sox = (sox + 128) & 1023; sow = sox * 1792; } while (0)
#define PIECE(c, i) do { \
    if ((i) == 0) DMA(rx, xvo, sox, 0, w1k, ((c) % 3) * L_SLOT); \
    if ((i) == 1) DMA(rx, xvo, sox, 65536, w1k, ((c) % 3) * L_SLOT + 8192); \
    if ((i) == 2) DMA(rw1, wvo, sow, 0, w2k, ((c) % 3) * L_SLOT + 16384); \
    if ((i) == 3) { DMA(rw1, wvo, sow, 4 * 3584, w2k, ((c) % 3) * L_SLOT + 17408); NEXT_CHUNK(); } } while (0)
#define HPIECE(c) DMA(rw1, wvo, 0u, (c) * 64 * 3584, w1k, (c) * 8192)
  BAR();
  if (is_g1) { PIECE(0, 0); PIECE(0, 1); PIECE(0, 2); PIECE(0, 3); }
  else { HPIECE(0); HPIECE(1); HPIECE(2); HPIECE(3); }
  SB();

  const srd_t rws = mk_srd(p.ws, WS_BYTES);
  const int tm = 4 * xcd + sidx;
  const int mi = w & 3, ni = w >> 2, hl = lane >> 5, l31 = lane & 31;
  u32 l0 = lane == 0 ? 0u : 0x80000000u;
  u32 a_kv = L_PK + (32 * ni + 8 * g + 4 + q4) * 128 + (((2 * mi + (p4 >> 1)) ^ (q4 & 2)) << 4) + 8 * (p4 & 1);
  u32 a_tap = L_PW + (16 * mi + r16) * 8;
  u32 o_kv = (u32)((r16 >> 1) * 8192 + (r16 & 1) * 64 + (3 - g) * 16);
  u32 s_st = WS_STATS + (u32)stok * 32;
  u32 s_kv = WS_KVT + (u32)((32 * xcd + 8 * mi) * 8192 + (63 - 2 * idx - ni) * 128);
  u32 xa[2][4], wa[2][2];
#pragma unroll
  for (int ks = 0; ks < 4; ++ks) { xa[0][ks] = (32 * mi + l31) * 128 + (((2 * ks + hl) ^ ((l31 >> 1) & 7)) << 4); xa[1][ks] = xa[0][ks] + 2 * L_SLOT; }
#pragma unroll
  for (int n = 0; n < 2; ++n) {
    wa[0][n] = 16384 + (8 * hl + q4) * 256 + (((8 * ni + 4 * n + 2 * (g & 1) + (p4 >> 1)) ^ (q4 << 2)) << 4) + 8 * (p4 & 1);
    wa[1][n] = wa[0][n] + 2 * L_SLOT;
  }
  constexpr u32 TR_HI = 1024;
  const int trow = 16 * hl + 4 * (g & 1) + q4;
  u32 w2vo = (u32)(rr * 1024) + chsw;
  const u32 w4k = w * 4096;
  u32 ta[4];
#pragma unroll
  for (int t = 0; t < 4; ++t)
    ta[t] = L_SLOT + w4k + trow * 128 + (((2 * t + (p4 >> 1)) ^ (trow & 6)) << 4) + 8 * (p4 & 1);
  u32 t16 = tid * 16;
  u32 a_u = L_U + ((tid >> 2) & 1) * U_STRIDE + (tid >> 3) * 64 + (tid & 3) * 16;
  u32 a_hc = L_HC + ni * (4 * HC_STRIDE - 4096) + 32 + t16;
  u32 a_A, a_B;
  { const int rho = (15 - r16) & 3;
    a_A = L_HC + ((mi >> 1) * 4 + rho) * HC_STRIDE + (r16 + 1 + rho + 8 * g) * 2 + (mi & 1) * 256;
    a_B = L_U + (mi >> 1) * U_STRIDE + (240 - 16 * r16 + 8 * g) * 2 + (mi & 1) * 256; }
  KEEP(l0); KEEP(a_kv); KEEP(a_tap); KEEP(o_kv); KEEP(w2vo); KEEP(t16); KEEP(a_u); KEEP(a_hc); KEEP(a_A); KEEP(a_B);
#pragma unroll
  for (int i = 0; i < 4; ++i) { KEEP(xa[0][i]); KEEP(xa[1][i]); KEEP(ta[i]); KEEP(wa[i >> 1][i & 1]); }
  const srd_t rw2 = mk_srd(p.w2, F * D * 2 * ZW2), rssm = mk_srd(p.ssm + cp * 4096, 8192);
  u32 ksh[7];
#define ROT2 xcd
  { int t = ROT2;
#pragma unroll
    for (int s = 0; s < 7; ++s) { t = t >= 7 ? t - 7 : t; ksh[s] = (u32)t * 1024; KEEPS(ksh[s]); ++t; } }
  u32 w2base = (u32)(224 * w * 1024 + tn * 128);
  u32 fo1, fo2;
  {
    const int m = 2 * t1 + ni;
    const u32 fkv = WS_FKV + ((31 - 4 * w - p4) * 8 + xcd) * FLAG_STRIDE;
    const u32 ft = WS_FKV + ((16 * (ti & 1) + 4 * mi + p4) * 8 + (ti >> 1)) * FLAG_STRIDE;
    const u32 fc = WS_FST + (((m >> 2) * 2 + (xcd & 1)) * 4 + (m & 3)) * FLAG_STRIDE;
    const u32 fk = WS_FKV + ((8 * sidx + w) * 8 + xcd) * FLAG_STRIDE;
    fo1 = (lane & 16) ? fk : (lane & (is_g1 ? 8 : 0)) ? fc : (lane & (is_g1 ? 4 : 0)) ? ft : fkv;
    const int tja = ((224 * w) >> 7) + p4, tjb = (224 * w + 223) >> 7;
    fo2 = WS_FH + ((tm >> 1) * 14 + (tja < tjb ? tja : tjb)) * FLAG_STRIDE;
  }
  u32 o_xe = WS_STATS + (u32)((128 * ti + 32 * mi + l31) * 32 + hl * 16), o_we = WS_CSUM + (u32)((128 * tj + 64 * ni + lane) * 16);
  u32 o_out = (u32)((64 * tm + (tid >> 3)) * 1024 + 128 * tn + (tid & 7) * 16);
  KEEP(o_out);
  u32 l01 = lane < 2 ? lane * 16 : 0x80000000u;
  u32 o_h = (u32)(((l31 >> 4) * 56 * 64 + r16 + 16 * hl) * 16);
  u32 s_h = WS_H + (u32)(((8 * ti + 2 * mi) * 56 + 4 * tj + 2 * ni) * 1024);
  u32 hb = WS_H + (u32)(((4 * tm * 56 + 7 * w) * 64 + lane) * 16);
  u32 s_cp = (u32)cp * 8192, s_t0 = (u32)((224 * tn + 8 * sidx) * 1024) + (ksh[0] << 5);
  u32 f_kv = WS_FKV + b * FLAG_STRIDE, f_h = WS_FH + (ti * 14 + tj) * FLAG_STRIDE;
  KEEPS(s_st); KEEPS(s_kv); KEEPS(w2base); KEEPS(s_h); KEEPS(s_cp); KEEPS(s_t0); KEEPS(f_kv); KEEPS(f_h);
  KEEP(fo1); KEEP(fo2); KEEP(o_xe); KEEP(o_we); KEEP(l01); KEEP(o_h); KEEP(hb);
  if (w == 2) lw128(L_U + hl * U_STRIDE + 4096 + l31 * 16, v4u{0, 0, 0, 0});
  if (w == 3) lw128(L_CNT0 + (lane & 1) * 16, v4u{0, 0, 0, 0});
  SB();
  VBAR(4);
  PIN(xr);
  STAMP_AT(1);
  tag += 0x9E3779B97F4A7C15ull;
  if (is_g1) { PIECE(1, 0); PIECE(1, 1); }
  else { HPIECE(4); HPIECE(5); }
  {
    const v2u k0 = trread(a_kv), k1 = trread(a_kv + 512), k2 = trread(a_kv + 1024);
    const v2u v0 = trread(a_kv + (L_PV - L_PK - 512)), v1 = trread(a_kv + (L_PV - L_PK));
    const v2u tw = *(LAS v2u*)lp(a_tap);
    SB();
    float xv8[8];
#pragma unroll
    for (int j = 0; j < 4; ++j) { xv8[2 * j] = bl(xr[j]); xv8[2 * j + 1] = bh(xr[j]); }
    const float mean = wave_sum(((xv8[0] + xv8[1]) + (xv8[2] + xv8[3])) + ((xv8[4] + xv8[5]) + (xv8[6] + xv8[7]))) * (1.0f / D);
    float q0 = 0.f, q1 = 0.f;
#pragma unroll
    for (int j = 0; j < 4; ++j) { const float d0 = xv8[2 * j] - mean, d1 = xv8[2 * j + 1] - mean; q0 += d0 * d0; q1 += d1 * d1; }
    const float var = wave_sum(q0 + q1) * (1.0f / D);
    const float r1 = __builtin_amdgcn_rsqf(var + EPS);
    const float r2 = __builtin_amdgcn_rsqf(var * r1 * r1 + EPS);
    const float kk[12] = {0.f, bh(k0.x), bl(k0.y), bh(k0.y), bl(k1.x), bh(k1.x), bl(k1.y), bh(k1.y), bl(k2.x), bh(k2.x), bl(k2.y), bh(k2.y)};
    const float vv[8] = {bl(v0.x), bh(v0.x), bl(v0.y), bh(v0.y), bl(v1.x), bh(v1.x), bl(v1.y), bh(v1.y)};
    const float w0 = bl(tw.x), w1 = bh(tw.x), w2 = bl(tw.y), w3 = bh(tw.y);
    float o[8];
#pragma unroll
    for (int j = 0; j < 8; ++j) o[j] = (kk[j + 4] * w0 + kk[j + 3] * w1 + kk[j + 2] * w2 + kk[j + 1] * w3) * vv[j];
    st_sc1(rws, o_kv, v4u{pk(o[7], o[6]), pk(o[5], o[4]), pk(o[3], o[2]), pk(o[1], o[0])}, s_kv);
    const float A = r1 * r2, m0 = -mean, m1 = rest(m0), m2 = rest(m1);
    float iA = __builtin_amdgcn_rcpf(A);
    u32 d0 = pkt(m0, m0), d1 = pkt(m1, m1), d2 = pkt(m2, m0), d3 = pkt(iA, rest(iA));
    KEEP(d0); KEEP(d1); KEEP(d2); KEEP(d3);
    const bool l1 = lane & 1;
    st_sc1(rws, l01, v4u{l1 ? asu(A) : d0, l1 ? asu(iA) : d1, l1 ? asu(mean) : d2, l1 ? asu(r1) : d3}, s_st);
  }
  SB();
  if (is_g1) { PIECE(1, 2); PIECE(1, 3); }
  else { HPIECE(6); HPIECE(7); }
  if (w == 6) TOUCH(rw2, (u32)(lane * 128), s_t0, 0);
  SB();
  STAMP_AT(2);
  v4u hd; v2u hp;
  v4u ud;
  v4u w2f[7][4];
  u32 sw2;
#define W2SLOT(s) ((s) == 0 ? 3 : ((s) + 1) % 3)
#define W2_PIECE(s, i) do { if ((i) == 0) sw2 = w2base + (ksh[s] << 5); DMA(rw2, w2vo, sw2, (i) * 8192, w4k, W2SLOT(s) * L_SLOT + (i) * 1024); } while (0)
#define W2_STAGE(s) do { W2_PIECE(s, 0); W2_PIECE(s, 1); W2_PIECE(s, 2); W2_PIECE(s, 3); } while (0)
  v4u xv, b2v; v2u stv;
#define LOAD_SSM() do { ALD128(hd, t16, rssm, SSM_FL); ALD64(hp, t16 - 8, rssm, SSM_FL); ALD128(xv, o_out, rx, ""); \
    const srd_t rb2 = mk_srd(p.b2, D * 2); ALD128(b2v, o_out & 1023u, rb2, ""); } while (0)
#define LOAD_U() do { ALD128S(ud, t16, rws, s_cp + WS_KVT, " sc1"); ALD64(stv, WS_STATS + 24 + (o_out >> 10) * 32, rws, " sc1"); } while (0)
  auto w2_read = [&](int s) {
#pragma unroll
    for (int t = 0; t < 4; ++t) w2f[s][t] = trfrag(ta[t] + W2SLOT(s) * L_SLOT - L_SLOT, TR_HI);
  };
  auto write_hc = [&](int rho) {
    const bool first = (tid & 255) == 0;
    const u32 p0 = first ? 0u : hp.x, p1 = first ? 0u : hp.y;
    const u32 a0 = __builtin_amdgcn_alignbit(p1, p0, 16), a1 = __builtin_amdgcn_alignbit(hd.x, p1, 16), a2 = __builtin_amdgcn_alignbit(hd.y, hd.x, 16),
              a3 = __builtin_amdgcn_alignbit(hd.z, hd.y, 16), a4 = __builtin_amdgcn_alignbit(hd.w, hd.z, 16);
    if (rho == 0) lw128(a_hc, hd);
    if (rho == 1) lw128(a_hc + HC_STRIDE, v4u{a1, a2, a3, a4});
    if (rho == 2) lw128(a_hc + 2 * HC_STRIDE, v4u{p1, hd.x, hd.y, hd.z});
    if (rho == 3) lw128(a_hc + 3 * HC_STRIDE, v4u{a0, a1, a2, a3});
  };

#define CONV_SETUP() do { PIN(ud); PIN(hd); PIN(hp); lw128(a_u, ud); write_hc(0); write_hc(1); write_hc(2); write_hc(3); \
    if (w == 4) lw128(L_HC + rr * HC_STRIDE + (lane & 1) * 16, v4u{0, 0, 0, 0}); } while (0)

  if (is_g1) {
    f16v acc[2];
#pragma unroll
    for (int i = 0; i < 16; ++i) { acc[0][i] = 0.f; acc[1][i] = 0.f; }
    v4u xf[2][2], wf[2][2][2];
#define REQ(dc, i) do { if ((dc) >= 8) W2_PIECE((dc) & 3, i); else PIECE(dc, i); } while (0)
#define STEP(st, dc, d0, nd) do { \
      constexpr int hs_ = (st) & 1, hn_ = hs_ ^ 1, sl_ = (((st) + 1) >> 1) % 3, sb_ = sl_ >> 1; constexpr u32 so_ = (sl_ & 1) * L_SLOT; \
      constexpr bool rd_ = (st) < 15, mm_ = (st) >= 0; \
      _Pragma("unroll") for (int i_ = 0; i_ < 4; ++i_) { \
        if (mm_) acc[i_ & 1] = mfma32(wf[hs_][i_ >> 1][i_ & 1], xf[hs_][i_ >> 1], acc[i_ & 1]); \
        if (rd_) { \
          if (!(i_ & 1)) xf[hn_][i_ >> 1] = lr128(xa[sb_][2 * hn_ + (i_ >> 1)] + so_); \
          wf[hn_][i_ >> 1][i_ & 1] = trfrag(wa[sb_][i_ & 1] + so_ + (32 * hn_ + 16 * (i_ >> 1)) * 256, 1024); } \
        if ((nd) == 4) { if (i_ == 0) REQ(dc, 0); if (i_ == 1) REQ(dc, 1); if (i_ == 2) REQ(dc, 2); if (i_ == 3) REQ(dc, 3); } \
        if ((nd) == 2) { if (i_ == 1) REQ(dc, d0); if (i_ == 3) REQ(dc, (d0) + 1); } \
        SB(); } } while (0)
#define ITER(c) do { STAMP_F(16 + (c)); STEP(2 * (c) + 1, (c) + 3, 0, 2); STEP(2 * (c) + 2, (c) + 3, 2, 2); } while (0)
    u64 pv; v4u xe, we;
    VBAR(6);
    STAMP_AT(15);
    STEP(-1, 0, 0, 0);
    STEP(0, 2, 0, 4);
    VBAR(4);
    if (w == 0) put_flag(rws, l0, f_kv, tag);
    ITER(0); W2_PIECE(0, 0);
    VBAR(5);
    ITER(1); W2_PIECE(0, 1);
    VBAR(6);
    ITER(2); W2_PIECE(0, 2);
    VBAR(6);
    ITER(3); W2_PIECE(0, 3);
    VBAR(6);
    ITER(4);
    ALD64(pv, fo1, rws, " sc1");
    LOAD_SSM();
    VBAR(10);
    STAMP_F(21);
    STEP(11, 9, 0, 2); STEP(12, 9, 2, 2);
    VBAR(8);
    STAMP_F(22);
    PIN(pv);
    wait_flags(rws, fo1, tag, pv);
    ALD128(xe, o_xe, rws, " sc1");
    ALD128(we, o_we, rws, " sc1");
    LOAD_U();
    STEP(13, 10, 0, 2); w2_read(0); STEP(14, 10, 2, 2);
    LBAR();
    STAMP_F(23);
    STEP(15, 0, 0, 0);
    WAITV(6);
    PIN(xe); PIN(we);
    float cA[2];
    {
      v4u xb;
#pragma unroll
      for (int j = 0; j < 4; ++j) { const auto r = __builtin_amdgcn_permlane32_swap(xe[j], xe[j], false, false); xb[j] = r[0]; if (j < 2) cA[j] = asf(r[1]); }
      const v4u z = {0, 0, 0, 0};
      acc[0] = mfma32(hl ? z : we, xb, acc[0]);
      acc[1] = mfma32(hl ? we : z, xb, acc[1]);
    }
    STAMP_AT(3);
    if (ni) { WAITV(5); CONV_SETUP(); SB(); }
    const float k1s = -2.3022082f * cA[0], k3s = -0.10294324f * cA[0] * cA[0] * cA[0];
    const f2 k1 = {k1s, k1s}, k3 = {k3s, k3s}, iA = {cA[1], cA[1]};
#pragma unroll
    for (int qi = 0; qi < 4; ++qi) {
      const int n = qi >> 1, sp = qi & 1;
      if (qi == 0) W2_PIECE(3, 0);
      if (qi == 1) W2_PIECE(3, 1);
      if (qi == 2) W2_PIECE(3, 2);
      if (qi == 3) W2_PIECE(3, 3);
      v4u o;
#pragma unroll
      for (int j = 0; j < 4; ++j) {
        const f2 a = {acc[n][8 * sp + 2 * j], acc[n][8 * sp + 2 * j + 1]};
        const f2 z = a * __builtin_elementwise_fma(a * a, k3, k1);
        const f2 e = {__builtin_amdgcn_exp2f(z.x), __builtin_amdgcn_exp2f(z.y)};
        const f2 d = __builtin_elementwise_fma(e, iA, iA);
        const f2 hv = a * f2{__builtin_amdgcn_rcpf(d.x), __builtin_amdgcn_rcpf(d.y)};
        o[j] = pk(hv.x, hv.y);
      }
      if (qi == 0) st_sc1<0>(rws, o_h, o, s_h);
      if (qi == 1) st_sc1<512>(rws, o_h, o, s_h);
      if (qi == 2) st_sc1<1024>(rws, o_h, o, s_h);
      if (qi == 3) st_sc1<1536>(rws, o_h, o, s_h);
      SB();
    }
    STAMP_AT(4);
    if (!ni) { WAITV(8); CONV_SETUP(); }
  } else {
    u32 b1v;
    { const srd_t rb1 = mk_srd(p.b1 + 64 * cgp, 128); ALD16(b1v, (u32)(16 * mi + 4 * g + p4) * 2, rb1, ""); }
    VBAR(0);
    if (w == 0) put_flag(rws, l0, f_kv, tag);
    STAMP_AT(15);
    if (w < 4) {
      const v4u ones = {0x3f803f80u, 0x3f803f80u, 0x3f803f80u, 0x3f803f80u};
      f4 s0 = {0, 0, 0, 0};
      const u32 a = trow * 128 + (((2 * w + (p4 >> 1)) ^ (trow & 6)) << 4) + 8 * (p4 & 1);
#pragma unroll
      for (int st = 0; st < 16; ++st) s0 = mfma(trfrag(a + st * 4096, TR_HI), ones, s0);
      const float ca = (p4 & 1) ? s0[1] : s0[0], cb = (p4 & 1) ? s0[3] : s0[2], c = (p4 & 2) ? cb : ca;
      const float c1 = rest(c), c2 = rest(c1);
      PIN(b1v);
      st_sc1(rws, r16 < 4 && hm < 14 ? WS_CSUM + (u32)(64 * cgp + 16 * w + 4 * g + p4) * 16 : 0x80000000u, v4u{pkt(c, c1), pkt(c, c1), pkt(c, c2), b1v * 0x10001u});
    }
    VBAR(0);
    if (w == 0) put_flag(rws, l0, WS_FST + (4 * xcd + sidx) * FLAG_STRIDE, tag);
    STAMP_AT(3);
#pragma unroll
    for (int i = 1; i <= NPF; ++i) { u32 kk = ksh[i]; KEEPS(kk); TOUCH(rw2, (u32)(lane * 128), (u32)((224 * w + 8 * sidx) * 1024) + (kk << 5), 0); }
    LOAD_SSM();
    W2_STAGE(0); W2_STAGE(1); W2_STAGE(2); W2_STAGE(3);
    if (b == 255) {
      if (w < 4) wait_flags(rws, WS_FKV + tid * FLAG_STRIDE, tag, ~tag);
      BAR();
      if (w == 0) put_flag(rws, l0, WS_EPOCH, tag);
    }
    wait_flags(rws, fo1, tag, ~tag);
    LOAD_U();
    w2_read(0);
    STAMP_AT(4);
    VBAR(0);
    CONV_SETUP();
  }
  v4u hf[7];
  u32 hbm;
#define MROT (2 * tn)
  const int mrot = MROT;
  auto load_h = [&](int m, int s) { if (s == 0) hbm = hb + (u32)(((m + mrot) & 3) * 57344); hf[s] = ld_sc1(rws, hbm, ksh[s]); };
  if (!ni) {
    WAITV(0);
    w2_read(1); w2_read(2); w2_read(3);
    v4u AD;
    { const u32 dv = (mi & 2) ? ddp >> 16 : ddp & 0xffffu; const int pos = 15 - r16 - 8 * g;
#pragma unroll
      for (int j = 0; j < 4; ++j) AD[j] = (pos >> 1) == j ? dv << ((pos & 1) * 16) : 0u; }
    u32 a_x = L_X + w4k + lane * 16, a_B0 = a_B - (mi & 1) * 256;
    u32 o_y = r16 * 32 + hl * 16 + (g & 1) * ((mi & 1) ? 512u : 1536u), s_y = s_cp + WS_YT + (mi >> 1) * 4096 + (mi & 1) * 512;
    KEEP(a_x); KEEP(a_B0); KEEP(o_y); KEEPS(s_y);
    LBAR();
    STAMP_AT(5);
    f4 acc[4];
    u64 pv2;
    auto conv = [&](auto ROLE) {
      constexpr int role = decltype(ROLE)::value;
      constexpr ConvList CL = make_list(role);
      f4 ac[8];
      v4u fa[4][8], fb[4][8], fd[4];
      auto rd = [&](auto N) {
        constexpr int n = decltype(N)::value, kl = n / 36;
        if constexpr (n < 144) {
          if constexpr (CL.newA[n]) {
            const v2u lo = *(volatile LAS v2u*)lp(a_A + CL.kh[n] * 512 + kl * 64), hi = *(volatile LAS v2u*)lp(a_A + CL.kh[n] * 512 + kl * 64 + 8);
            fa[kl][CL.kh[n]] = v4u{lo.x, lo.y, hi.x, hi.y};
          }
          if constexpr (CL.newB[n]) fb[kl][CL.e[n]] = lr128(a_B + (7 - CL.e[n]) * 512 + kl * 64);
        } else if constexpr (n < 148) fd[n - 144] = lr128(a_B0 + (7 - TS[role][n - 144]) * 512);
      };
      static_for<CLA>(rd);
      static_for<144>([&](auto N) {
        constexpr int n = decltype(N)::value, kl = n / 36, T = CL.kh[n] + CL.e[n];
        ac[T] = mfma(fa[kl][CL.kh[n]], fb[kl][CL.e[n]], n < 8 ? f4{0, 0, 0, 0} : ac[T]);
        rd(std::integral_constant<int, n + CLA>{});
        if constexpr (n >= 9 && n < 9 + 12 * 5 && (n - 9) % 5 == 0) { constexpr int q = (n - 9) / 5; W2_PIECE(4 + q / 4, q % 4); }
        if constexpr (n == 100) ALD64(pv2, fo2, rws, " sc1");
        if constexpr (n >= 128 && n < 136 && (n & 1) == 0) lwf4(a_x + ((n - 128) / 2) * 1024, ac[TS[role ^ 1][(n - 128) / 2]]);
        if constexpr (n == 136) *(volatile LAS u32*)lp(L_CNT0 + 16 + w * 4) = 1u;
        SB();
      });
#pragma unroll
      for (int j = 0; j < 4; ++j) acc[j] = mfma(AD, fd[j], ac[TS[role][j]]);
    };
    if (mi & 1) conv(std::integral_constant<int, 1>{}); else conv(std::integral_constant<int, 0>{});
    STAMP_AT(8);
#pragma unroll 1
    for (u32 spins = 0; spins < SPIN_CAP && *(volatile LAS u32*)lp(L_CNT0 + 16 + (w ^ 1) * 4) == 0u; ++spins) __builtin_amdgcn_s_sleep(1);
    asm volatile("" ::: "memory");
#pragma unroll
    for (int j = 0; j < 4; ++j) acc[j] += lrf4(a_x + ((mi & 1) ? -4096 : 4096) + j * 1024);
#pragma unroll
    for (int j = 0; j < 4; j += 2) {
      const auto r0 = __builtin_amdgcn_permlane16_swap(pk(acc[j][0], acc[j][1]), pk(acc[j + 1][0], acc[j + 1][1]), false, false);
      const auto r1 = __builtin_amdgcn_permlane16_swap(pk(acc[j][2], acc[j][3]), pk(acc[j + 1][2], acc[j + 1][3]), false, false);
      if (j == 0) st_sc1<0>(rws, o_y, v4u{r0[0], r1[0], r0[1], r1[1]}, s_y);
      else st_sc1<2048>(rws, o_y, v4u{r0[0], r1[0], r0[1], r1[1]}, s_y);
    }
    WAITV(2);
    PIN(pv2);
    wait_flags(rws, fo2, tag, pv2);
  } else {
    LBAR();
    STAMP_AT(5);
    WAITV(0);
    if (is_g1) arrive_publish(L_CNT0, rws, l0, f_h, tag);
    w2_read(1); w2_read(2); w2_read(3);
    WAITL();
    W2_STAGE(4); W2_STAGE(5); W2_STAGE(6);
    STAMP_AT(8);
    wait_flags(rws, fo2, tag, ~tag);
  }
  STAMP_AT(9);
#pragma unroll
  for (int s = 0; s < 7; ++s) load_h(0, s);
  SB();
  w2_read(4); w2_read(5); w2_read(6);
  if (!ni) {
    WAITV(7);
    arrive_publish(L_CNT0 + 4, rws, l0, WS_FY + cp * FLAG_STRIDE, tag);
  }
  STAMP_AT(10);
  const int tok = tid >> 3, chunk = tid & 7;

  const u32 fo3 = WS_FY + (32 * tn + 4 * w + p4) * FLAG_STRIDE;
  v4u yv;
  PIN(xv); PIN(b2v); PIN(stv);
  {
    u64 pv3 = 0;
    const u32 pp0 = w4k + (((lane & 48) | ((lane + 4 * hl) & 15)) << 4);
    f4 acc[2][4];
#pragma unroll
    for (int m = 0; m < 4; ++m) {
#pragma unroll
      for (int s = 0; s < 7; ++s) {
#pragma unroll
        for (int t = 0; t < 4; ++t) acc[m & 1][t] = mfma(w2f[s][t], hf[s], s == 0 ? f4{0, 0, 0, 0} : acc[m & 1][t]);
        if (m < 3) load_h(m + 1, s);
        if (m > 0 && s >= 1 && s <= 4) lwf4(((pp0 + (m - 1) * L_SLOT) ^ (((s - 1) & 1) * 128)) + (s - 1) * 1024, acc[(m - 1) & 1][s - 1]);
        if (m == 2 && s == 0) pv3 = get_flag(rws, fo3);
        if (m == 3 && s == 1) {
          STAMP_F(28);
          wait_flags(rws, fo3, tag, pv3);
          STAMP_F(29);
          yv = ld_sc1(rws, WS_YT + (u32)((64 * tn + tok) * 4096 + (64 * tm + 8 * chunk) * 2));
        }
        SB();
      }
      STAMP_F(24 + m);
    }
#pragma unroll 1
    for (u32 spins = 0; spins < SPIN_CAP && *(volatile LAS u32*)lp(L_CNT0 + 4) < 4u; ++spins) __builtin_amdgcn_s_sleep(1);
#pragma unroll
    for (int t = 0; t < 4; ++t) lwf4(((pp0 + 3 * L_SLOT) ^ ((t & 1) * 128)) + t * 1024, acc[1][t]);
  }
  STAMP_AT(11);
  {
    const u32 yp = L_Y + (8 * chunk) * 128 + ((w ^ chunk) << 4) + (tok & 7) * 2;
#pragma unroll
    for (int j = 0; j < 4; ++j) {
      *(LAS u16*)lp(yp + (2 * j) * 128) = (u16)(yv[j] & 0xffff);
      *(LAS u16*)lp(yp + (2 * j + 1) * 128) = (u16)(yv[j] >> 16);
    }
  }
  LBAR();
  STAMP_AT(12);
  {
    const u32 rp = (((w >> 1) - mrot) & 3) * L_SLOT + (chunk >> 1) * 1024 + ((32 * (chunk & 1) + ((tok + 4 * (chunk & 3)) & 15)) << 4);
    const f4 o0 = ((lrf4(rp) + lrf4(rp + 4096)) + (lrf4(rp + 8192) + lrf4(rp + 12288))) +
                  ((lrf4(rp + 16384) + lrf4(rp + 20480)) + (lrf4(rp + 24576) + lrf4(rp + 28672)));
    const f4 o1 = ((lrf4(rp + 256) + lrf4(rp + 4352)) + (lrf4(rp + 8448) + lrf4(rp + 12544))) +
                  ((lrf4(rp + 16640) + lrf4(rp + 20736)) + (lrf4(rp + 24832) + lrf4(rp + 28928)));
    const v4u yy = lr128(L_Y + tok * 128 + ((chunk ^ w) << 4));
    const float r1 = asf(stv.y), mr = -asf(stv.x) * r1;
    float res[8];
#pragma unroll
    for (int j = 0; j < 8; ++j) {
      const float ov = (j < 4 ? o0[j & 3] : o1[j & 3]) + ((j & 1) ? bh(b2v[j >> 1]) : bl(b2v[j >> 1]));
      const float gate = ov * __builtin_amdgcn_rcpf(1.0f + __builtin_amdgcn_exp2f(ov * -1.4426950409f));
      const float xx = (j & 1) ? bh(xv[j >> 1]) : bl(xv[j >> 1]);
      const float yj = (j & 1) ? bh(yy[j >> 1]) : bl(yy[j >> 1]);
      res[j] = xx + ((xx * r1 + mr) * yj) * gate;
    }
    __builtin_amdgcn_raw_buffer_store_b128(v4u{pk(res[0], res[1]), pk(res[2], res[3]), pk(res[4], res[5]), pk(res[6], res[7])}, mk_srd(p.out, S * D * 2), o_out, 0, OUT_AUX);
  }
  WAITV(0);
  STAMP_AT(13);
  while (__builtin_amdgcn_s_memrealtime() - prt0 < 3000ull + pd) __builtin_amdgcn_s_sleep(1);
  asm volatile("mk_code_end:");
}
}

extern "C" void kernel_launch(void* const* d_in, const int* in_sizes, int n_in, void* d_out, int out_size, void* d_ws, size_t ws_size,
                              hipStream_t stream) {
  using namespace mk;
  Params p;
  p.x = (const u16*)d_in[0]; p.k = (const u16*)d_in[1]; p.v = (const u16*)d_in[2]; p.sk = (const u16*)d_in[3];
  p.ssm = (const u16*)d_in[4]; p.dd = (const u16*)d_in[5]; p.w1 = (const u16*)d_in[7]; p.b1 = (const u16*)d_in[8];
  p.w2 = (const u16*)d_in[9]; p.b2 = (const u16*)d_in[10];
  p.out = (u16*)d_out; p.ws = (unsigned char*)d_ws;
  (void)hipFuncSetAttribute((const void*)fused, hipFuncAttributeMaxDynamicSharedMemorySize, LDS_BYTES);
  fused<<<256, 512, LDS_BYTES, stream>>>(p);
}
```

```cpp
#include <hip/hip_runtime.h>
#include <stdint.h>
#define ROT1 xcd
#define NO_T0
#include <type_traits>
#include <utility>

namespace mk {
typedef unsigned short u16;
typedef unsigned u32;
typedef unsigned long long u64;
typedef u32 v4u __attribute__((ext_vector_type(4)));
typedef u32 v2u __attribute__((ext_vector_type(2)));
typedef float f4 __attribute__((ext_vector_type(4)));
typedef float f2 __attribute__((ext_vector_type(2)));
typedef float f16v __attribute__((ext_vector_type(16)));
typedef short s8v __attribute__((ext_vector_type(8)));
typedef short s4v __attribute__((ext_vector_type(4)));
typedef __bf16 bf2 __attribute__((ext_vector_type(2)));
#define GAS __attribute__((address_space(1)))
#define LAS __attribute__((address_space(3)))
#define CAS __attribute__((address_space(4)))
#define DI __device__ __forceinline__
typedef __amdgpu_buffer_rsrc_t srd_t;
typedef LAS unsigned char* lptr;

constexpr int S = 2048, D = 512, F = 1792;
constexpr float EPS = 1e-5f;
constexpr u32 WS_EPOCH = 0, WS_FKV = 4096, WS_FST = 36864, WS_FH = 40960, WS_FY = 69632, WS_STATS = 131072, WS_CSUM = 196608,
              WS_KVT = 1u << 20, WS_YT = 3u << 20, WS_H = 5u << 20, WS_BYTES = 13u << 20;
constexpr u32 FLAG_STRIDE = 128;
constexpr u32 L_SLOT = 32768;
constexpr u32 L_HC = 98304, HC_STRIDE = 4160, L_U = L_HC + 8 * HC_STRIDE, U_STRIDE = 4608;
constexpr u32 L_PK = L_U + 2 * U_STRIDE, L_PV = L_PK + 72 * 128, L_PW = L_PV + 64 * 128;
constexpr u32 L_JUNK = L_PW + 1024, L_CNT0 = L_JUNK + 256;
constexpr u32 L_Y = L_PK, L_X = L_PK;
constexpr u32 LDS_BYTES = 163840;
static_assert(L_JUNK + 256 <= LDS_BYTES, "LDS map");
constexpr u32 SPIN_CAP = 1u << 16;

struct Params {
  const u16 *x, *k, *v, *sk, *ssm, *dd, *w1, *b1, *w2, *b2;
  u16* out;
  unsigned char* ws;
};

DI srd_t mk_srd(const void* p, u32 bytes) { return __builtin_amdgcn_make_buffer_rsrc((void*)p, (short)0, (int)bytes, 0x00020000); }
DI float asf(u32 d) { return __builtin_bit_cast(float, d); }
DI u32 asu(float f) { return __builtin_bit_cast(u32, f); }
DI bool all_lanes(bool c) { return __builtin_amdgcn_ballot_w64(!c) == 0; }
DI float bl(u32 d) { return asf(d << 16); }
DI float bh(u32 d) { return asf(d & 0xffff0000u); }
DI u32 pk(float a, float b) { f2 t = {a, b}; return __builtin_bit_cast(u32, __builtin_convertvector(t, bf2)); }
DI float rest(float v) { return v - bh(asu(v)); }
DI u32 pkt(float lo, float hi) { return __builtin_amdgcn_perm(asu(hi), asu(lo), 0x07060302u); }
DI f4 mfma(v4u a, v4u b, f4 c) { return __builtin_amdgcn_mfma_f32_16x16x32_bf16(__builtin_bit_cast(s8v, a), __builtin_bit_cast(s8v, b), c, 0, 0, 0); }
DI f16v mfma32(v4u a, v4u b, f16v c) { return __builtin_amdgcn_mfma_f32_32x32x16_bf16(__builtin_bit_cast(s8v, a), __builtin_bit_cast(s8v, b), c, 0, 0, 0); }
DI lptr lp(u32 a) { return (lptr)(uintptr_t)a; }
DI v2u trread(u32 a) { return __builtin_bit_cast(v2u, __builtin_amdgcn_ds_read_tr16_b64_v4i16((LAS s4v*)lp(a))); }
DI v4u trfrag(u32 a, u32 hi_off) { const v2u lo = trread(a), hi = trread(a + hi_off); return v4u{lo.x, lo.y, hi.x, hi.y}; }
DI v4u lr128(u32 a) { return *(LAS v4u*)lp(a); }
DI f4 lrf4(u32 a) { return *(LAS f4*)lp(a); }
DI void lw128(u32 a, v4u v) { *(LAS v4u*)lp(a) = v; }
DI void lwf4(u32 a, f4 v) { *(LAS f4*)lp(a) = v; }
#define DMA_(OP, FL, rs, voff, sb, SI, lb, LI) do { u32 t_; \
  asm volatile("s_add_u32 m0, %1, %2\n\ts_add_u32 %0, %3, %4\n\t" OP " %5, %6, %0 offen" FL " lds" \
               : "=&s"(t_) : "s"(lb), "i"(LI), "s"(sb), "i"(SI), "v"(voff), "s"(rs) : "memory", "scc"); } while (0)
#define DFL ""
#define DMA(rs, voff, sb, SI, lb, LI) DMA_("buffer_load_dwordx4", DFL, rs, voff, sb, SI, lb, LI)
#define SSM_FL ""
#define DMANT(rs, voff, sb, SI, lb, LI) DMA_("buffer_load_dwordx4", " nt", rs, voff, sb, SI, lb, LI)
#define M0SET(lb, LI) asm volatile("s_add_u32 m0, %0, %1\n\ts_nop 0" ::"s"(lb), "i"(LI) : "memory", "scc")
#define DML(rs, voff, so, OFF) asm volatile("buffer_load_dwordx4 %0, %1, %2 offen offset:%3 lds" ::"v"(voff), "s"(rs), "s"(so), "i"(OFF) : "memory")
#define TOUCH(rs, voff, sb, SI) DMA_("buffer_load_dword", "", rs, voff, sb, SI, 0u, L_JUNK)
#define ALD128(d, off, rs, FL) asm volatile("buffer_load_dwordx4 %0, %1, %2, 0 offen" FL : "=v"(d) : "v"(off), "s"(rs) : "memory")
#define ALD128S(d, off, rs, so, FL) asm volatile("buffer_load_dwordx4 %0, %1, %2, %3 offen" FL : "=v"(d) : "v"(off), "s"(rs), "s"(so) : "memory")
#define ALD64(d, off, rs, FL) asm volatile("buffer_load_dwordx2 %0, %1, %2, 0 offen" FL : "=v"(d) : "v"(off), "s"(rs) : "memory")
#define ALD16(d, off, rs, FL) asm volatile("buffer_load_ushort %0, %1, %2, 0 offen" FL : "=v"(d) : "v"(off), "s"(rs) : "memory")
#define WAITV(n) asm volatile("s_waitcnt vmcnt(" #n ")" ::: "memory")
#define PIN(a) asm volatile("" : "+v"(a)::"memory")
#define KEEP(a) asm volatile("" : "+v"(a))
#define KEEPP(a) asm volatile("" : "+s"(a))
#define KEEPS(a) do { a = __builtin_amdgcn_readfirstlane(a); asm volatile("" : "+s"(a)); } while (0)
#define WAITL() asm volatile("s_waitcnt lgkmcnt(0)" ::: "memory")
#define BAR() __builtin_amdgcn_s_barrier()
#define VBAR(n) do { __builtin_amdgcn_s_waitcnt(0xC07F); asm volatile("s_waitcnt vmcnt(" #n ") lgkmcnt(0)\n\ts_barrier" ::: "memory"); } while (0)
#define LBAR() do { __builtin_amdgcn_s_waitcnt(0xC07F); asm volatile("s_waitcnt lgkmcnt(0)\n\ts_barrier" ::: "memory"); } while (0)
#define SB() __builtin_amdgcn_sched_barrier(0)
DI float wave_sum(float v) {
  v += asf(__builtin_amdgcn_update_dpp(0u, asu(v), 0xB1, 0xF, 0xF, true));
  v += asf(__builtin_amdgcn_update_dpp(0u, asu(v), 0x4E, 0xF, 0xF, true));
  v += asf(__builtin_amdgcn_update_dpp(0u, asu(v), 0x141, 0xF, 0xF, true));
  v += asf(__builtin_amdgcn_update_dpp(0u, asu(v), 0x140, 0xF, 0xF, true));
  const auto a = __builtin_amdgcn_permlane16_swap(asu(v), asu(v), false, false);
  v = asf(a[0]) + asf(a[1]);
  const auto c = __builtin_amdgcn_permlane32_swap(asu(v), asu(v), false, false);
  return asf(c[0]) + asf(c[1]);
}
DI v4u ld_sc1(srd_t rs, u32 off, u32 soff = 0) { return __builtin_amdgcn_raw_buffer_load_b128(rs, off, soff, 16); }
template <int IMM = 0> DI void st_sc1(srd_t rs, u32 off, v4u v, u32 soff = 0) {
  asm volatile("buffer_store_dwordx4 %0, %1, %2, %3 offen offset:%4 sc1\n\ts_nop 1" ::"v"(v), "v"(off), "s"(rs), "s"(soff), "i"(IMM) : "memory");
}
DI void put_flag(srd_t rs, u32 l0, u32 off, u64 tag) { __builtin_amdgcn_raw_buffer_store_b64(__builtin_bit_cast(v2u, tag), rs, l0, off, 16); }
DI u64 get_flag(srd_t rs, u32 off) { return __builtin_bit_cast(u64, __builtin_amdgcn_raw_buffer_load_b64(rs, off, 0, 16)); }
DI void wait_flags(srd_t rs, u32 off, u64 tag, u64 v) {
  if (!all_lanes(v == tag)) {
#pragma unroll 1
    for (u32 spins = 0; spins < SPIN_CAP; ++spins) {
      u64 x;
      asm volatile("buffer_load_dwordx2 %0, %1, %2, 0 offen sc1\n\ts_waitcnt vmcnt(0)" : "=&v"(x) : "v"(off), "s"(rs) : "memory");
      if (all_lanes(x == tag)) break;
      __builtin_amdgcn_s_sleep(1);
    }
  }
  asm volatile("" ::: "memory");
}
DI void arrive_publish(u32 cnt, srd_t rs, u32 l0, u32 off, u64 tag) {
  u32 old;
  asm volatile("s_mov_b64 exec, 1\n\tds_add_rtn_u32 %0, %1, %2\n\ts_mov_b64 exec, -1\n\ts_waitcnt lgkmcnt(0)" : "=&v"(old) : "v"(cnt), "v"(1u) : "memory");
  if (__builtin_amdgcn_readfirstlane(old) == 3u) put_flag(rs, l0, off, tag);
}
#define STAMP_AT(i)
#define STAMP_F(i)
struct ConvList { int kh[144], e[144]; bool newA[144], newB[144]; };
constexpr int KEEP_MASK[2] = {0x99, 0x66};
constexpr int TS[2][4] = {{0, 3, 4, 7}, {1, 2, 5, 6}};
constexpr ConvList make_list(int role) {
  ConvList L = {};
  int n = 0;
  for (int kl = 0; kl < 4; ++kl) {
    bool seen[16] = {};
    for (int part = 0; part < (kl == 3 ? 2 : 1); ++part) {
      const int mask = kl < 3 ? 0xff : KEEP_MASK[part ? role : role ^ 1];
      for (int kh = 0; kh < 8; ++kh)
        for (int T = 7; T >= kh; --T) {
          if (!((mask >> T) & 1)) continue;
          const int e = T - kh;
          L.kh[n] = kh; L.e[n] = e; L.newA[n] = !seen[kh]; L.newB[n] = !seen[8 + e];
          seen[kh] = seen[8 + e] = true;
          ++n;
        }
    }
  }
  return L;
}
template <class F, int... I> DI void static_for_(F&& f, std::integer_sequence<int, I...>) { (f(std::integral_constant<int, I>{}), ...); }
template <int N, class F> DI void static_for(F&& f) { static_for_(f, std::make_integer_sequence<int, N>{}); }
#define CLA 10
#define NPF 0
#define ZX 1
#define ZW1 1
#define ZW2 1

__global__ void __launch_bounds__(512, 2) fused(Params p) {
  const int tid = __builtin_amdgcn_workitem_id_x(), lane = tid & 63, w = __builtin_amdgcn_readfirstlane(tid >> 6);
  const int b = __builtin_amdgcn_workgroup_id_x(), xcd = b & 7, idx = b >> 3;
  const int g = lane >> 4, r16 = lane & 15, q4 = r16 >> 2, p4 = lane & 3, rr = lane >> 3;
  STAMP_AT(0);
  KEEPP(p.ws); KEEPP(p.out); KEEPP(p.w2); KEEPP(p.b2); KEEPP(p.k);
  const int cp = 32 * xcd + idx;
  u64 tag = *(const CAS u64*)(p.ws + WS_EPOCH);
  u32 ddp = *(const CAS u32*)(p.dd + 2 * cp);
  const bool is_g1 = idx < 28;
  const srd_t rx = mk_srd(p.x, S * D * 2 * ZX), rw1 = mk_srd(p.w1 - 1024, D * F * 2 * ZW1 + 2048);
  const u32 w1k = w * 1024, w4k = w * 4096;
  v4u xr;
  const int stok = 256 * xcd + 8 * idx + w;
  ALD128(xr, (u32)(stok * 1024 + lane * 16), rx, "");
  SB();
  const int sidx = idx & 3, ti = 4 * (xcd >> 1) + sidx, t1 = idx >> 2, tj = 7 * (xcd & 1) + t1;
  const int hm = 4 * (xcd >> 1) + sidx, cgp = 14 * (xcd & 1) + (hm < 14 ? hm : 0);
  const u32 chsw = (u32)(((lane & 7) ^ (rr & 6)) << 4);
  const u32 xvo = (u32)((128 * ti + 16 * w + rr) * 1024 + (((lane & 7) ^ g) << 4)), xvo1 = (xvo ^ 64) + 7168;
  const u32 wvo = (u32)((8 * w + (is_g1 ? g : rr)) * 3584 + (is_g1 ? tj * 256 : cgp * 128)) + (is_g1 ? (u32)((r16 ^ (g << 2)) << 4) : chsw), wvo1 = wvo + 13312;
  u32 sox = (u32)(ROT1) * 128, sow = sox * 1792;
#define NEXT_CHUNK() do { sox = (sox + 128) & 1023; sow = sox * 1792; } while (0)
#define PIECE(c, i) do { \
    if ((i) == 0) { M0SET(w4k, ((c) % 3) * L_SLOT); DML(rx, xvo, sox, 0); } \
    if ((i) == 1) DML(rx, xvo1, sox, 1024); \
    if ((i) == 2) DML(rw1, wvo, sow, 2048); \
    if ((i) == 3) { DML(rw1, wvo1, sow, 3072); NEXT_CHUNK(); } } while (0)
#define HPIECE(c) DMA(rw1, wvo, 0u, (c) * 64 * 3584 + 2048, w1k, (c) * 8192)
  if (w == 7) {
    u64 pc; u32 len;
    asm volatile("mk_code_begin:\n\ts_getpc_b64 %0\n\ts_mov_b32 %1, mk_code_end-mk_code_begin" : "=s"(pc), "=s"(len));
    const srd_t rc = mk_srd((const void*)pc, len);
    TOUCH(rc, (u32)(lane * 128), (u32)(sidx * 8192), 0);
  }
  if (is_g1) { PIECE(0, 0); PIECE(0, 1); PIECE(0, 2); PIECE(0, 3); }
  else { HPIECE(0); HPIECE(1); HPIECE(2); HPIECE(3); }
  SB();

  const srd_t rws = mk_srd(p.ws, WS_BYTES);
  const int tm = 4 * xcd + sidx;
  const int tn = (t1 + 4 - (int)(((uintptr_t)p.w2 >> 7) & 7)) & 7;
  const int mi = w & 3, ni = w >> 2, hl = lane >> 5, l31 = lane & 31;
  u32 l0 = lane == 0 ? 0u : 0x80000000u, l01 = lane < 2 ? lane * 16 : 0x80000000u;
  u32 s_st = WS_STATS + (u32)stok * 32;
  const int m2 = w & 1, nq = w >> 1;
  u32 xa[2][4], wa[2];
#pragma unroll
  for (int ks = 0; ks < 4; ++ks) { xa[0][ks] = (4 * m2 + (g & 1)) * 4096 + r16 * 128 + (((2 * ks + hl) ^ (r16 >> 1)) << 4); xa[1][ks] = xa[0][ks] + 2 * L_SLOT; }
  wa[0] = 2048 + hl * 4096 + q4 * 256 + (((4 * nq + 2 * (g & 1) + (p4 >> 1)) ^ (q4 << 2)) << 4) + 8 * (p4 & 1);
  wa[1] = wa[0] + 2 * L_SLOT;
  u32 kvo = (u32)(rr * 1024 + (((lane & 7) ^ (rr & 2)) << 4)), kso = (u32)((64 * idx + 8 * w) * 1024 + xcd * 128);
  KEEP(l0); KEEP(l01); KEEPS(s_st); KEEP(kvo); KEEPS(kso);
#pragma unroll
  for (int i = 0; i < 4; ++i) { KEEP(xa[0][i]); KEEP(xa[1][i]); }
  KEEP(wa[0]); KEEP(wa[1]);
#define STAGE_K() do { const srd_t rk = mk_srd(p.k, S * D * 2); DMANT(rk, kvo, kso, 0, w1k, L_PK + 1024); \
    if (w == 0) DMANT(rk, kvo + kso - 8192u, 0u, 0, 0u, L_PK); } while (0)
#define STAGE_V() do { const srd_t rv = mk_srd(p.v, S * D * 2); DMANT(rv, kvo, kso, 0, w1k, L_PV); \
    if (w == 1) { const srd_t rsk = mk_srd(p.sk, D * 4 * 2); DMA(rsk, (u32)(lane * 16), (u32)(xcd * 512), 0, 0u, L_PW); } } while (0)
  constexpr u32 TR_HI = 1024;
  const srd_t rw2 = mk_srd(p.w2, F * D * 2 * ZW2), rssm = mk_srd(p.ssm + cp * 4096, 8192);
  u32 a_kv, a_tap, o_kv, s_kv, w2vo[4], w2base, ksh[7], s_t0, ta[4], t16, a_hc, fo1, fo2, o_xe, o_we, o_out, o_h, s_h, hb, s_cp;
  v2u ck[3], cv[2], ctw;
  float cw[4], kk[12], vv[8], co[8];
  const u32 l16 = lane * 16;
  auto fill = [&](auto KK) {
    constexpr int K = decltype(KK)::value;
    if constexpr (K == 200) { s_t0 = (u32)((224 * t1 + 8 * sidx) * 1024) + (ksh[0] << 5); KEEPS(s_t0); }
    if constexpr (K == 201) { a_kv = L_PK + 512 + ni * 4096 + (8 * g + q4) * 128 + (((2 * mi + (p4 >> 1)) ^ (q4 & 2)) << 4) + 8 * (p4 & 1); KEEP(a_kv); }
    if constexpr (K == 202) { a_tap = L_PW + mi * 128 + r16 * 8; KEEP(a_tap);
#pragma unroll
      for (int i = 0; i < 4; ++i) { w2vo[i] = (u32)(rr * 1024 + i * 7168) + chsw; KEEP(w2vo[i]); } }
    if constexpr (K == 203) { o_kv = (u32)((r16 >> 1) * 8192 + (r16 & 1) * 64 + (3 - g) * 16); KEEP(o_kv); }
    if constexpr (K == 204) { s_kv = WS_KVT + (u32)((32 * xcd + 8 * mi) * 8192 + (63 - 2 * idx - ni) * 128); KEEPS(s_kv); }
    if constexpr (K == 205) {
      if (w == 2) lw128(L_U + hl * U_STRIDE + 4096 + l31 * 16, v4u{0, 0, 0, 0});
      if (w == 3) lw128(L_CNT0 + (lane & 1) * 16, v4u{0, 0, 0, 0});
    }
    if constexpr (K == 9) { fo1 = WS_FKV + ((16 * (ti & 1) + 8 * m2) * 8 + (ti >> 1)) * FLAG_STRIDE + (lane & 7) * (8 * FLAG_STRIDE); KEEP(fo1); }
    if constexpr (K == 13) { const u32 fk = WS_FKV + ((8 * sidx + w) * 8 + xcd) * FLAG_STRIDE;
                             fo1 = (lane & (is_g1 ? 16 : 0)) ? fo1 : fk; KEEP(fo1); }
    if constexpr (K == 17) { const int m = 2 * t1 + ni; const u32 fc = WS_FST + (((m >> 2) * 2 + (xcd & 1)) * 4 + (m & 3)) * FLAG_STRIDE;
                             fo1 = (lane & (is_g1 ? 8 : 0)) ? fc : fo1; KEEP(fo1); }
    if constexpr (K == 29) { t16 = tid * 16; o_out = (u32)((64 * tm + 8 * w) * 1024 + 128 * tn) + rr * 1024 + (lane & 7) * 16; KEEP(t16); KEEP(o_out); }
    if constexpr (K == 33) { o_xe = WS_STATS + (u32)((128 * ti + 64 * m2) * 32) + lane * 32; o_we = WS_CSUM + (u32)((128 * tj + 32 * nq) * 16) + l31 * 16; KEEP(o_xe); KEEP(o_we); }
    if constexpr (K == 37) { const int trow = 16 * hl + 4 * (g & 1) + q4; ta[0] = L_SLOT + w4k + trow * 128 + (((p4 >> 1) ^ (trow & 6)) << 4) + 8 * (p4 & 1); KEEP(ta[0]); }
    if constexpr (K == 41) { ta[1] = ta[0] ^ 32; ta[2] = ta[0] ^ 64; ta[3] = ta[0] ^ 96; KEEP(ta[1]); KEEP(ta[2]); KEEP(ta[3]);
                             a_hc = L_HC + ni * (4 * HC_STRIDE - 4096) + 32 + t16; KEEP(a_hc); }
    if constexpr (K == 21) { const u32 fkv = WS_FKV + ((31 - 4 * mi) * 8 + xcd) * FLAG_STRIDE - (p4 + 4 * (lane & 4)) * (8 * FLAG_STRIDE);
                             fo1 = hl ? fkv : fo1; KEEP(fo1); }
    if constexpr (K == 45) { o_h = (u32)((g & 1) * 57344 + r16 * 16 + hl * 256); KEEP(o_h); }
    if constexpr (K == 49) { s_h = WS_H + (u32)(((8 * ti + 4 * m2) * 56 + 4 * tj + nq) * 1024); KEEPS(s_h); }
    if constexpr (K == 100) {
      ctw = *(LAS v2u*)lp(a_tap);
      ck[0] = trread(a_kv); ck[1] = trread(a_kv + 512); ck[2] = trread(a_kv + 1024);
      cv[0] = trread(a_kv + (L_PV - L_PK - 512)); cv[1] = trread(a_kv + (L_PV - L_PK));
    }
    if constexpr (K == 102) {
      cw[0] = bl(ctw.x); cw[1] = bh(ctw.x); cw[2] = bl(ctw.y); cw[3] = bh(ctw.y);
      kk[1] = bh(ck[0].x); kk[2] = bl(ck[0].y); kk[3] = bh(ck[0].y);
#pragma unroll
      for (int i = 0; i < 4; ++i) KEEP(cw[i]);
#pragma unroll
      for (int i = 1; i < 4; ++i) KEEP(kk[i]);
    }
    if constexpr (K >= 103 && K < 111) {
      constexpr int j = K - 103;
      const u32 kd = ck[1 + j / 4][(j / 2) & 1], vd = cv[j / 4][(j / 2) & 1];
      kk[j + 4] = (j & 1) ? bh(kd) : bl(kd); vv[j] = (j & 1) ? bh(vd) : bl(vd);
      asm volatile("v_mul_f32 %0, %1, %5\n\tv_fmac_f32 %0, %2, %6\n\tv_fmac_f32 %0, %3, %7\n\tv_fmac_f32 %0, %4, %8\n\tv_mul_f32 %0, %0, %9"
                   : "=&v"(co[j]) : "v"(kk[j + 1]), "v"(kk[j + 2]), "v"(kk[j + 3]), "v"(kk[j + 4]), "v"(cw[3]), "v"(cw[2]), "v"(cw[1]), "v"(cw[0]), "v"(vv[j]));
    }
    if constexpr (K == 111) st_sc1(rws, o_kv, v4u{pk(co[7], co[6]), pk(co[5], co[4]), pk(co[3], co[2]), pk(co[1], co[0])}, s_kv);
  };
  SB();
  BAR();
  STAGE_K(); STAGE_V();
  WAITV(6);
  PIN(xr);
  STAMP_AT(1);
  {
    float xv8[8];
#pragma unroll
    for (int j = 0; j < 4; ++j) { xv8[2 * j] = bl(xr[j]); xv8[2 * j + 1] = bh(xr[j]); }
    const float mean = wave_sum(((xv8[0] + xv8[1]) + (xv8[2] + xv8[3])) + ((xv8[4] + xv8[5]) + (xv8[6] + xv8[7]))) * (1.0f / D);
    float q0 = 0.f, q1 = 0.f;
#pragma unroll
    for (int j = 0; j < 4; ++j) { const float d0 = xv8[2 * j] - mean, d1 = xv8[2 * j + 1] - mean; q0 += d0 * d0; q1 += d1 * d1; }
    const float var = wave_sum(q0 + q1) * (1.0f / D);
    const float r1 = __builtin_amdgcn_rsqf(var + EPS);
    const float r2 = __builtin_amdgcn_rsqf(var * r1 * r1 + EPS);
    const float A = r1 * r2, m0 = -mean, m1 = rest(m0), m2 = rest(m1);
    float iA = __builtin_amdgcn_rcpf(A);
    u32 d0 = pkt(m0, m0), d1 = pkt(m1, m1), d2 = pkt(m2, m0), d3 = pkt(iA, rest(iA));
    KEEP(d0); KEEP(d1); KEEP(d2); KEEP(d3);
    const bool l1 = lane & 1;
    st_sc1(rws, l01, v4u{l1 ? asu(A) : d0, l1 ? asu(iA) : d1, l1 ? asu(mean) : d2, l1 ? asu(r1) : d3}, s_st);
  }
  tag += 0x9E3779B97F4A7C15ull;
  { int t = xcd;
    KEEPS(t);
#pragma unroll
    for (int s = 0; s < 7; ++s) { t = t >= 7 ? t - 7 : t; ksh[s] = (u32)t * 1024; KEEPS(ksh[s]); ++t; } }
  w2base = (u32)(224 * w * 1024 + tn * 128);
  s_cp = (u32)cp * 8192;
  KEEPS(w2base); KEEPS(s_cp);
  SB();
  if (is_g1) { PIECE(1, 0); PIECE(1, 1); PIECE(1, 2); PIECE(1, 3); }
  else { HPIECE(4); HPIECE(5); HPIECE(6); HPIECE(7); }
  static_for<6>([&](auto I) { fill(std::integral_constant<int, 200 + decltype(I)::value>{}); });
#define MAKE_KV() static_for<12>([&](auto I) { fill(std::integral_constant<int, 100 + decltype(I)::value>{}); })
  SB();
  STAMP_AT(2);
  v4u hd; v2u hp;
  v4u w2f[7][4];
  u32 sw2;
#define W2SLOT(s) ((s) == 0 ? 3 : ((s) + 1) % 3)
#define W2_PIECE(s, i) do { if ((i) == 0) { sw2 = w2base + (ksh[s] << 5); M0SET(w4k, W2SLOT(s) * L_SLOT); } DML(rw2, w2vo[i], sw2, (i) * 1024); } while (0)
#define W2_STAGE(s) do { W2_PIECE(s, 0); W2_PIECE(s, 1); W2_PIECE(s, 2); W2_PIECE(s, 3); } while (0)
  v4u xv, b2v; v2u stv;
#define LOAD_SSM() do { ALD128(hd, t16, rssm, SSM_FL); ALD64(hp, t16 - 8, rssm, SSM_FL); } while (0)
  v4u u0, u1;
#define LOAD_U() do { ALD128S(u0, t16, rws, s_cp + WS_KVT, " sc1"); ALD128S(u1, t16, rws, s_cp + WS_KVT + 4096, " sc1"); } while (0)
#define LOAD_LAST() do { ALD128(xv, o_out, rx, ""); const srd_t rb2 = mk_srd(p.b2, D * 2); ALD128(b2v, o_out & 1023u, rb2, ""); \
    ALD64(stv, WS_STATS + 24 + (o_out >> 10) * 32, rws, " sc1"); } while (0)
  auto w2_read = [&](int s) {
#pragma unroll
    for (int t = 0; t < 4; ++t) w2f[s][t] = trfrag(ta[t] + W2SLOT(s) * L_SLOT - L_SLOT, TR_HI);
  };
  auto write_hc = [&](int rho) {
    const bool first = (tid & 255) == 0;
    const u32 p0 = first ? 0u : hp.x, p1 = first ? 0u : hp.y;
    const u32 a0 = __builtin_amdgcn_alignbit(p1, p0, 16), a1 = __builtin_amdgcn_alignbit(hd.x, p1, 16), a2 = __builtin_amdgcn_alignbit(hd.y, hd.x, 16),
              a3 = __builtin_amdgcn_alignbit(hd.z, hd.y, 16), a4 = __builtin_amdgcn_alignbit(hd.w, hd.z, 16);
    if (rho == 0) lw128(a_hc, hd);
    if (rho == 1) lw128(a_hc + HC_STRIDE, v4u{a1, a2, a3, a4});
    if (rho == 2) lw128(a_hc + 2 * HC_STRIDE, v4u{p1, hd.x, hd.y, hd.z});
    if (rho == 3) lw128(a_hc + 3 * HC_STRIDE, v4u{a0, a1, a2, a3});
  };

#define CONV_SETUP() do { PIN(hd); PIN(hp); write_hc(0); write_hc(1); write_hc(2); write_hc(3); \
    if (w == 4) lw128(L_HC + rr * HC_STRIDE + (lane & 1) * 16, v4u{0, 0, 0, 0}); } while (0)

  if (is_g1) {
    f16v acc[2];
#pragma unroll
    for (int i = 0; i < 16; ++i) { acc[0][i] = 0.f; acc[1][i] = 0.f; }
    v4u xf[2][2][2], wf[2][2];
#define REQ(dc, i) do { if ((dc) >= 8) W2_PIECE((dc) & 3, i); else PIECE(dc, i); } while (0)
#define STEP(st, dc, d0, nd) do { \
      constexpr int hs_ = (st) & 1, hn_ = hs_ ^ 1, sl_ = (((st) + 1) >> 1) % 3, sb_ = sl_ >> 1; constexpr u32 so_ = (sl_ & 1) * L_SLOT; \
      constexpr bool rd_ = (st) < 15, mm_ = (st) >= 0; \
      _Pragma("unroll") for (int i_ = 0; i_ < 4; ++i_) { \
        if (mm_) acc[i_ & 1] = mfma32(wf[hs_][i_ >> 1], xf[hs_][i_ >> 1][i_ & 1], acc[i_ & 1]); \
        if (rd_) { \
          if (!(i_ & 1)) wf[hn_][i_ >> 1] = trfrag(wa[sb_] + so_ + (2 * hn_ + (i_ >> 1)) * 8192, 1024); \
          else { xf[hn_][i_ >> 1][0] = lr128(xa[sb_][2 * hn_ + (i_ >> 1)] + so_); xf[hn_][i_ >> 1][1] = lr128(xa[sb_][2 * hn_ + (i_ >> 1)] + so_ + 8192); } } \
        if ((nd) == 4) { if (i_ == 0) REQ(dc, 0); if (i_ == 1) REQ(dc, 1); if (i_ == 2) REQ(dc, 2); if (i_ == 3) REQ(dc, 3); } \
        if ((nd) == 2) { if (i_ == 0) REQ(dc, d0); if (i_ == 2) REQ(dc, (d0) + 1); } \
        if (mm_) { if (i_ == 0) fill(std::integral_constant<int, 4 * (st)>{}); if (i_ == 1) fill(std::integral_constant<int, 4 * (st) + 1>{}); \
                   if (i_ == 2) fill(std::integral_constant<int, 4 * (st) + 2>{}); if (i_ == 3) fill(std::integral_constant<int, 4 * (st) + 3>{}); } \
        SB(); } } while (0)
#define ITER(c) do { STAMP_F(16 + (c)); STEP(2 * (c) + 1, (c) + 3, 0, 2); STEP(2 * (c) + 2, (c) + 3, 2, 2); } while (0)
    u64 pv; v4u xe, we;
    VBAR(7);
    STAMP_AT(15);
    STEP(-1, 0, 0, 0);
    STEP(0, 2, 0, 4);
    VBAR(9);
    STAMP_F(30);
    MAKE_KV();
    SB();
    VBAR(5);
    ITER(0);
    VBAR(5);
    ITER(1); W2_PIECE(0, 0); W2_PIECE(0, 1);
    VBAR(6);
    if (w == 0) put_flag(rws, l0, WS_FKV + b * FLAG_STRIDE, tag);
    ITER(2); M0SET(w4k, 3 * L_SLOT); W2_PIECE(0, 2); W2_PIECE(0, 3);
    VBAR(8);
    ITER(3);
    VBAR(6);
    ITER(4);
    ALD64(pv, fo1, rws, " sc1");
    LOAD_SSM();
    VBAR(7);
    STAMP_F(21);
    STEP(11, 9, 0, 2); STEP(12, 9, 2, 2);
    VBAR(6);
    STAMP_F(22);
    PIN(pv);
    wait_flags(rws, fo1, tag, pv);
    ALD128(xe, o_xe, rws, " sc1");
    ALD128(we, o_we, rws, " sc1");
    if (!ni) LOAD_U();
    STEP(13, 0, 0, 0); w2_read(0); STEP(14, 10, 0, 2);
    LBAR();
    STAMP_F(23);
    STEP(15, 10, 2, 2);
    if (ni) WAITV(4); else WAITV(6);
    PIN(xe); PIN(we);
    {
      const v4u z = {0, 0, 0, 0};
      acc[0] = mfma32(we, hl ? z : xe, acc[0]);
      acc[1] = mfma32(we, hl ? xe : z, acc[1]);
    }
    STAMP_AT(3);
    if (ni) { CONV_SETUP(); SB(); }
    f2 k1v[2], k3v[2], iAv[2];
    { const auto r = __builtin_amdgcn_permlane32_swap(xe.w, xe.w, false, false);
#pragma unroll
      for (int m = 0; m < 2; ++m) {
        const float iA = bl(r[m]) + bh(r[m]), A = __builtin_amdgcn_rcpf(iA), k1s = -2.3022082f * A, k3s = -0.10294324f * A * A * A;
        k1v[m] = f2{k1s, k1s}; k3v[m] = f2{k3s, k3s}; iAv[m] = f2{iA, iA};
      } }
#pragma unroll
    for (int qi = 0; qi < 4; ++qi) {
      const int n = qi >> 1, sp = qi & 1;
      if (qi == 0) W2_PIECE(3, 0);
      if (qi == 1) W2_PIECE(3, 1);
      if (qi == 2) W2_PIECE(3, 2);
      if (qi == 3) W2_PIECE(3, 3);
      v4u o;
#pragma unroll
      for (int j = 0; j < 4; ++j) {
        const f2 a = {acc[n][8 * sp + 2 * j], acc[n][8 * sp + 2 * j + 1]}, k1 = k1v[n], k3 = k3v[n], iA = iAv[n];
        const f2 z = a * __builtin_elementwise_fma(a * a, k3, k1);
        const f2 e = {__builtin_amdgcn_exp2f(z.x), __builtin_amdgcn_exp2f(z.y)};
        const f2 d = __builtin_elementwise_fma(e, iA, iA);
        const f2 hv = a * f2{__builtin_amdgcn_rcpf(d.x), __builtin_amdgcn_rcpf(d.y)};
        o[j] = pk(hv.x, hv.y);
      }
      if (qi == 0) st_sc1<0>(rws, o_h, o, s_h);
      if (qi == 1) st_sc1<512>(rws, o_h, o, s_h);
      if (qi == 2) st_sc1<0>(rws, o_h, o, s_h + 114688);
      if (qi == 3) st_sc1<512>(rws, o_h, o, s_h + 114688);
      SB();
    }
    STAMP_AT(4);
  } else {
    u32 b1v;
    { const srd_t rb1 = mk_srd(p.b1 + 64 * cgp, 128); ALD16(b1v, (u32)(16 * mi + 4 * g + p4) * 2, rb1, ""); }
    static_for<45>(fill);
    VBAR(0);
    STAMP_AT(15);
    if (w < 4) {
      const v4u ones = {0x3f803f80u, 0x3f803f80u, 0x3f803f80u, 0x3f803f80u};
      f4 s0 = {0, 0, 0, 0};
      const u32 a = (w == 0 ? ta[0] : w == 1 ? ta[1] : w == 2 ? ta[2] : ta[3]) - L_SLOT - w4k;
#pragma unroll
      for (int st = 0; st < 16; ++st) s0 = mfma(trfrag(a + st * 4096, TR_HI), ones, s0);
      const float ca = (p4 & 1) ? s0[1] : s0[0], cb = (p4 & 1) ? s0[3] : s0[2], c = (p4 & 2) ? cb : ca;
      const float c1 = rest(c), c2 = rest(c1);
      PIN(b1v);
      st_sc1(rws, r16 < 4 && hm < 14 ? WS_CSUM + (u32)(64 * cgp + 16 * w + 4 * g + p4) * 16 : 0x80000000u, v4u{pkt(c, c1), pkt(c, c1), pkt(c, c2), b1v * 0x10001u});
    }
    MAKE_KV();
    VBAR(0);
    if (w == 0) { put_flag(rws, l0, WS_FST + (4 * xcd + sidx) * FLAG_STRIDE, tag); put_flag(rws, l0, WS_FKV + b * FLAG_STRIDE, tag); }
    STAMP_AT(3);
#pragma unroll
    for (int i = 1; i <= NPF; ++i) { u32 kk = ksh[i]; KEEPS(kk); TOUCH(rw2, (u32)(lane * 128), (u32)((224 * w + 8 * sidx) * 1024) + (kk << 5), 0); }
    LOAD_SSM();
    W2_STAGE(0); W2_STAGE(1); W2_STAGE(2); W2_STAGE(3);
    if (b == 255) {
      if (w < 4) wait_flags(rws, WS_FKV + tid * FLAG_STRIDE, tag, ~tag);
      BAR();
      if (w == 0) put_flag(rws, l0, WS_EPOCH, tag);
    }
    wait_flags(rws, fo1, tag, ~tag);
    w2_read(0);
    STAMP_AT(4);
    LBAR();
    if (ni) CONV_SETUP(); else LOAD_U();
  }
#define LATE_SETUP() do { const int tja = ((224 * w) >> 7) + p4, tjb = (224 * w + 223) >> 7; \
    fo2 = WS_FH + ((tm >> 1) * 14 + (tja < tjb ? tja : tjb)) * FLAG_STRIDE; hb = WS_H + (u32)((4 * tm * 56 + 7 * w) * 1024) + l16; KEEP(fo2); KEEP(hb); } while (0)
  v4u hf[7];
  u32 hbm;
#define MROT (2 * tn)
  const int mrot = MROT;
  auto load_h = [&](int m, int s) { if (s == 0) hbm = hb + (u32)(((m + mrot) & 3) * 57344); hf[s] = ld_sc1(rws, hbm, ksh[s]); };
  if (!ni) {
    CONV_SETUP();
    const int rho = (15 - r16) & 3;
    u32 a_A = L_HC + ((mi >> 1) * 4 + rho) * HC_STRIDE + (r16 + 1 + rho + 8 * g) * 2 + (mi & 1) * 256;
    u32 a_B = L_U + (mi >> 1) * U_STRIDE + (240 - 16 * r16 + 8 * g) * 2 + (mi & 1) * 256;
    u32 a_u = L_U + ((tid >> 2) & 1) * U_STRIDE + (tid >> 3) * 64 + (tid & 3) * 16;
    KEEP(a_A); KEEP(a_B); KEEP(a_u);
    LATE_SETUP();
    WAITV(0);
    PIN(u0); PIN(u1);
    lw128(a_u, u0); lw128(a_u + 2048, u1);
    w2_read(1); w2_read(2); w2_read(3);
    v4u AD;
    { const u32 dv = (mi & 2) ? ddp >> 16 : ddp & 0xffffu; const int pos = 15 - r16 - 8 * g;
#pragma unroll
      for (int j = 0; j < 4; ++j) AD[j] = (pos >> 1) == j ? dv << ((pos & 1) * 16) : 0u; }
    u32 a_x = L_X + w4k + lane * 16, a_B0 = a_B - (mi & 1) * 256;
    u32 o_y = r16 * 32 + hl * 16 + (g & 1) * ((mi & 1) ? 512u : 1536u), s_y = s_cp + WS_YT + (mi >> 1) * 4096 + (mi & 1) * 512;
    KEEP(a_x); KEEP(a_B0); KEEP(o_y); KEEPS(s_y);
    LBAR();
    STAMP_AT(5);
    f4 acc[4];
    u64 pv2;
    auto conv = [&](auto ROLE) {
      constexpr int role = decltype(ROLE)::value;
      constexpr ConvList CL = make_list(role);
      f4 ac[8];
      v4u fa[4][8], fb[4][8], fd[4];
      auto rd = [&](auto N) {
        constexpr int n = decltype(N)::value, kl = n / 36;
        if constexpr (n < 144) {
          if constexpr (CL.newA[n]) {
            const v2u lo = *(volatile LAS v2u*)lp(a_A + CL.kh[n] * 512 + kl * 64), hi = *(volatile LAS v2u*)lp(a_A + CL.kh[n] * 512 + kl * 64 + 8);
            fa[kl][CL.kh[n]] = v4u{lo.x, lo.y, hi.x, hi.y};
          }
          if constexpr (CL.newB[n]) fb[kl][CL.e[n]] = lr128(a_B + (7 - CL.e[n]) * 512 + kl * 64);
        } else if constexpr (n < 148) fd[n - 144] = lr128(a_B0 + (7 - TS[role][n - 144]) * 512);
      };
      static_for<CLA>(rd);
      static_for<144>([&](auto N) {
        constexpr int n = decltype(N)::value, kl = n / 36, T = CL.kh[n] + CL.e[n];
        ac[T] = mfma(fa[kl][CL.kh[n]], fb[kl][CL.e[n]], n < 8 ? f4{0, 0, 0, 0} : ac[T]);
        rd(std::integral_constant<int, n + CLA>{});
        if constexpr (n >= 9 && n < 9 + 12 * 5 && (n - 9) % 5 == 0) { constexpr int q = (n - 9) / 5; W2_PIECE(4 + q / 4, q % 4); }
        if constexpr (n == 80) LOAD_LAST();
        if constexpr (n == 100) ALD64(pv2, fo2, rws, " sc1");
        if constexpr (n >= 128 && n < 136 && (n & 1) == 0) lwf4(a_x + ((n - 128) / 2) * 1024, ac[TS[role ^ 1][(n - 128) / 2]]);
        if constexpr (n == 136) *(volatile LAS u32*)lp(L_CNT0 + 16 + w * 4) = 1u;
        SB();
      });
#pragma unroll
      for (int j = 0; j < 4; ++j) acc[j] = mfma(AD, fd[j], ac[TS[role][j]]);
    };
    if (mi & 1) conv(std::integral_constant<int, 1>{}); else conv(std::integral_constant<int, 0>{});
    STAMP_AT(8);
#pragma unroll 1
    for (u32 spins = 0; spins < SPIN_CAP && *(volatile LAS u32*)lp(L_CNT0 + 16 + (w ^ 1) * 4) == 0u; ++spins) __builtin_amdgcn_s_sleep(1);
    asm volatile("" ::: "memory");
#pragma unroll
    for (int j = 0; j < 4; ++j) acc[j] += lrf4(a_x + ((mi & 1) ? -4096 : 4096) + j * 1024);
#pragma unroll
    for (int j = 0; j < 4; j += 2) {
      const auto r0 = __builtin_amdgcn_permlane16_swap(pk(acc[j][0], acc[j][1]), pk(acc[j + 1][0], acc[j + 1][1]), false, false);
      const auto r1 = __builtin_amdgcn_permlane16_swap(pk(acc[j][2], acc[j][3]), pk(acc[j + 1][2], acc[j + 1][3]), false, false);
      if (j == 0) st_sc1<0>(rws, o_y, v4u{r0[0], r1[0], r0[1], r1[1]}, s_y);
      else st_sc1<2048>(rws, o_y, v4u{r0[0], r1[0], r0[1], r1[1]}, s_y);
    }
    WAITV(2);
    PIN(pv2);
    wait_flags(rws, fo2, tag, pv2);
  } else {
    LBAR();
    STAMP_AT(5);
    LATE_SETUP();
    WAITV(0);
    if (is_g1) arrive_publish(L_CNT0, rws, l0, WS_FH + (ti * 14 + tj) * FLAG_STRIDE, tag);
    w2_read(1); w2_read(2); w2_read(3);
    WAITL();
    W2_STAGE(4); W2_STAGE(5); W2_STAGE(6);
    LOAD_LAST();
    STAMP_AT(8);
    wait_flags(rws, fo2, tag, ~tag);
  }
  STAMP_AT(9);
#pragma unroll
  for (int s = 0; s < 7; ++s) load_h(0, s);
  SB();
  w2_read(4); w2_read(5); w2_read(6);
  if (!ni) {
    WAITV(7);
    arrive_publish(L_CNT0 + 4, rws, l0, WS_FY + cp * FLAG_STRIDE, tag);
  }
  STAMP_AT(10);
  const int tok = tid >> 3, chunk = tid & 7;

  const u32 fo3 = WS_FY + (32 * tn + 4 * w + p4) * FLAG_STRIDE;
  v4u yv;
  PIN(xv); PIN(b2v); PIN(stv);
  {
    u64 pv3 = 0;
    const u32 pp0 = w4k + (((lane & 48) | ((lane + 4 * hl) & 15)) << 4);
    f4 acc[2][4];
#pragma unroll
    for (int m = 0; m < 4; ++m) {
#pragma unroll
      for (int s = 0; s < 7; ++s) {
#pragma unroll
        for (int t = 0; t < 4; ++t) acc[m & 1][t] = mfma(w2f[s][t], hf[s], s == 0 ? f4{0, 0, 0, 0} : acc[m & 1][t]);
        if (m < 3) load_h(m + 1, s);
        if (m > 0 && s >= 1 && s <= 4) lwf4(((pp0 + (m - 1) * L_SLOT) ^ (((s - 1) & 1) * 128)) + (s - 1) * 1024, acc[(m - 1) & 1][s - 1]);
        if (m == 2 && s == 0) pv3 = get_flag(rws, fo3);
        if (m == 3 && s == 1) {
          STAMP_F(28);
          wait_flags(rws, fo3, tag, pv3);
          STAMP_F(29);
          yv = ld_sc1(rws, WS_YT + (u32)((64 * tn + tok) * 4096 + (64 * tm + 8 * chunk) * 2));
        }
        SB();
      }
      STAMP_F(24 + m);
    }
#pragma unroll 1
    for (u32 spins = 0; spins < SPIN_CAP && *(volatile LAS u32*)lp(L_CNT0 + 4) < 4u; ++spins) __builtin_amdgcn_s_sleep(1);
#pragma unroll
    for (int t = 0; t < 4; ++t) lwf4(((pp0 + 3 * L_SLOT) ^ ((t & 1) * 128)) + t * 1024, acc[1][t]);
  }
  STAMP_AT(11);
  {
    const u32 yp = L_Y + (8 * chunk) * 128 + ((w ^ chunk) << 4) + (tok & 7) * 2;
#pragma unroll
    for (int j = 0; j < 4; ++j) {
      *(LAS u16*)lp(yp + (2 * j) * 128) = (u16)(yv[j] & 0xffff);
      *(LAS u16*)lp(yp + (2 * j + 1) * 128) = (u16)(yv[j] >> 16);
    }
  }
  LBAR();
  STAMP_AT(12);
  {
    const u32 rp = (((w >> 1) - mrot) & 3) * L_SLOT + (chunk >> 1) * 1024 + ((32 * (chunk & 1) + ((tok + 4 * (chunk & 3)) & 15)) << 4);
    const f4 o0 = ((lrf4(rp) + lrf4(rp + 4096)) + (lrf4(rp + 8192) + lrf4(rp + 12288))) +
                  ((lrf4(rp + 16384) + lrf4(rp + 20480)) + (lrf4(rp + 24576) + lrf4(rp + 28672)));
    const f4 o1 = ((lrf4(rp + 256) + lrf4(rp + 4352)) + (lrf4(rp + 8448) + lrf4(rp + 12544))) +
                  ((lrf4(rp + 16640) + lrf4(rp + 20736)) + (lrf4(rp + 24832) + lrf4(rp + 28928)));
    const v4u yy = lr128(L_Y + tok * 128 + ((chunk ^ w) << 4));
    const float r1 = asf(stv.y), mr = -asf(stv.x) * r1;
    float res[8];
#pragma unroll
    for (int j = 0; j < 8; ++j) {
      const float ov = (j < 4 ? o0[j & 3] : o1[j & 3]) + ((j & 1) ? bh(b2v[j >> 1]) : bl(b2v[j >> 1]));
      const float gate = ov * __builtin_amdgcn_rcpf(1.0f + __builtin_amdgcn_exp2f(ov * -1.4426950409f));
      const float xx = (j & 1) ? bh(xv[j >> 1]) : bl(xv[j >> 1]);
      const float yj = (j & 1) ? bh(yy[j >> 1]) : bl(yy[j >> 1]);
      res[j] = xx + ((xx * r1 + mr) * yj) * gate;
    }
#define OUT_AUX 16
    __builtin_amdgcn_raw_buffer_store_b128(v4u{pk(res[0], res[1]), pk(res[2], res[3]), pk(res[4], res[5]), pk(res[6], res[7])}, mk_srd(p.out, S * D * 2), o_out, 0, OUT_AUX);
  }
  asm volatile("mk_code_end:");
}
}

extern "C" void kernel_launch(void* const* d_in, const int* in_sizes, int n_in, void* d_out, int out_size, void* d_ws, size_t ws_size,
                              hipStream_t stream) {
  using namespace mk;
  Params p;
  p.x = (const u16*)d_in[0]; p.k = (const u16*)d_in[1]; p.v = (const u16*)d_in[2]; p.sk = (const u16*)d_in[3];
  p.ssm = (const u16*)d_in[4]; p.dd = (const u16*)d_in[5]; p.w1 = (const u16*)d_in[7]; p.b1 = (const u16*)d_in[8];
  p.w2 = (const u16*)d_in[9]; p.b2 = (const u16*)d_in[10];
  p.out = (u16*)d_out; p.ws = (unsigned char*)d_ws;
  (void)hipFuncSetAttribute((const void*)fused, hipFuncAttributeMaxDynamicSharedMemorySize, LDS_BYTES);
  fused<<<256, 512, LDS_BYTES, stream>>>(p);
}
```

```cpp
#include <hip/hip_runtime.h>
#include <stdint.h>
#define ROT1 ((4*((t1+sidx)&1)+xcd)&7)
#include <type_traits>
#include <utility>

namespace mk {
typedef unsigned short u16;
typedef unsigned u32;
typedef unsigned long long u64;
typedef u32 v4u __attribute__((ext_vector_type(4)));
typedef u32 v2u __attribute__((ext_vector_type(2)));
typedef float f4 __attribute__((ext_vector_type(4)));
typedef float f2 __attribute__((ext_vector_type(2)));
typedef float f16v __attribute__((ext_vector_type(16)));
typedef short s8v __attribute__((ext_vector_type(8)));
typedef short s4v __attribute__((ext_vector_type(4)));
typedef __bf16 bf2 __attribute__((ext_vector_type(2)));
#define GAS __attribute__((address_space(1)))
#define LAS __attribute__((address_space(3)))
#define CAS __attribute__((address_space(4)))
#define DI __device__ __forceinline__
typedef __amdgpu_buffer_rsrc_t srd_t;
typedef LAS unsigned char* lptr;

constexpr int S = 2048, D = 512, F = 1792;
constexpr float EPS = 1e-5f;
constexpr u32 WS_EPOCH = 0, WS_FKV = 4096, WS_FST = 36864, WS_FH = 40960, WS_FY = 69632, WS_STATS = 131072, WS_CSUM = 196608,
              WS_KVT = 1u << 20, WS_YT = 3u << 20, WS_H = 5u << 20, WS_BYTES = 13u << 20;
constexpr u32 FLAG_STRIDE = 128;
constexpr u32 L_SLOT = 32768;
constexpr u32 L_HC = 98304, HC_STRIDE = 4160, L_U = L_HC + 8 * HC_STRIDE, U_STRIDE = 4608;
constexpr u32 L_PK = L_U + 2 * U_STRIDE, L_PV = L_PK + 72 * 128, L_PW = L_PV + 64 * 128;
constexpr u32 L_JUNK = L_PW + 1024, L_CNT0 = L_JUNK + 256;
constexpr u32 L_Y = L_PK, L_X = L_PK;
constexpr u32 LDS_BYTES = 163840;
static_assert(L_JUNK + 256 <= LDS_BYTES, "LDS map");
constexpr u32 SPIN_CAP = 1u << 16;

struct Params {
  const u16 *x, *k, *v, *sk, *ssm, *dd, *w1, *b1, *w2, *b2;
  u16* out;
  unsigned char* ws;
};

DI srd_t mk_srd(const void* p, u32 bytes) { return __builtin_amdgcn_make_buffer_rsrc((void*)p, (short)0, (int)bytes, 0x00020000); }
DI float asf(u32 d) { return __builtin_bit_cast(float, d); }
DI u32 asu(float f) { return __builtin_bit_cast(u32, f); }
DI bool all_lanes(bool c) { return __builtin_amdgcn_ballot_w64(!c) == 0; }
DI float bl(u32 d) { return asf(d << 16); }
DI float bh(u32 d) { return asf(d & 0xffff0000u); }
DI u32 pk(float a, float b) { f2 t = {a, b}; return __builtin_bit_cast(u32, __builtin_convertvector(t, bf2)); }
DI float rest(float v) { return v - bh(asu(v)); }
DI u32 pkt(float lo, float hi) { return __builtin_amdgcn_perm(asu(hi), asu(lo), 0x07060302u); }
DI f4 mfma(v4u a, v4u b, f4 c) { return __builtin_amdgcn_mfma_f32_16x16x32_bf16(__builtin_bit_cast(s8v, a), __builtin_bit_cast(s8v, b), c, 0, 0, 0); }
DI f16v mfma32(v4u a, v4u b, f16v c) { return __builtin_amdgcn_mfma_f32_32x32x16_bf16(__builtin_bit_cast(s8v, a), __builtin_bit_cast(s8v, b), c, 0, 0, 0); }
DI lptr lp(u32 a) { return (lptr)(uintptr_t)a; }
DI v2u trread(u32 a) { return __builtin_bit_cast(v2u, __builtin_amdgcn_ds_read_tr16_b64_v4i16((LAS s4v*)lp(a))); }
DI v4u trfrag(u32 a, u32 hi_off) { const v2u lo = trread(a), hi = trread(a + hi_off); return v4u{lo.x, lo.y, hi.x, hi.y}; }
DI v4u lr128(u32 a) { return *(LAS v4u*)lp(a); }
DI f4 lrf4(u32 a) { return *(LAS f4*)lp(a); }
DI void lw128(u32 a, v4u v) { *(LAS v4u*)lp(a) = v; }
DI void lwf4(u32 a, f4 v) { *(LAS f4*)lp(a) = v; }
#define DMA_(OP, FL, rs, voff, sb, SI, lb, LI) do { u32 t_; \
  asm volatile("s_add_u32 m0, %1, %2\n\ts_add_u32 %0, %3, %4\n\t" OP " %5, %6, %0 offen" FL " lds" \
               : "=&s"(t_) : "s"(lb), "i"(LI), "s"(sb), "i"(SI), "v"(voff), "s"(rs) : "memory", "scc"); } while (0)
#define DFL ""
#define DMA(rs, voff, sb, SI, lb, LI) DMA_("buffer_load_dwordx4", DFL, rs, voff, sb, SI, lb, LI)
#define SSM_FL ""
#define DMANT(rs, voff, sb, SI, lb, LI) DMA_("buffer_load_dwordx4", " nt", rs, voff, sb, SI, lb, LI)
#define M0SET(lb, LI) asm volatile("s_add_u32 m0, %0, %1\n\ts_nop 0" ::"s"(lb), "i"(LI) : "memory", "scc")
#define DML(rs, voff, so, OFF) asm volatile("buffer_load_dwordx4 %0, %1, %2 offen offset:%3 lds" ::"v"(voff), "s"(rs), "s"(so), "i"(OFF) : "memory")
#define TOUCH(rs, voff, sb, SI) DMA_("buffer_load_dword", "", rs, voff, sb, SI, 0u, L_JUNK)
#define ALD128(d, off, rs, FL) asm volatile("buffer_load_dwordx4 %0, %1, %2, 0 offen" FL : "=v"(d) : "v"(off), "s"(rs) : "memory")
#define ALD128S(d, off, rs, so, FL) asm volatile("buffer_load_dwordx4 %0, %1, %2, %3 offen" FL : "=v"(d) : "v"(off), "s"(rs), "s"(so) : "memory")
#define ALD64(d, off, rs, FL) asm volatile("buffer_load_dwordx2 %0, %1, %2, 0 offen" FL : "=v"(d) : "v"(off), "s"(rs) : "memory")
#define ALD16(d, off, rs, FL) asm volatile("buffer_load_ushort %0, %1, %2, 0 offen" FL : "=v"(d) : "v"(off), "s"(rs) : "memory")
#define WAITV(n) asm volatile("s_waitcnt vmcnt(" #n ")" ::: "memory")
#define PIN(a) asm volatile("" : "+v"(a)::"memory")
#define KEEP(a) asm volatile("" : "+v"(a))
#define KEEPP(a) asm volatile("" : "+s"(a))
#define KEEPS(a) do { a = __builtin_amdgcn_readfirstlane(a); asm volatile("" : "+s"(a)); } while (0)
#define WAITL() asm volatile("s_waitcnt lgkmcnt(0)" ::: "memory")
#define BAR() __builtin_amdgcn_s_barrier()
#define VBAR(n) do { __builtin_amdgcn_s_waitcnt(0xC07F); asm volatile("s_waitcnt vmcnt(" #n ") lgkmcnt(0)\n\ts_barrier" ::: "memory"); } while (0)
#define LBAR() do { __builtin_amdgcn_s_waitcnt(0xC07F); asm volatile("s_waitcnt lgkmcnt(0)\n\ts_barrier" ::: "memory"); } while (0)
#define SB() __builtin_amdgcn_sched_barrier(0)
DI float wave_sum(float v) {
  v += asf(__builtin_amdgcn_update_dpp(0u, asu(v), 0xB1, 0xF, 0xF, true));
  v += asf(__builtin_amdgcn_update_dpp(0u, asu(v), 0x4E, 0xF, 0xF, true));
  v += asf(__builtin_amdgcn_update_dpp(0u, asu(v), 0x141, 0xF, 0xF, true));
  v += asf(__builtin_amdgcn_update_dpp(0u, asu(v), 0x140, 0xF, 0xF, true));
  const auto a = __builtin_amdgcn_permlane16_swap(asu(v), asu(v), false, false);
  v = asf(a[0]) + asf(a[1]);
  const auto c = __builtin_amdgcn_permlane32_swap(asu(v), asu(v), false, false);
  return asf(c[0]) + asf(c[1]);
}
DI v4u ld_sc1(srd_t rs, u32 off, u32 soff = 0) { return __builtin_amdgcn_raw_buffer_load_b128(rs, off, soff, 16); }
template <int IMM = 0> DI void st_sc1(srd_t rs, u32 off, v4u v, u32 soff = 0) {
  asm volatile("buffer_store_dwordx4 %0, %1, %2, %3 offen offset:%4 sc1\n\ts_nop 1" ::"v"(v), "v"(off), "s"(rs), "s"(soff), "i"(IMM) : "memory");
}
DI void put_flag(srd_t rs, u32 l0, u32 off, u64 tag) { __builtin_amdgcn_raw_buffer_store_b64(__builtin_bit_cast(v2u, tag), rs, l0, off, 16); }
DI u64 get_flag(srd_t rs, u32 off) { return __builtin_bit_cast(u64, __builtin_amdgcn_raw_buffer_load_b64(rs, off, 0, 16)); }
DI void wait_flags(srd_t rs, u32 off, u64 tag, u64 v) {
  if (!all_lanes(v == tag)) {
#pragma unroll 1
    for (u32 spins = 0; spins < SPIN_CAP; ++spins) {
      u64 x;
      asm volatile("buffer_load_dwordx2 %0, %1, %2, 0 offen sc1\n\ts_waitcnt vmcnt(0)" : "=&v"(x) : "v"(off), "s"(rs) : "memory");
      if (all_lanes(x == tag)) break;
      __builtin_amdgcn_s_sleep(1);
    }
  }
  asm volatile("" ::: "memory");
}
DI void arrive_publish(u32 cnt, srd_t rs, u32 l0, u32 off, u64 tag) {
  u32 old;
  asm volatile("s_mov_b64 exec, 1\n\tds_add_rtn_u32 %0, %1, %2\n\ts_mov_b64 exec, -1\n\ts_waitcnt lgkmcnt(0)" : "=&v"(old) : "v"(cnt), "v"(1u) : "memory");
  if (__builtin_amdgcn_readfirstlane(old) == 3u) put_flag(rs, l0, off, tag);
}
#define STAMP_AT(i)
#define STAMP_F(i)
struct ConvList { int kh[144], e[144]; bool newA[144], newB[144]; };
constexpr int KEEP_MASK[2] = {0x99, 0x66};
constexpr int TS[2][4] = {{0, 3, 4, 7}, {1, 2, 5, 6}};
constexpr ConvList make_list(int role) {
  ConvList L = {};
  int n = 0;
  for (int kl = 0; kl < 4; ++kl) {
    bool seen[16] = {};
    for (int part = 0; part < (kl == 3 ? 2 : 1); ++part) {
      const int mask = kl < 3 ? 0xff : KEEP_MASK[part ? role : role ^ 1];
      for (int kh = 0; kh < 8; ++kh)
        for (int T = 7; T >= kh; --T) {
          if (!((mask >> T) & 1)) continue;
          const int e = T - kh;
          L.kh[n] = kh; L.e[n] = e; L.newA[n] = !seen[kh]; L.newB[n] = !seen[8 + e];
          seen[kh] = seen[8 + e] = true;
          ++n;
        }
    }
  }
  return L;
}
template <class F, int... I> DI void static_for_(F&& f, std::integer_sequence<int, I...>) { (f(std::integral_constant<int, I>{}), ...); }
template <int N, class F> DI void static_for(F&& f) { static_for_(f, std::make_integer_sequence<int, N>{}); }
#define CLA 10
#define NPF 0
#define ZX 1
#define ZW1 1
#define ZW2 1

__global__ void __launch_bounds__(512, 2) fused(Params p) {
  const int tid = __builtin_amdgcn_workitem_id_x(), lane = tid & 63, w = __builtin_amdgcn_readfirstlane(tid >> 6);
  const int b = __builtin_amdgcn_workgroup_id_x(), xcd = b & 7, idx = b >> 3;
  const int g = lane >> 4, r16 = lane & 15, q4 = r16 >> 2, p4 = lane & 3, rr = lane >> 3;
  STAMP_AT(0);
  if (w == 0) {
    u64 pc; u32 len;
    asm volatile("mk_code_begin:\n\ts_getpc_b64 %0\n\ts_mov_b32 %1, mk_code_end-mk_code_begin\n\ts_max_i32 %1, %1, mk_code_end2-mk_code_begin" : "=s"(pc), "=s"(len) :: "scc");
    const srd_t rc = mk_srd((const void*)pc, len);
    TOUCH(rc, (u32)(lane * 128), (u32)((idx & 3) * 8192), 0);
  }
  SB();
  KEEPP(p.ws); KEEPP(p.out); KEEPP(p.w2); KEEPP(p.b2); KEEPP(p.k);
  const int cp = 32 * xcd + idx;
  u64 tag = *(const CAS u64*)(p.ws + WS_EPOCH);
  u32 ddp = *(const CAS u32*)(p.dd + 2 * cp);
  const bool is_g1 = idx < 28;
  const srd_t rx = mk_srd(p.x, S * D * 2 * ZX), rw1 = mk_srd(p.w1 - 1024, D * F * 2 * ZW1 + 2048);
  const u32 w1k = w * 1024, w4k = w * 4096;
  v4u xr;
  const int stok = 256 * xcd + 8 * idx + w;
  ALD128(xr, (u32)(stok * 1024 + lane * 16), rx, "");
  SB();
  const int sidx = idx & 3, ti = 4 * (xcd >> 1) + sidx, t1 = idx >> 2, tj = 7 * (xcd & 1) + t1;
  const int hm = 4 * (xcd >> 1) + sidx, cgp = 14 * (xcd & 1) + (hm < 14 ? hm : 0);
  const u32 chsw = (u32)(((lane & 7) ^ (rr & 6)) << 4);
  const u32 xvo = (u32)((128 * ti + 16 * w + rr) * 1024 + (((lane & 7) ^ g) << 4)), xvo1 = (xvo ^ 64) + 7168;
  const u32 wvo = (u32)((8 * w + (is_g1 ? g : rr)) * 3584 + (is_g1 ? tj * 256 : cgp * 128)) + (is_g1 ? (u32)((r16 ^ (g << 2)) << 4) : chsw), wvo1 = wvo + 13312;
  u32 sox = (u32)(ROT1) * 128, sow = sox * 1792;
#define NEXT_CHUNK() do { sox = (sox + 128) & 1023; sow = sox * 1792; } while (0)
#define PIECE(c, i) do { \
    if ((i) == 0) { M0SET(w4k, ((c) % 3) * L_SLOT); DML(rx, xvo, sox, 0); } \
    if ((i) == 1) DML(rx, xvo1, sox, 1024); \
    if ((i) == 2) DML(rw1, wvo, sow, 2048); \
    if ((i) == 3) { DML(rw1, wvo1, sow, 3072); NEXT_CHUNK(); } } while (0)
#define HPIECE(c) DMA(rw1, wvo, 0u, (c) * 64 * 3584 + 2048, w1k, (c) * 8192)
  if (is_g1) { PIECE(0, 0); PIECE(0, 1); PIECE(0, 2); PIECE(0, 3); }
  else { HPIECE(0); HPIECE(1); HPIECE(2); HPIECE(3); }
  SB();

  const srd_t rws = mk_srd(p.ws, WS_BYTES);
  const int tm = 4 * xcd + sidx;
  const int tn = (t1 + 4 - (int)(((uintptr_t)p.w2 >> 7) & 7)) & 7;
  const int mi = w & 3, ni = w >> 2, hl = lane >> 5, l31 = lane & 31;
  u32 l0 = lane == 0 ? 0u : 0x80000000u, l01 = lane < 2 ? lane * 16 : 0x80000000u;
  u32 s_st = WS_STATS + (u32)stok * 32;
  const int m2 = w & 1, nq = w >> 1;
  u32 xa[2][4], wa[2];
#pragma unroll
  for (int ks = 0; ks < 4; ++ks) { xa[0][ks] = (4 * m2 + (g & 1)) * 4096 + r16 * 128 + (((2 * ks + hl) ^ (r16 >> 1)) << 4); xa[1][ks] = xa[0][ks] + 2 * L_SLOT; }
  wa[0] = 2048 + hl * 4096 + q4 * 256 + (((4 * nq + 2 * (g & 1) + (p4 >> 1)) ^ (q4 << 2)) << 4) + 8 * (p4 & 1);
  wa[1] = wa[0] + 2 * L_SLOT;
  u32 kvo = (u32)(rr * 1024 + (((lane & 7) ^ (rr & 2)) << 4)), kso = (u32)((64 * idx + 8 * w) * 1024 + xcd * 128);
  KEEP(l0); KEEP(l01); KEEPS(s_st); KEEP(kvo); KEEPS(kso);
#pragma unroll
  for (int i = 0; i < 4; ++i) { KEEP(xa[0][i]); KEEP(xa[1][i]); }
  KEEP(wa[0]); KEEP(wa[1]);
#define STAGE_K() do { const srd_t rk = mk_srd(p.k, S * D * 2); DMANT(rk, kvo, kso, 0, w1k, L_PK + 1024); \
    if (w == 0) DMANT(rk, kvo + kso - 8192u, 0u, 0, 0u, L_PK); } while (0)
#define STAGE_V() do { const srd_t rv = mk_srd(p.v, S * D * 2); DMANT(rv, kvo, kso, 0, w1k, L_PV); \
    if (w == 1) { const srd_t rsk = mk_srd(p.sk, D * 4 * 2); DMA(rsk, (u32)(lane * 16), (u32)(xcd * 512), 0, 0u, L_PW); } } while (0)
  constexpr u32 TR_HI = 1024;
  const srd_t rw2 = mk_srd(p.w2, F * D * 2 * ZW2), rssm = mk_srd(p.ssm + cp * 4096, 8192);
  u32 a_kv, a_tap, o_kv, s_kv, w2vo[4], w2base, ksh[7], s_t0, ta[4], t16, a_hc, fo1, fo2, o_xe, o_we, o_out, o_h, s_h, hb, s_cp;
  v2u ck[3], cv[2], ctw;
  float cw[4], kk[12], vv[8], co[8];
  const u32 l16 = lane * 16;
  auto fill = [&](auto KK) {
    constexpr int K = decltype(KK)::value;
    if constexpr (K == 200) { s_t0 = (u32)((224 * t1 + 8 * sidx) * 1024) + (ksh[0] << 5); KEEPS(s_t0); }
    if constexpr (K == 201) { a_kv = L_PK + 512 + ni * 4096 + (8 * g + q4) * 128 + (((2 * mi + (p4 >> 1)) ^ (q4 & 2)) << 4) + 8 * (p4 & 1); KEEP(a_kv); }
    if constexpr (K == 202) { a_tap = L_PW + mi * 128 + r16 * 8; KEEP(a_tap);
#pragma unroll
      for (int i = 0; i < 4; ++i) { w2vo[i] = (u32)(rr * 1024 + i * 7168) + chsw; KEEP(w2vo[i]); } }
    if constexpr (K == 203) { o_kv = (u32)((r16 >> 1) * 8192 + (r16 & 1) * 64 + (3 - g) * 16); KEEP(o_kv); }
    if constexpr (K == 204) { s_kv = WS_KVT + (u32)((32 * xcd + 8 * mi) * 8192 + (63 - 2 * idx - ni) * 128); KEEPS(s_kv); }
    if constexpr (K == 205) {
      if (w == 2) lw128(L_U + hl * U_STRIDE + 4096 + l31 * 16, v4u{0, 0, 0, 0});
      if (w == 3) lw128(L_CNT0 + (lane & 1) * 16, v4u{0, 0, 0, 0});
    }
    if constexpr (K == 9) { fo1 = WS_FKV + ((16 * (ti & 1) + 8 * m2) * 8 + (ti >> 1)) * FLAG_STRIDE + (lane & 7) * (8 * FLAG_STRIDE); KEEP(fo1); }
    if constexpr (K == 13) { const u32 fk = WS_FKV + ((8 * sidx + w) * 8 + xcd) * FLAG_STRIDE;
                             fo1 = (lane & (is_g1 ? 16 : 0)) ? fo1 : fk; KEEP(fo1); }
    if constexpr (K == 17) { const int m = 2 * t1 + ni; const u32 fc = WS_FST + (((m >> 2) * 2 + (xcd & 1)) * 4 + (m & 3)) * FLAG_STRIDE;
                             fo1 = (lane & (is_g1 ? 8 : 0)) ? fc : fo1; KEEP(fo1); }
    if constexpr (K == 29) { t16 = tid * 16; o_out = (u32)((64 * tm + 8 * w) * 1024 + 128 * tn) + rr * 1024 + (lane & 7) * 16; KEEP(t16); KEEP(o_out); }
    if constexpr (K == 33) { o_xe = WS_STATS + (u32)((128 * ti + 64 * m2) * 32) + lane * 32; o_we = WS_CSUM + (u32)((128 * tj + 32 * nq) * 16) + l31 * 16; KEEP(o_xe); KEEP(o_we); }
    if constexpr (K == 37) { const int trow = 16 * hl + 4 * (g & 1) + q4; ta[0] = L_SLOT + w4k + trow * 128 + (((p4 >> 1) ^ (trow & 6)) << 4) + 8 * (p4 & 1); KEEP(ta[0]); }
    if constexpr (K == 41) { ta[1] = ta[0] ^ 32; ta[2] = ta[0] ^ 64; ta[3] = ta[0] ^ 96; KEEP(ta[1]); KEEP(ta[2]); KEEP(ta[3]);
                             a_hc = L_HC + ni * (4 * HC_STRIDE - 4096) + 32 + t16; KEEP(a_hc); }
    if constexpr (K == 21) { const u32 fkv = WS_FKV + ((31 - 4 * mi) * 8 + xcd) * FLAG_STRIDE - (p4 + 4 * (lane & 4)) * (8 * FLAG_STRIDE);
                             fo1 = hl ? fkv : fo1; KEEP(fo1); }
    if constexpr (K == 45) { o_h = (u32)((g & 1) * 57344 + r16 * 16 + hl * 256); KEEP(o_h); }
    if constexpr (K == 49) { s_h = WS_H + (u32)(((8 * ti + 4 * m2) * 56 + 4 * tj + nq) * 1024); KEEPS(s_h); }
    if constexpr (K == 100) {
      ctw = *(LAS v2u*)lp(a_tap);
      ck[0] = trread(a_kv); ck[1] = trread(a_kv + 512); ck[2] = trread(a_kv + 1024);
      cv[0] = trread(a_kv + (L_PV - L_PK - 512)); cv[1] = trread(a_kv + (L_PV - L_PK));
    }
    if constexpr (K == 102) {
      cw[0] = bl(ctw.x); cw[1] = bh(ctw.x); cw[2] = bl(ctw.y); cw[3] = bh(ctw.y);
      kk[1] = bh(ck[0].x); kk[2] = bl(ck[0].y); kk[3] = bh(ck[0].y);
#pragma unroll
      for (int i = 0; i < 4; ++i) KEEP(cw[i]);
#pragma unroll
      for (int i = 1; i < 4; ++i) KEEP(kk[i]);
    }
    if constexpr (K >= 103 && K < 111) {
      constexpr int j = K - 103;
      const u32 kd = ck[1 + j / 4][(j / 2) & 1], vd = cv[j / 4][(j / 2) & 1];
      kk[j + 4] = (j & 1) ? bh(kd) : bl(kd); vv[j] = (j & 1) ? bh(vd) : bl(vd);
      asm volatile("v_mul_f32 %0, %1, %5\n\tv_fmac_f32 %0, %2, %6\n\tv_fmac_f32 %0, %3, %7\n\tv_fmac_f32 %0, %4, %8\n\tv_mul_f32 %0, %0, %9"
                   : "=&v"(co[j]) : "v"(kk[j + 1]), "v"(kk[j + 2]), "v"(kk[j + 3]), "v"(kk[j + 4]), "v"(cw[3]), "v"(cw[2]), "v"(cw[1]), "v"(cw[0]), "v"(vv[j]));
    }
    if constexpr (K == 111) st_sc1(rws, o_kv, v4u{pk(co[7], co[6]), pk(co[5], co[4]), pk(co[3], co[2]), pk(co[1], co[0])}, s_kv);
  };
  SB();
  BAR();
  STAGE_K(); STAGE_V();
  WAITV(6);
  PIN(xr);
  STAMP_AT(1);
  {
    float xv8[8];
#pragma unroll
    for (int j = 0; j < 4; ++j) { xv8[2 * j] = bl(xr[j]); xv8[2 * j + 1] = bh(xr[j]); }
    const float mean = wave_sum(((xv8[0] + xv8[1]) + (xv8[2] + xv8[3])) + ((xv8[4] + xv8[5]) + (xv8[6] + xv8[7]))) * (1.0f / D);
    float q0 = 0.f, q1 = 0.f;
#pragma unroll
    for (int j = 0; j < 4; ++j) { const float d0 = xv8[2 * j] - mean, d1 = xv8[2 * j + 1] - mean; q0 += d0 * d0; q1 += d1 * d1; }
    const float var = wave_sum(q0 + q1) * (1.0f / D);
    const float r1 = __builtin_amdgcn_rsqf(var + EPS);
    const float r2 = __builtin_amdgcn_rsqf(var * r1 * r1 + EPS);
    const float A = r1 * r2, m0 = -mean, m1 = rest(m0), m2 = rest(m1);
    float iA = __builtin_amdgcn_rcpf(A);
    u32 d0 = pkt(m0, m0), d1 = pkt(m1, m1), d2 = pkt(m2, m0), d3 = pkt(iA, rest(iA));
    KEEP(d0); KEEP(d1); KEEP(d2); KEEP(d3);
    const bool l1 = lane & 1;
    st_sc1(rws, l01, v4u{l1 ? asu(A) : d0, l1 ? asu(iA) : d1, l1 ? asu(mean) : d2, l1 ? asu(r1) : d3}, s_st);
  }
  tag += 0x9E3779B97F4A7C15ull;
  { int t = xcd;
    KEEPS(t);
#pragma unroll
    for (int s = 0; s < 7; ++s) { t = t >= 7 ? t - 7 : t; ksh[s] = (u32)t * 1024; KEEPS(ksh[s]); ++t; } }
  w2base = (u32)(224 * w * 1024 + tn * 128);
  s_cp = (u32)cp * 8192;
  KEEPS(w2base); KEEPS(s_cp);
  SB();
  if (is_g1) { PIECE(1, 0); PIECE(1, 1); PIECE(1, 2); PIECE(1, 3); }
  else { HPIECE(4); HPIECE(5); HPIECE(6); HPIECE(7); }
  static_for<6>([&](auto I) { fill(std::integral_constant<int, 200 + decltype(I)::value>{}); });
  if (w == 6) TOUCH(rw2, (u32)(lane * 128), s_t0, 0);
#define MAKE_KV() static_for<12>([&](auto I) { fill(std::integral_constant<int, 100 + decltype(I)::value>{}); })
  SB();
  STAMP_AT(2);
  v4u hd; v2u hp;
  v4u w2f[7][4];
  u32 sw2;
#define W2SLOT(s) ((s) == 0 ? 3 : ((s) + 1) % 3)
#define W2_PIECE(s, i) do { if ((i) == 0) { sw2 = w2base + (ksh[s] << 5); M0SET(w4k, W2SLOT(s) * L_SLOT); } DML(rw2, w2vo[i], sw2, (i) * 1024); } while (0)
#define W2_STAGE(s) do { W2_PIECE(s, 0); W2_PIECE(s, 1); W2_PIECE(s, 2); W2_PIECE(s, 3); } while (0)
  v4u xv, b2v; v2u stv;
#define LOAD_SSM() do { ALD128(hd, t16, rssm, SSM_FL); ALD64(hp, t16 - 8, rssm, SSM_FL); } while (0)
  v4u u0, u1;
#define LOAD_U() do { ALD128S(u0, t16, rws, s_cp + WS_KVT, " sc1"); ALD128S(u1, t16, rws, s_cp + WS_KVT + 4096, " sc1"); } while (0)
#define LOAD_LAST() do { ALD128(xv, o_out, rx, ""); const srd_t rb2 = mk_srd(p.b2, D * 2); ALD128(b2v, o_out & 1023u, rb2, ""); \
    ALD64(stv, WS_STATS + 24 + (o_out >> 10) * 32, rws, " sc1"); } while (0)
  auto w2_read = [&](int s) {
#pragma unroll
    for (int t = 0; t < 4; ++t) w2f[s][t] = trfrag(ta[t] + W2SLOT(s) * L_SLOT - L_SLOT, TR_HI);
  };
  auto write_hc = [&](int rho) {
    const bool first = (tid & 255) == 0;
    const u32 p0 = first ? 0u : hp.x, p1 = first ? 0u : hp.y;
    const u32 a0 = __builtin_amdgcn_alignbit(p1, p0, 16), a1 = __builtin_amdgcn_alignbit(hd.x, p1, 16), a2 = __builtin_amdgcn_alignbit(hd.y, hd.x, 16),
              a3 = __builtin_amdgcn_alignbit(hd.z, hd.y, 16), a4 = __builtin_amdgcn_alignbit(hd.w, hd.z, 16);
    if (rho == 0) lw128(a_hc, hd);
    if (rho == 1) lw128(a_hc + HC_STRIDE, v4u{a1, a2, a3, a4});
    if (rho == 2) lw128(a_hc + 2 * HC_STRIDE, v4u{p1, hd.x, hd.y, hd.z});
    if (rho == 3) lw128(a_hc + 3 * HC_STRIDE, v4u{a0, a1, a2, a3});
  };

#define CONV_SETUP() do { PIN(hd); PIN(hp); write_hc(0); write_hc(1); write_hc(2); write_hc(3); \
    if (w == 4) lw128(L_HC + rr * HC_STRIDE + (lane & 1) * 16, v4u{0, 0, 0, 0}); } while (0)

  if (__builtin_expect(is_g1, 1)) {
    f16v acc[2];
#pragma unroll
    for (int i = 0; i < 16; ++i) { acc[0][i] = 0.f; acc[1][i] = 0.f; }
    v4u xf[2][2][2], wf[2][2];
#define REQ(dc, i) do { if ((dc) >= 8) W2_PIECE((dc) & 3, i); else PIECE(dc, i); } while (0)
#define STEP(st, dc, d0, nd) do { \
      constexpr int hs_ = (st) & 1, hn_ = hs_ ^ 1, sl_ = (((st) + 1) >> 1) % 3, sb_ = sl_ >> 1; constexpr u32 so_ = (sl_ & 1) * L_SLOT; \
      constexpr bool rd_ = (st) < 15, mm_ = (st) >= 0; \
      _Pragma("unroll") for (int i_ = 0; i_ < 4; ++i_) { \
        if (mm_) acc[i_ & 1] = mfma32(wf[hs_][i_ >> 1], xf[hs_][i_ >> 1][i_ & 1], acc[i_ & 1]); \
        if (rd_) { \
          if (!(i_ & 1)) wf[hn_][i_ >> 1] = trfrag(wa[sb_] + so_ + (2 * hn_ + (i_ >> 1)) * 8192, 1024); \
          else { xf[hn_][i_ >> 1][0] = lr128(xa[sb_][2 * hn_ + (i_ >> 1)] + so_); xf[hn_][i_ >> 1][1] = lr128(xa[sb_][2 * hn_ + (i_ >> 1)] + so_ + 8192); } } \
        if ((nd) == 4) { if (i_ == 0) REQ(dc, 0); if (i_ == 1) REQ(dc, 1); if (i_ == 2) REQ(dc, 2); if (i_ == 3) REQ(dc, 3); } \
        if ((nd) == 2) { if (i_ == 0) REQ(dc, d0); if (i_ == 2) REQ(dc, (d0) + 1); } \
        if (mm_) { if (i_ == 0) fill(std::integral_constant<int, 4 * (st)>{}); if (i_ == 1) fill(std::integral_constant<int, 4 * (st) + 1>{}); \
                   if (i_ == 2) fill(std::integral_constant<int, 4 * (st) + 2>{}); if (i_ == 3) fill(std::integral_constant<int, 4 * (st) + 3>{}); } \
        SB(); } } while (0)
#define ITER(c) do { STAMP_F(16 + (c)); STEP(2 * (c) + 1, (c) + 3, 0, 2); STEP(2 * (c) + 2, (c) + 3, 2, 2); } while (0)
    u64 pv; v4u xe, we;
    VBAR(7);
    STAMP_AT(15);
    STEP(-1, 0, 0, 0);
    STEP(0, 2, 0, 4);
    VBAR(9);
    STAMP_F(30);
    MAKE_KV();
    SB();
    VBAR(5);
    ITER(0);
    VBAR(5);
    ITER(1); W2_PIECE(0, 0); W2_PIECE(0, 1);
    VBAR(6);
    if (w == 0) put_flag(rws, l0, WS_FKV + b * FLAG_STRIDE, tag);
    ITER(2); M0SET(w4k, 3 * L_SLOT); W2_PIECE(0, 2); W2_PIECE(0, 3);
    VBAR(8);
    ITER(3);
    VBAR(6);
    ITER(4);
    ALD64(pv, fo1, rws, " sc1");
    LOAD_SSM();
    VBAR(7);
    STAMP_F(21);
    STEP(11, 9, 0, 2); STEP(12, 9, 2, 2);
    VBAR(6);
    STAMP_F(22);
    PIN(pv);
    wait_flags(rws, fo1, tag, pv);
    ALD128(xe, o_xe, rws, " sc1");
    ALD128(we, o_we, rws, " sc1");
    if (!ni) LOAD_U();
    STEP(13, 0, 0, 0); w2_read(0); STEP(14, 10, 0, 2);
    LBAR();
    STAMP_F(23);
    STEP(15, 10, 2, 2);
    if (ni) WAITV(4); else WAITV(6);
    PIN(xe); PIN(we);
    {
      const v4u z = {0, 0, 0, 0};
      acc[0] = mfma32(we, hl ? z : xe, acc[0]);
      acc[1] = mfma32(we, hl ? xe : z, acc[1]);
    }
    STAMP_AT(3);
    if (ni) { CONV_SETUP(); SB(); }
    f2 k1v[2], k3v[2], iAv[2];
    { const auto r = __builtin_amdgcn_permlane32_swap(xe.w, xe.w, false, false);
#pragma unroll
      for (int m = 0; m < 2; ++m) {
        const float iA = bl(r[m]) + bh(r[m]), A = __builtin_amdgcn_rcpf(iA), k1s = -2.3022082f * A, k3s = -0.10294324f * A * A * A;
        k1v[m] = f2{k1s, k1s}; k3v[m] = f2{k3s, k3s}; iAv[m] = f2{iA, iA};
      } }
#pragma unroll
    for (int qi = 0; qi < 4; ++qi) {
      const int n = qi >> 1, sp = qi & 1;
      if (qi == 0) W2_PIECE(3, 0);
      if (qi == 1) W2_PIECE(3, 1);
      if (qi == 2) W2_PIECE(3, 2);
      if (qi == 3) W2_PIECE(3, 3);
      v4u o;
#pragma unroll
      for (int j = 0; j < 4; ++j) {
        const f2 a = {acc[n][8 * sp + 2 * j], acc[n][8 * sp + 2 * j + 1]}, k1 = k1v[n], k3 = k3v[n], iA = iAv[n];
        const f2 z = a * __builtin_elementwise_fma(a * a, k3, k1);
        const f2 e = {__builtin_amdgcn_exp2f(z.x), __builtin_amdgcn_exp2f(z.y)};
        const f2 d = __builtin_elementwise_fma(e, iA, iA);
        const f2 hv = a * f2{__builtin_amdgcn_rcpf(d.x), __builtin_amdgcn_rcpf(d.y)};
        o[j] = pk(hv.x, hv.y);
      }
      if (qi == 0) st_sc1<0>(rws, o_h, o, s_h);
      if (qi == 1) st_sc1<512>(rws, o_h, o, s_h);
      if (qi == 2) st_sc1<0>(rws, o_h, o, s_h + 114688);
      if (qi == 3) st_sc1<512>(rws, o_h, o, s_h + 114688);
      SB();
    }
    STAMP_AT(4);
  } else {
    u32 b1v;
    { const srd_t rb1 = mk_srd(p.b1 + 64 * cgp, 128); ALD16(b1v, (u32)(16 * mi + 4 * g + p4) * 2, rb1, ""); }
    static_for<45>(fill);
    VBAR(0);
    STAMP_AT(15);
    if (w < 4) {
      const v4u ones = {0x3f803f80u, 0x3f803f80u, 0x3f803f80u, 0x3f803f80u};
      f4 s0 = {0, 0, 0, 0};
      const u32 a = (w == 0 ? ta[0] : w == 1 ? ta[1] : w == 2 ? ta[2] : ta[3]) - L_SLOT - w4k;
#pragma unroll
      for (int st = 0; st < 16; ++st) s0 = mfma(trfrag(a + st * 4096, TR_HI), ones, s0);
      const float ca = (p4 & 1) ? s0[1] : s0[0], cb = (p4 & 1) ? s0[3] : s0[2], c = (p4 & 2) ? cb : ca;
      const float c1 = rest(c), c2 = rest(c1);
      PIN(b1v);
      st_sc1(rws, r16 < 4 && hm < 14 ? WS_CSUM + (u32)(64 * cgp + 16 * w + 4 * g + p4) * 16 : 0x80000000u, v4u{pkt(c, c1), pkt(c, c1), pkt(c, c2), b1v * 0x10001u});
    }
    MAKE_KV();
    VBAR(0);
    if (w == 0) { put_flag(rws, l0, WS_FST + (4 * xcd + sidx) * FLAG_STRIDE, tag); put_flag(rws, l0, WS_FKV + b * FLAG_STRIDE, tag); }
    STAMP_AT(3);
#pragma unroll
    for (int i = 1; i <= NPF; ++i) { u32 kk = ksh[i]; KEEPS(kk); TOUCH(rw2, (u32)(lane * 128), (u32)((224 * w + 8 * sidx) * 1024) + (kk << 5), 0); }
    LOAD_SSM();
    W2_STAGE(0); W2_STAGE(1); W2_STAGE(2); W2_STAGE(3);
    if (b == 255) {
      if (w < 4) wait_flags(rws, WS_FKV + tid * FLAG_STRIDE, tag, ~tag);
      BAR();
      if (w == 0) put_flag(rws, l0, WS_EPOCH, tag);
    }
    wait_flags(rws, fo1, tag, ~tag);
    w2_read(0);
    STAMP_AT(4);
    LBAR();
    asm volatile("mk_code_end2:");
    if (ni) CONV_SETUP(); else LOAD_U();
  }
#define LATE_SETUP() do { const int tja = ((224 * w) >> 7) + p4, tjb = (224 * w + 223) >> 7; \
    fo2 = WS_FH + ((tm >> 1) * 14 + (tja < tjb ? tja : tjb)) * FLAG_STRIDE; hb = WS_H + (u32)((4 * tm * 56 + 7 * w) * 1024) + l16; KEEP(fo2); KEEP(hb); } while (0)
  v4u hf[7];
  u32 hbm;
#define MROT (2 * tn)
  const int mrot = MROT;
  auto load_h = [&](int m, int s) { if (s == 0) hbm = hb + (u32)(((m + mrot) & 3) * 57344); hf[s] = ld_sc1(rws, hbm, ksh[s]); };
  if (!ni) {
    CONV_SETUP();
    const int rho = (15 - r16) & 3;
    u32 a_A = L_HC + ((mi >> 1) * 4 + rho) * HC_STRIDE + (r16 + 1 + rho + 8 * g) * 2 + (mi & 1) * 256;
    u32 a_B = L_U + (mi >> 1) * U_STRIDE + (240 - 16 * r16 + 8 * g) * 2 + (mi & 1) * 256;
    u32 a_u = L_U + ((tid >> 2) & 1) * U_STRIDE + (tid >> 3) * 64 + (tid & 3) * 16;
    KEEP(a_A); KEEP(a_B); KEEP(a_u);
    LATE_SETUP();
    WAITV(0);
    PIN(u0); PIN(u1);
    lw128(a_u, u0); lw128(a_u + 2048, u1);
    w2_read(1); w2_read(2); w2_read(3);
    v4u AD;
    { const u32 dv = (mi & 2) ? ddp >> 16 : ddp & 0xffffu; const int pos = 15 - r16 - 8 * g;
#pragma unroll
      for (int j = 0; j < 4; ++j) AD[j] = (pos >> 1) == j ? dv << ((pos & 1) * 16) : 0u; }
    u32 a_x = L_X + w4k + lane * 16, a_B0 = a_B - (mi & 1) * 256;
    u32 o_y = r16 * 32 + hl * 16 + (g & 1) * ((mi & 1) ? 512u : 1536u), s_y = s_cp + WS_YT + (mi >> 1) * 4096 + (mi & 1) * 512;
    KEEP(a_x); KEEP(a_B0); KEEP(o_y); KEEPS(s_y);
    LBAR();
    STAMP_AT(5);
    f4 acc[4];
    u64 pv2;
    auto conv = [&](auto ROLE) {
      constexpr int role = decltype(ROLE)::value;
      constexpr ConvList CL = make_list(role);
      f4 ac[8];
      v4u fa[4][8], fb[4][8], fd[4];
      auto rd = [&](auto N) {
        constexpr int n = decltype(N)::value, kl = n / 36;
        if constexpr (n < 144) {
          if constexpr (CL.newA[n]) {
            const v2u lo = *(volatile LAS v2u*)lp(a_A + CL.kh[n] * 512 + kl * 64), hi = *(volatile LAS v2u*)lp(a_A + CL.kh[n] * 512 + kl * 64 + 8);
            fa[kl][CL.kh[n]] = v4u{lo.x, lo.y, hi.x, hi.y};
          }
          if constexpr (CL.newB[n]) fb[kl][CL.e[n]] = lr128(a_B + (7 - CL.e[n]) * 512 + kl * 64);
        } else if constexpr (n < 148) fd[n - 144] = lr128(a_B0 + (7 - TS[role][n - 144]) * 512);
      };
      static_for<CLA>(rd);
      static_for<144>([&](auto N) {
        constexpr int n = decltype(N)::value, kl = n / 36, T = CL.kh[n] + CL.e[n];
        ac[T] = mfma(fa[kl][CL.kh[n]], fb[kl][CL.e[n]], n < 8 ? f4{0, 0, 0, 0} : ac[T]);
        rd(std::integral_constant<int, n + CLA>{});
        if constexpr (n >= 9 && n < 9 + 12 * 5 && (n - 9) % 5 == 0) { constexpr int q = (n - 9) / 5; W2_PIECE(4 + q / 4, q % 4); }
        if constexpr (n == 80) LOAD_LAST();
        if constexpr (n == 100) ALD64(pv2, fo2, rws, " sc1");
        if constexpr (n >= 128 && n < 136 && (n & 1) == 0) lwf4(a_x + ((n - 128) / 2) * 1024, ac[TS[role ^ 1][(n - 128) / 2]]);
        if constexpr (n == 136) *(volatile LAS u32*)lp(L_CNT0 + 16 + w * 4) = 1u;
        SB();
      });
#pragma unroll
      for (int j = 0; j < 4; ++j) acc[j] = mfma(AD, fd[j], ac[TS[role][j]]);
    };
    if (mi & 1) conv(std::integral_constant<int, 1>{}); else conv(std::integral_constant<int, 0>{});
    STAMP_AT(8);
#pragma unroll 1
    for (u32 spins = 0; spins < SPIN_CAP && *(volatile LAS u32*)lp(L_CNT0 + 16 + (w ^ 1) * 4) == 0u; ++spins) __builtin_amdgcn_s_sleep(1);
    asm volatile("" ::: "memory");
#pragma unroll
    for (int j = 0; j < 4; ++j) acc[j] += lrf4(a_x + ((mi & 1) ? -4096 : 4096) + j * 1024);
#pragma unroll
    for (int j = 0; j < 4; j += 2) {
      const auto r0 = __builtin_amdgcn_permlane16_swap(pk(acc[j][0], acc[j][1]), pk(acc[j + 1][0], acc[j + 1][1]), false, false);
      const auto r1 = __builtin_amdgcn_permlane16_swap(pk(acc[j][2], acc[j][3]), pk(acc[j + 1][2], acc[j + 1][3]), false, false);
      if (j == 0) st_sc1<0>(rws, o_y, v4u{r0[0], r1[0], r0[1], r1[1]}, s_y);
      else st_sc1<2048>(rws, o_y, v4u{r0[0], r1[0], r0[1], r1[1]}, s_y);
    }
    WAITV(2);
    PIN(pv2);
    wait_flags(rws, fo2, tag, pv2);
  } else {
    LBAR();
    STAMP_AT(5);
    LATE_SETUP();
    WAITV(0);
    if (is_g1) arrive_publish(L_CNT0, rws, l0, WS_FH + (ti * 14 + tj) * FLAG_STRIDE, tag);
    w2_read(1); w2_read(2); w2_read(3);
    WAITL();
    W2_STAGE(4); W2_STAGE(5); W2_STAGE(6);
    LOAD_LAST();
    STAMP_AT(8);
    wait_flags(rws, fo2, tag, ~tag);
  }
  STAMP_AT(9);
#pragma unroll
  for (int s = 0; s < 7; ++s) load_h(0, s);
  SB();
  w2_read(4); w2_read(5); w2_read(6);
  if (!ni) {
    WAITV(7);
    arrive_publish(L_CNT0 + 4, rws, l0, WS_FY + cp * FLAG_STRIDE, tag);
  }
  STAMP_AT(10);
  const int tok = tid >> 3, chunk = tid & 7;

  const u32 fo3 = WS_FY + (32 * tn + 4 * w + p4) * FLAG_STRIDE;
  v4u yv;
  PIN(xv); PIN(b2v); PIN(stv);
  {
    u64 pv3 = 0;
    const u32 pp0 = w4k + (((lane & 48) | ((lane + 4 * hl) & 15)) << 4);
    f4 acc[2][4];
#pragma unroll
    for (int m = 0; m < 4; ++m) {
#pragma unroll
      for (int s = 0; s < 7; ++s) {
#pragma unroll
        for (int t = 0; t < 4; ++t) acc[m & 1][t] = mfma(w2f[s][t], hf[s], s == 0 ? f4{0, 0, 0, 0} : acc[m & 1][t]);
        if (m < 3) load_h(m + 1, s);
        if (m > 0 && s >= 1 && s <= 4) lwf4(((pp0 + (m - 1) * L_SLOT) ^ (((s - 1) & 1) * 128)) + (s - 1) * 1024, acc[(m - 1) & 1][s - 1]);
        if (m == 2 && s == 0) pv3 = get_flag(rws, fo3);
        if (m == 3 && s == 1) {
          STAMP_F(28);
          wait_flags(rws, fo3, tag, pv3);
          STAMP_F(29);
          yv = ld_sc1(rws, WS_YT + (u32)((64 * tn + tok) * 4096 + (64 * tm + 8 * chunk) * 2));
        }
        SB();
      }
      STAMP_F(24 + m);
    }
#pragma unroll 1
    for (u32 spins = 0; spins < SPIN_CAP && *(volatile LAS u32*)lp(L_CNT0 + 4) < 4u; ++spins) __builtin_amdgcn_s_sleep(1);
#pragma unroll
    for (int t = 0; t < 4; ++t) lwf4(((pp0 + 3 * L_SLOT) ^ ((t & 1) * 128)) + t * 1024, acc[1][t]);
  }
  STAMP_AT(11);
  {
    const u32 yp = L_Y + (8 * chunk) * 128 + ((w ^ chunk) << 4) + (tok & 7) * 2;
#pragma unroll
    for (int j = 0; j < 4; ++j) {
      *(LAS u16*)lp(yp + (2 * j) * 128) = (u16)(yv[j] & 0xffff);
      *(LAS u16*)lp(yp + (2 * j + 1) * 128) = (u16)(yv[j] >> 16);
    }
  }
  LBAR();
  STAMP_AT(12);
  {
    const u32 rp = (((w >> 1) - mrot) & 3) * L_SLOT + (chunk >> 1) * 1024 + ((32 * (chunk & 1) + ((tok + 4 * (chunk & 3)) & 15)) << 4);
    const f4 o0 = ((lrf4(rp) + lrf4(rp + 4096)) + (lrf4(rp + 8192) + lrf4(rp + 12288))) +
                  ((lrf4(rp + 16384) + lrf4(rp + 20480)) + (lrf4(rp + 24576) + lrf4(rp + 28672)));
    const f4 o1 = ((lrf4(rp + 256) + lrf4(rp + 4352)) + (lrf4(rp + 8448) + lrf4(rp + 12544))) +
                  ((lrf4(rp + 16640) + lrf4(rp + 20736)) + (lrf4(rp + 24832) + lrf4(rp + 28928)));
    const v4u yy = lr128(L_Y + tok * 128 + ((chunk ^ w) << 4));
    const float r1 = asf(stv.y), mr = -asf(stv.x) * r1;
    float res[8];
#pragma unroll
    for (int j = 0; j < 8; ++j) {
      const float ov = (j < 4 ? o0[j & 3] : o1[j & 3]) + ((j & 1) ? bh(b2v[j >> 1]) : bl(b2v[j >> 1]));
      const float gate = ov * __builtin_amdgcn_rcpf(1.0f + __builtin_amdgcn_exp2f(ov * -1.4426950409f));
      const float xx = (j & 1) ? bh(xv[j >> 1]) : bl(xv[j >> 1]);
      const float yj = (j & 1) ? bh(yy[j >> 1]) : bl(yy[j >> 1]);
      res[j] = xx + ((xx * r1 + mr) * yj) * gate;
    }
#define OUT_AUX 16
    __builtin_amdgcn_raw_buffer_store_b128(v4u{pk(res[0], res[1]), pk(res[2], res[3]), pk(res[4], res[5]), pk(res[6], res[7])}, mk_srd(p.out, S * D * 2), o_out, 0, OUT_AUX);
  }
  asm volatile("mk_code_end:");
}
}

extern "C" void kernel_launch(void* const* d_in, const int* in_sizes, int n_in, void* d_out, int out_size, void* d_ws, size_t ws_size,
                              hipStream_t stream) {
  using namespace mk;
  Params p;
  p.x = (const u16*)d_in[0]; p.k = (const u16*)d_in[1]; p.v = (const u16*)d_in[2]; p.sk = (const u16*)d_in[3];
  p.ssm = (const u16*)d_in[4]; p.dd = (const u16*)d_in[5]; p.w1 = (const u16*)d_in[7]; p.b1 = (const u16*)d_in[8];
  p.w2 = (const u16*)d_in[9]; p.b2 = (const u16*)d_in[10];
  p.out = (u16*)d_out; p.ws = (unsigned char*)d_ws;
  (void)hipFuncSetAttribute((const void*)fused, hipFuncAttributeMaxDynamicSharedMemorySize, LDS_BYTES);
  fused<<<256, 512, LDS_BYTES, stream>>>(p);
}
```

```cpp
#include <hip/hip_runtime.h>
#include <stdint.h>
#include <type_traits>
#include <utility>

namespace mk {
typedef unsigned short u16;
typedef unsigned u32;
typedef unsigned long long u64;
typedef u32 v4u __attribute__((ext_vector_type(4)));
typedef u32 v2u __attribute__((ext_vector_type(2)));
typedef float f4 __attribute__((ext_vector_type(4)));
typedef float f2 __attribute__((ext_vector_type(2)));
typedef float f16v __attribute__((ext_vector_type(16)));
typedef short s8v __attribute__((ext_vector_type(8)));
typedef short s4v __attribute__((ext_vector_type(4)));
typedef __bf16 bf2 __attribute__((ext_vector_type(2)));
#define GAS __attribute__((address_space(1)))
#define LAS __attribute__((address_space(3)))
#define CAS __attribute__((address_space(4)))
#define DI __device__ __forceinline__
typedef __amdgpu_buffer_rsrc_t srd_t;
typedef LAS unsigned char* lptr;

constexpr int S = 2048, D = 512, F = 1792;
constexpr float EPS = 1e-5f;
constexpr u32 WS_EPOCH = 0, WS_FKV = 4096, WS_FST = 36864, WS_FH = 40960, WS_FY = 69632, WS_STATS = 131072, WS_CSUM = 196608,
              WS_KVT = 1u << 20, WS_YT = 3u << 20, WS_H = 5u << 20, WS_BYTES = 13u << 20;
constexpr u32 FLAG_STRIDE = 128;
constexpr u32 L_SLOT = 32768;
constexpr u32 L_HC = 98304, HC_STRIDE = 4160, L_U = L_HC + 8 * HC_STRIDE, U_STRIDE = 4608;
constexpr u32 L_PK = L_U + 2 * U_STRIDE, L_PV = L_PK + 72 * 128, L_PW = L_PV + 64 * 128;
constexpr u32 L_JUNK = L_PW + 1024, L_CNT0 = L_JUNK + 256;
constexpr u32 L_Y = L_PK, L_X = L_PK;
constexpr u32 LDS_BYTES = 163840;
static_assert(L_JUNK + 256 <= LDS_BYTES, "LDS map");
constexpr u32 SPIN_CAP = 1u << 16;

struct Params {
  const u16 *x, *k, *v, *sk, *ssm, *dd, *w1, *b1, *w2, *b2;
  u16* out;
  unsigned char* ws;
};

DI srd_t mk_srd(const void* p, u32 bytes) { return __builtin_amdgcn_make_buffer_rsrc((void*)p, (short)0, (int)bytes, 0x00020000); }
DI float asf(u32 d) { return __builtin_bit_cast(float, d); }
DI u32 asu(float f) { return __builtin_bit_cast(u32, f); }
DI bool all_lanes(bool c) { return __builtin_amdgcn_ballot_w64(!c) == 0; }
DI float bl(u32 d) { return asf(d << 16); }
DI float bh(u32 d) { return asf(d & 0xffff0000u); }
DI u32 pk(float a, float b) { f2 t = {a, b}; return __builtin_bit_cast(u32, __builtin_convertvector(t, bf2)); }
DI float rest(float v) { return v - bh(asu(v)); }
DI u32 pkt(float lo, float hi) { return __builtin_amdgcn_perm(asu(hi), asu(lo), 0x07060302u); }
DI f4 mfma(v4u a, v4u b, f4 c) { return __builtin_amdgcn_mfma_f32_16x16x32_bf16(__builtin_bit_cast(s8v, a), __builtin_bit_cast(s8v, b), c, 0, 0, 0); }
DI f16v mfma32(v4u a, v4u b, f16v c) { return __builtin_amdgcn_mfma_f32_32x32x16_bf16(__builtin_bit_cast(s8v, a), __builtin_bit_cast(s8v, b), c, 0, 0, 0); }
DI lptr lp(u32 a) { return (lptr)(uintptr_t)a; }
DI v2u trread(u32 a) { return __builtin_bit_cast(v2u, __builtin_amdgcn_ds_read_tr16_b64_v4i16((LAS s4v*)lp(a))); }
DI v4u trfrag(u32 a, u32 hi_off) { const v2u lo = trread(a), hi = trread(a + hi_off); return v4u{lo.x, lo.y, hi.x, hi.y}; }
DI v4u lr128(u32 a) { return *(LAS v4u*)lp(a); }
DI f4 lrf4(u32 a) { return *(LAS f4*)lp(a); }
DI void lw128(u32 a, v4u v) { *(LAS v4u*)lp(a) = v; }
DI void lwf4(u32 a, f4 v) { *(LAS f4*)lp(a) = v; }
#define DMA_(OP, FL, rs, voff, sb, SI, lb, LI) do { u32 t_; \
  asm volatile("s_add_u32 m0, %1, %2\n\ts_add_u32 %0, %3, %4\n\t" OP " %5, %6, %0 offen" FL " lds" \
               : "=&s"(t_) : "s"(lb), "i"(LI), "s"(sb), "i"(SI), "v"(voff), "s"(rs) : "memory", "scc"); } while (0)
#define DFL ""
#define DMA(rs, voff, sb, SI, lb, LI) DMA_("buffer_load_dwordx4", DFL, rs, voff, sb, SI, lb, LI)
#define SSM_FL ""
#define DMANT(rs, voff, sb, SI, lb, LI) DMA_("buffer_load_dwordx4", " nt", rs, voff, sb, SI, lb, LI)
#define M0SET(lb, LI) asm volatile("s_add_u32 m0, %0, %1\n\ts_nop 0" ::"s"(lb), "i"(LI) : "memory", "scc")
#define DML(rs, voff, so, OFF) asm volatile("buffer_load_dwordx4 %0, %1, %2 offen offset:%3 lds" ::"v"(voff), "s"(rs), "s"(so), "i"(OFF) : "memory")
#define TOUCH(rs, voff, sb, SI) DMA_("buffer_load_dword", "", rs, voff, sb, SI, 0u, L_JUNK)
#define ALD128(d, off, rs, FL) asm volatile("buffer_load_dwordx4 %0, %1, %2, 0 offen" FL : "=v"(d) : "v"(off), "s"(rs) : "memory")
#define ALD128S(d, off, rs, so, FL) asm volatile("buffer_load_dwordx4 %0, %1, %2, %3 offen" FL : "=v"(d) : "v"(off), "s"(rs), "s"(so) : "memory")
#define ALD64(d, off, rs, FL) asm volatile("buffer_load_dwordx2 %0, %1, %2, 0 offen" FL : "=v"(d) : "v"(off), "s"(rs) : "memory")
#define ALD16(d, off, rs, FL) asm volatile("buffer_load_ushort %0, %1, %2, 0 offen" FL : "=v"(d) : "v"(off), "s"(rs) : "memory")
#define WAITV(n) asm volatile("s_waitcnt vmcnt(" #n ")" ::: "memory")
#define PIN(a) asm volatile("" : "+v"(a)::"memory")
#define KEEP(a) asm volatile("" : "+v"(a))
#define KEEPP(a) asm volatile("" : "+s"(a))
#define KEEPS(a) do { a = __builtin_amdgcn_readfirstlane(a); asm volatile("" : "+s"(a)); } while (0)
#define WAITL() asm volatile("s_waitcnt lgkmcnt(0)" ::: "memory")
#define BAR() __builtin_amdgcn_s_barrier()
#define VBAR(n) do { __builtin_amdgcn_s_waitcnt(0xC07F); asm volatile("s_waitcnt vmcnt(" #n ") lgkmcnt(0)\n\ts_barrier" ::: "memory"); } while (0)
#define LBAR() do { __builtin_amdgcn_s_waitcnt(0xC07F); asm volatile("s_waitcnt lgkmcnt(0)\n\ts_barrier" ::: "memory"); } while (0)
#define SB() __builtin_amdgcn_sched_barrier(0)
DI float wave_sum(float v) {
  v += asf(__builtin_amdgcn_update_dpp(0u, asu(v), 0xB1, 0xF, 0xF, true));
  v += asf(__builtin_amdgcn_update_dpp(0u, asu(v), 0x4E, 0xF, 0xF, true));
  v += asf(__builtin_amdgcn_update_dpp(0u, asu(v), 0x141, 0xF, 0xF, true));
  v += asf(__builtin_amdgcn_update_dpp(0u, asu(v), 0x140, 0xF, 0xF, true));
  const auto a = __builtin_amdgcn_permlane16_swap(asu(v), asu(v), false, false);
  v = asf(a[0]) + asf(a[1]);
  const auto c = __builtin_amdgcn_permlane32_swap(asu(v), asu(v), false, false);
  return asf(c[0]) + asf(c[1]);
}
DI v4u ld_sc1(srd_t rs, u32 off, u32 soff = 0) { return __builtin_amdgcn_raw_buffer_load_b128(rs, off, soff, 16); }
template <int IMM = 0> DI void st_sc1(srd_t rs, u32 off, v4u v, u32 soff = 0) {
  asm volatile("buffer_store_dwordx4 %0, %1, %2, %3 offen offset:%4 sc1\n\ts_nop 1" ::"v"(v), "v"(off), "s"(rs), "s"(soff), "i"(IMM) : "memory");
}
DI void put_flag(srd_t rs, u32 l0, u32 off, u64 tag) { __builtin_amdgcn_raw_buffer_store_b64(__builtin_bit_cast(v2u, tag), rs, l0, off, 16); }
DI u64 get_flag(srd_t rs, u32 off) { return __builtin_bit_cast(u64, __builtin_amdgcn_raw_buffer_load_b64(rs, off, 0, 16)); }
DI void wait_flags(srd_t rs, u32 off, u64 tag, u64 v) {
  if (!all_lanes(v == tag)) {
#pragma unroll 1
    for (u32 spins = 0; spins < SPIN_CAP; ++spins) {
      u64 x;
      asm volatile("buffer_load_dwordx2 %0, %1, %2, 0 offen sc1\n\ts_waitcnt vmcnt(0)" : "=&v"(x) : "v"(off), "s"(rs) : "memory");
      if (all_lanes(x == tag)) break;
      __builtin_amdgcn_s_sleep(1);
    }
  }
  asm volatile("" ::: "memory");
}
DI void arrive_publish(u32 cnt, srd_t rs, u32 l0, u32 off, u64 tag) {
  u32 old;
  asm volatile("s_mov_b64 exec, 1\n\tds_add_rtn_u32 %0, %1, %2\n\ts_mov_b64 exec, -1\n\ts_waitcnt lgkmcnt(0)" : "=&v"(old) : "v"(cnt), "v"(1u) : "memory");
  if (__builtin_amdgcn_readfirstlane(old) == 3u) put_flag(rs, l0, off, tag);
}
#define STAMP_AT(i)
#define STAMP_F(i)
struct ConvList { int kh[144], e[144]; bool newA[144], newB[144]; };
constexpr int KEEP_MASK[2] = {0x99, 0x66};
constexpr int TS[2][4] = {{0, 3, 4, 7}, {1, 2, 5, 6}};
constexpr ConvList make_list(int role) {
  ConvList L = {};
  int n = 0;
  for (int kl = 0; kl < 4; ++kl) {
    bool seen[16] = {};
    for (int part = 0; part < (kl == 3 ? 2 : 1); ++part) {
      const int mask = kl < 3 ? 0xff : KEEP_MASK[part ? role : role ^ 1];
      for (int kh = 0; kh < 8; ++kh)
        for (int T = 7; T >= kh; --T) {
          if (!((mask >> T) & 1)) continue;
          const int e = T - kh;
          L.kh[n] = kh; L.e[n] = e; L.newA[n] = !seen[kh]; L.newB[n] = !seen[8 + e];
          seen[kh] = seen[8 + e] = true;
          ++n;
        }
    }
  }
  return L;
}
template <class F, int... I> DI void static_for_(F&& f, std::integer_sequence<int, I...>) { (f(std::integral_constant<int, I>{}), ...); }
template <int N, class F> DI void static_for(F&& f) { static_for_(f, std::make_integer_sequence<int, N>{}); }
#define CLA 10
#define NPF 0
#define ZX 1
#define ZW1 1
#define ZW2 1

__global__ void __launch_bounds__(512, 2) fused(Params p) {
  const int tid = __builtin_amdgcn_workitem_id_x(), lane = tid & 63, w = __builtin_amdgcn_readfirstlane(tid >> 6);
  const int b = __builtin_amdgcn_workgroup_id_x(), xcd = b & 7, idx = b >> 3;
  const int g = lane >> 4, r16 = lane & 15, q4 = r16 >> 2, p4 = lane & 3, rr = lane >> 3;
  STAMP_AT(0);
  if (w == 0) {
    u64 pc; u32 len;
    asm volatile("mk_code_begin:\n\ts_getpc_b64 %0\n\ts_mov_b32 %1, mk_code_end-mk_code_begin\n\ts_max_i32 %1, %1, mk_code_end2-mk_code_begin" : "=s"(pc), "=s"(len) :: "scc");
    const srd_t rc = mk_srd((const void*)pc, len);
    TOUCH(rc, (u32)(lane * 128), (u32)((idx & 3) * 8192), 0);
  }
  SB();
  KEEPP(p.ws); KEEPP(p.out); KEEPP(p.w2); KEEPP(p.b2); KEEPP(p.k);
  const int cp = 32 * xcd + idx;
  u64 tag = *(const CAS u64*)(p.ws + WS_EPOCH);
  u32 ddp = *(const CAS u32*)(p.dd + 2 * cp);
  const bool is_g1 = idx < 28;
  const srd_t rx = mk_srd(p.x, S * D * 2 * ZX), rw1 = mk_srd(p.w1 - 1024, D * F * 2 * ZW1 + 2048);
  const u32 w1k = w * 1024, w4k = w * 4096;
  v4u xr;
  const int stok = 256 * xcd + 8 * idx + w;
  ALD128(xr, (u32)(stok * 1024 + lane * 16), rx, "");
  SB();
  const int sidx = idx & 3, ti = 4 * (xcd >> 1) + sidx, t1 = idx >> 2, tj = 7 * (xcd & 1) + t1;
  const int hm = 4 * (xcd >> 1) + sidx, cgp = 14 * (xcd & 1) + (hm < 14 ? hm : 0);
  const u32 chsw = (u32)(((lane & 7) ^ (rr & 6)) << 4);
  const u32 xvo = (u32)((128 * ti + 16 * w + rr) * 1024 + (((lane & 7) ^ g) << 4)), xvo1 = (xvo ^ 64) + 7168;
  const u32 wvo = (u32)((8 * w + (is_g1 ? g : rr)) * 3584 + (is_g1 ? tj * 256 : cgp * 128)) + (is_g1 ? (u32)((r16 ^ (g << 2)) << 4) : chsw), wvo1 = wvo + 13312;
#define ROT1 ((4 * ((t1 + sidx) & 1) + xcd) & 7)
  u32 sox = (u32)(ROT1) * 128, sow = sox * 1792;
#define NEXT_CHUNK() do { sox = (sox + 128) & 1023; sow = sox * 1792; } while (0)
#define PIECE(c, i) do { \
    if ((i) == 0) { M0SET(w4k, ((c) % 3) * L_SLOT); DML(rx, xvo, sox, 0); } \
    if ((i) == 1) DML(rx, xvo1, sox, 1024); \
    if ((i) == 2) DML(rw1, wvo, sow, 2048); \
    if ((i) == 3) { DML(rw1, wvo1, sow, 3072); NEXT_CHUNK(); } } while (0)
#define HPIECE(c) DMA(rw1, wvo, 0u, (c) * 64 * 3584 + 2048, w1k, (c) * 8192)
  if (is_g1) { PIECE(0, 0); PIECE(0, 1); PIECE(0, 2); PIECE(0, 3); }
  else { HPIECE(0); HPIECE(1); HPIECE(2); HPIECE(3); }
  SB();

  const srd_t rws = mk_srd(p.ws, WS_BYTES);
  const int tm = 4 * xcd + sidx;
  const int tn = (t1 + 4 - (int)(((uintptr_t)p.w2 >> 7) & 7)) & 7;
  const int mi = w & 3, ni = w >> 2, hl = lane >> 5, l31 = lane & 31;
  u32 l0 = lane == 0 ? 0u : 0x80000000u, l01 = lane < 2 ? lane * 16 : 0x80000000u;
  u32 s_st = WS_STATS + (u32)stok * 32;
  const int m2 = w & 1, nq = w >> 1;
  u32 xa[2][4], wa[2];
#pragma unroll
  for (int ks = 0; ks < 4; ++ks) { xa[0][ks] = (4 * m2 + (g & 1)) * 4096 + r16 * 128 + (((2 * ks + hl) ^ (r16 >> 1)) << 4); xa[1][ks] = xa[0][ks] + 2 * L_SLOT; }
  wa[0] = 2048 + hl * 4096 + q4 * 256 + (((4 * nq + 2 * (g & 1) + (p4 >> 1)) ^ (q4 << 2)) << 4) + 8 * (p4 & 1);
  wa[1] = wa[0] + 2 * L_SLOT;
  u32 kvo = (u32)(rr * 1024 + (((lane & 7) ^ (rr & 2)) << 4)), kso = (u32)((64 * idx + 8 * w) * 1024 + xcd * 128);
  KEEP(l0); KEEP(l01); KEEPS(s_st); KEEP(kvo); KEEPS(kso);
#pragma unroll
  for (int i = 0; i < 4; ++i) { KEEP(xa[0][i]); KEEP(xa[1][i]); }
  KEEP(wa[0]); KEEP(wa[1]);
#define STAGE_K() do { const srd_t rk = mk_srd(p.k, S * D * 2); DMANT(rk, kvo, kso, 0, w1k, L_PK + 1024); \
    if (w == 0) DMANT(rk, kvo + kso - 8192u, 0u, 0, 0u, L_PK); } while (0)
#define STAGE_V() do { const srd_t rv = mk_srd(p.v, S * D * 2); DMANT(rv, kvo, kso, 0, w1k, L_PV); \
    if (w == 1) { const srd_t rsk = mk_srd(p.sk, D * 4 * 2); DMA(rsk, (u32)(lane * 16), (u32)(xcd * 512), 0, 0u, L_PW); } } while (0)
  constexpr u32 TR_HI = 1024;
  const srd_t rw2 = mk_srd(p.w2, F * D * 2 * ZW2), rssm = mk_srd(p.ssm + cp * 4096, 8192);
  u32 a_kv, a_tap, o_kv, s_kv, w2vo[4], w2base, ksh[7], s_t0, ta[4], t16, a_hc, fo1, fo2, o_xe, o_we, o_out, o_h, s_h, hb, s_cp;
  v2u ck[3], cv[2], ctw;
  float cw[4], kk[12], vv[8], co[8];
  const u32 l16 = lane * 16;
  auto fill = [&](auto KK) {
    constexpr int K = decltype(KK)::value;
    if constexpr (K == 200) { s_t0 = (u32)((224 * t1 + 8 * sidx) * 1024) + (ksh[0] << 5); KEEPS(s_t0); }
    if constexpr (K == 201) { a_kv = L_PK + 512 + ni * 4096 + (8 * g + q4) * 128 + (((2 * mi + (p4 >> 1)) ^ (q4 & 2)) << 4) + 8 * (p4 & 1); KEEP(a_kv); }
    if constexpr (K == 202) { a_tap = L_PW + mi * 128 + r16 * 8; KEEP(a_tap);
#pragma unroll
      for (int i = 0; i < 4; ++i) { w2vo[i] = (u32)(rr * 1024 + i * 7168) + chsw; KEEP(w2vo[i]); } }
    if constexpr (K == 203) { o_kv = (u32)((r16 >> 1) * 8192 + (r16 & 1) * 64 + (3 - g) * 16); KEEP(o_kv); }
    if constexpr (K == 204) { s_kv = WS_KVT + (u32)((32 * xcd + 8 * mi) * 8192 + (63 - 2 * idx - ni) * 128); KEEPS(s_kv); }
    if constexpr (K == 205) {
      if (w == 2) lw128(L_U + hl * U_STRIDE + 4096 + l31 * 16, v4u{0, 0, 0, 0});
      if (w == 3) lw128(L_CNT0 + (lane & 1) * 16, v4u{0, 0, 0, 0});
    }
    if constexpr (K == 9) { fo1 = WS_FKV + ((16 * (ti & 1) + 8 * m2) * 8 + (ti >> 1)) * FLAG_STRIDE + (lane & 7) * (8 * FLAG_STRIDE); KEEP(fo1); }
    if constexpr (K == 13) { const u32 fk = WS_FKV + ((8 * sidx + w) * 8 + xcd) * FLAG_STRIDE;
                             fo1 = (lane & (is_g1 ? 16 : 0)) ? fo1 : fk; KEEP(fo1); }
    if constexpr (K == 17) { const int m = 2 * t1 + ni; const u32 fc = WS_FST + (((m >> 2) * 2 + (xcd & 1)) * 4 + (m & 3)) * FLAG_STRIDE;
                             fo1 = (lane & (is_g1 ? 8 : 0)) ? fc : fo1; KEEP(fo1); }
    if constexpr (K == 29) { t16 = tid * 16; o_out = (u32)((64 * tm + 8 * w) * 1024 + 128 * tn) + rr * 1024 + (lane & 7) * 16; KEEP(t16); KEEP(o_out); }
    if constexpr (K == 33) { o_xe = WS_STATS + (u32)((128 * ti + 64 * m2) * 32) + lane * 32; o_we = WS_CSUM + (u32)((128 * tj + 32 * nq) * 16) + l31 * 16; KEEP(o_xe); KEEP(o_we); }
    if constexpr (K == 37) { const int trow = 16 * hl + 4 * (g & 1) + q4; ta[0] = L_SLOT + w4k + trow * 128 + (((p4 >> 1) ^ (trow & 6)) << 4) + 8 * (p4 & 1); KEEP(ta[0]); }
    if constexpr (K == 41) { ta[1] = ta[0] ^ 32; ta[2] = ta[0] ^ 64; ta[3] = ta[0] ^ 96; KEEP(ta[1]); KEEP(ta[2]); KEEP(ta[3]);
                             a_hc = L_HC + ni * (4 * HC_STRIDE - 4096) + 32 + t16; KEEP(a_hc); }
    if constexpr (K == 21) { const u32 fkv = WS_FKV + ((31 - 4 * mi) * 8 + xcd) * FLAG_STRIDE - (p4 + 4 * (lane & 4)) * (8 * FLAG_STRIDE);
                             fo1 = hl ? fkv : fo1; KEEP(fo1); }
    if constexpr (K == 45) { o_h = (u32)((g & 1) * 57344 + r16 * 16 + hl * 256); KEEP(o_h); }
    if constexpr (K == 49) { s_h = WS_H + (u32)(((8 * ti + 4 * m2) * 56 + 4 * tj + nq) * 1024); KEEPS(s_h); }
    if constexpr (K == 100) {
      ctw = *(LAS v2u*)lp(a_tap);
      ck[0] = trread(a_kv); ck[1] = trread(a_kv + 512); ck[2] = trread(a_kv + 1024);
      cv[0] = trread(a_kv + (L_PV - L_PK - 512)); cv[1] = trread(a_kv + (L_PV - L_PK));
    }
    if constexpr (K == 102) {
      cw[0] = bl(ctw.x); cw[1] = bh(ctw.x); cw[2] = bl(ctw.y); cw[3] = bh(ctw.y);
      kk[1] = bh(ck[0].x); kk[2] = bl(ck[0].y); kk[3] = bh(ck[0].y);
#pragma unroll
      for (int i = 0; i < 4; ++i) KEEP(cw[i]);
#pragma unroll
      for (int i = 1; i < 4; ++i) KEEP(kk[i]);
    }
    if constexpr (K >= 103 && K < 111) {
      constexpr int j = K - 103;
      const u32 kd = ck[1 + j / 4][(j / 2) & 1], vd = cv[j / 4][(j / 2) & 1];
      kk[j + 4] = (j & 1) ? bh(kd) : bl(kd); vv[j] = (j & 1) ? bh(vd) : bl(vd);
      asm volatile("v_mul_f32 %0, %1, %5\n\tv_fmac_f32 %0, %2, %6\n\tv_fmac_f32 %0, %3, %7\n\tv_fmac_f32 %0, %4, %8\n\tv_mul_f32 %0, %0, %9"
                   : "=&v"(co[j]) : "v"(kk[j + 1]), "v"(kk[j + 2]), "v"(kk[j + 3]), "v"(kk[j + 4]), "v"(cw[3]), "v"(cw[2]), "v"(cw[1]), "v"(cw[0]), "v"(vv[j]));
    }
    if constexpr (K == 111) st_sc1(rws, o_kv, v4u{pk(co[7], co[6]), pk(co[5], co[4]), pk(co[3], co[2]), pk(co[1], co[0])}, s_kv);
  };
  SB();
  BAR();
  STAGE_K(); STAGE_V();
  WAITV(6);
  PIN(xr);
  STAMP_AT(1);
  {
    float xv8[8];
#pragma unroll
    for (int j = 0; j < 4; ++j) { xv8[2 * j] = bl(xr[j]); xv8[2 * j + 1] = bh(xr[j]); }
    const float mean = wave_sum(((xv8[0] + xv8[1]) + (xv8[2] + xv8[3])) + ((xv8[4] + xv8[5]) + (xv8[6] + xv8[7]))) * (1.0f / D);
    float q0 = 0.f, q1 = 0.f;
#pragma unroll
    for (int j = 0; j < 4; ++j) { const float d0 = xv8[2 * j] - mean, d1 = xv8[2 * j + 1] - mean; q0 += d0 * d0; q1 += d1 * d1; }
    const float var = wave_sum(q0 + q1) * (1.0f / D);
    const float r1 = __builtin_amdgcn_rsqf(var + EPS);
    const float r2 = __builtin_amdgcn_rsqf(var * r1 * r1 + EPS);
    const float A = r1 * r2, m0 = -mean, m1 = rest(m0), m2 = rest(m1);
    float iA = __builtin_amdgcn_rcpf(A);
    u32 d0 = pkt(m0, m0), d1 = pkt(m1, m1), d2 = pkt(m2, m0), d3 = pkt(iA, rest(iA));
    KEEP(d0); KEEP(d1); KEEP(d2); KEEP(d3);
    const bool l1 = lane & 1;
    const u32 mv = pk(mean, var);
    float rq = __builtin_amdgcn_rcpf(bl(pk(__builtin_amdgcn_sqrtf(bl(pk(bh(mv) + EPS, 0.f))), 0.f))), mq = -bl(mv) * rq;
    KEEP(rq); KEEP(mq);
    st_sc1(rws, l01, v4u{l1 ? asu(rq) : d0, l1 ? asu(mq) : d1, d2, d3}, s_st);
  }
  tag += 0x9E3779B97F4A7C15ull;
  { int t = xcd;
    KEEPS(t);
#pragma unroll
    for (int s = 0; s < 7; ++s) { t = t >= 7 ? t - 7 : t; ksh[s] = (u32)t * 1024; KEEPS(ksh[s]); ++t; } }
  w2base = (u32)(224 * w * 1024 + tn * 128);
  s_cp = (u32)cp * 8192;
  KEEPS(w2base); KEEPS(s_cp);
  SB();
  if (is_g1) { PIECE(1, 0); PIECE(1, 1); PIECE(1, 2); PIECE(1, 3); }
  else { HPIECE(4); HPIECE(5); HPIECE(6); HPIECE(7); }
  static_for<6>([&](auto I) { fill(std::integral_constant<int, 200 + decltype(I)::value>{}); });
  if (w == 6) TOUCH(rw2, (u32)(lane * 128), s_t0, 0);
#define MAKE_KV() static_for<12>([&](auto I) { fill(std::integral_constant<int, 100 + decltype(I)::value>{}); })
  SB();
  STAMP_AT(2);
  v4u hd; v2u hp;
  v4u w2f[7][4];
  u32 sw2;
#define W2SLOT(s) ((s) == 0 ? 3 : ((s) + 1) % 3)
#define W2_PIECE(s, i) do { if ((i) == 0) { sw2 = w2base + (ksh[s] << 5); M0SET(w4k, W2SLOT(s) * L_SLOT); } DML(rw2, w2vo[i], sw2, (i) * 1024); } while (0)
#define W2_STAGE(s) do { W2_PIECE(s, 0); W2_PIECE(s, 1); W2_PIECE(s, 2); W2_PIECE(s, 3); } while (0)
  v4u xv, b2v; v2u stv;
#define LOAD_SSM() do { ALD128(hd, t16, rssm, SSM_FL); ALD64(hp, t16 - 8, rssm, SSM_FL); } while (0)
  v4u u0, u1;
#define LOAD_U() do { ALD128S(u0, t16, rws, s_cp + WS_KVT, " sc1"); ALD128S(u1, t16, rws, s_cp + WS_KVT + 4096, " sc1"); } while (0)
#define LOAD_LAST() do { ALD128(xv, o_out, rx, ""); const srd_t rb2 = mk_srd(p.b2, D * 2); ALD128(b2v, o_out & 1023u, rb2, ""); \
    ALD64(stv, WS_STATS + 16 + (o_out >> 10) * 32, rws, " sc1"); } while (0)
  auto w2_read = [&](int s) {
#pragma unroll
    for (int t = 0; t < 4; ++t) w2f[s][t] = trfrag(ta[t] + W2SLOT(s) * L_SLOT - L_SLOT, TR_HI);
  };
  auto write_hc = [&](int rho) {
    const bool first = (tid & 255) == 0;
    const u32 p0 = first ? 0u : hp.x, p1 = first ? 0u : hp.y;
    const u32 a0 = __builtin_amdgcn_alignbit(p1, p0, 16), a1 = __builtin_amdgcn_alignbit(hd.x, p1, 16), a2 = __builtin_amdgcn_alignbit(hd.y, hd.x, 16),
              a3 = __builtin_amdgcn_alignbit(hd.z, hd.y, 16), a4 = __builtin_amdgcn_alignbit(hd.w, hd.z, 16);
    if (rho == 0) lw128(a_hc, hd);
    if (rho == 1) lw128(a_hc + HC_STRIDE, v4u{a1, a2, a3, a4});
    if (rho == 2) lw128(a_hc + 2 * HC_STRIDE, v4u{p1, hd.x, hd.y, hd.z});
    if (rho == 3) lw128(a_hc + 3 * HC_STRIDE, v4u{a0, a1, a2, a3});
  };

#define CONV_SETUP() do { PIN(hd); PIN(hp); write_hc(0); write_hc(1); write_hc(2); write_hc(3); \
    if (w == 4) lw128(L_HC + rr * HC_STRIDE + (lane & 1) * 16, v4u{0, 0, 0, 0}); } while (0)

  if (__builtin_expect(is_g1, 1)) {
    f16v acc[2];
#pragma unroll
    for (int i = 0; i < 16; ++i) { acc[0][i] = 0.f; acc[1][i] = 0.f; }
    v4u xf[2][2][2], wf[2][2];
#define REQ(dc, i) do { if ((dc) >= 8) W2_PIECE((dc) & 3, i); else PIECE(dc, i); } while (0)
#define STEP(st, dc, d0, nd) do { \
      constexpr int hs_ = (st) & 1, hn_ = hs_ ^ 1, sl_ = (((st) + 1) >> 1) % 3, sb_ = sl_ >> 1; constexpr u32 so_ = (sl_ & 1) * L_SLOT; \
      constexpr bool rd_ = (st) < 15, mm_ = (st) >= 0; \
      _Pragma("unroll") for (int i_ = 0; i_ < 4; ++i_) { \
        if (mm_) acc[i_ & 1] = mfma32(wf[hs_][i_ >> 1], xf[hs_][i_ >> 1][i_ & 1], acc[i_ & 1]); \
        if (rd_) { \
          if (!(i_ & 1)) wf[hn_][i_ >> 1] = trfrag(wa[sb_] + so_ + (2 * hn_ + (i_ >> 1)) * 8192, 1024); \
          else { xf[hn_][i_ >> 1][0] = lr128(xa[sb_][2 * hn_ + (i_ >> 1)] + so_); xf[hn_][i_ >> 1][1] = lr128(xa[sb_][2 * hn_ + (i_ >> 1)] + so_ + 8192); } } \
        if ((nd) == 4) { if (i_ == 0) REQ(dc, 0); if (i_ == 1) REQ(dc, 1); if (i_ == 2) REQ(dc, 2); if (i_ == 3) REQ(dc, 3); } \
        if ((nd) == 2) { if (i_ == 0) REQ(dc, d0); if (i_ == 2) REQ(dc, (d0) + 1); } \
        if (mm_) { if (i_ == 0) fill(std::integral_constant<int, 4 * (st)>{}); if (i_ == 1) fill(std::integral_constant<int, 4 * (st) + 1>{}); \
                   if (i_ == 2) fill(std::integral_constant<int, 4 * (st) + 2>{}); if (i_ == 3) fill(std::integral_constant<int, 4 * (st) + 3>{}); } \
        SB(); } } while (0)
#define ITER(c) do { STAMP_F(16 + (c)); STEP(2 * (c) + 1, (c) + 3, 0, 2); STEP(2 * (c) + 2, (c) + 3, 2, 2); } while (0)
    u64 pv; v4u xe, we;
    VBAR(7);
    STAMP_AT(15);
    STEP(-1, 0, 0, 0);
    STEP(0, 2, 0, 4);
    VBAR(9);
    STAMP_F(30);
    MAKE_KV();
    SB();
    VBAR(5);
    ITER(0);
    VBAR(5);
    ITER(1); W2_PIECE(0, 0); W2_PIECE(0, 1);
    VBAR(6);
    if (w == 0) put_flag(rws, l0, WS_FKV + b * FLAG_STRIDE, tag);
    ITER(2); M0SET(w4k, 3 * L_SLOT); W2_PIECE(0, 2); W2_PIECE(0, 3);
    VBAR(8);
    ITER(3);
    VBAR(6);
    ITER(4);
    ALD64(pv, fo1, rws, " sc1");
    LOAD_SSM();
    VBAR(7);
    STAMP_F(21);
    STEP(11, 9, 0, 2); STEP(12, 9, 2, 2);
    VBAR(6);
    STAMP_F(22);
    PIN(pv);
    wait_flags(rws, fo1, tag, pv);
    ALD128(xe, o_xe, rws, " sc1");
    ALD128(we, o_we, rws, " sc1");
    if (!ni) LOAD_U();
    STEP(13, 0, 0, 0); w2_read(0); STEP(14, 10, 0, 2);
    LBAR();
    STAMP_F(23);
    STEP(15, 10, 2, 2);
    if (ni) WAITV(4); else WAITV(6);
    PIN(xe); PIN(we);
    {
      const v4u z = {0, 0, 0, 0};
      acc[0] = mfma32(we, hl ? z : xe, acc[0]);
      acc[1] = mfma32(we, hl ? xe : z, acc[1]);
    }
    STAMP_AT(3);
    if (ni) { CONV_SETUP(); SB(); }
    f2 k1v[2], k3v[2], iAv[2];
    { const auto r = __builtin_amdgcn_permlane32_swap(xe.w, xe.w, false, false);
#pragma unroll
      for (int m = 0; m < 2; ++m) {
        const float iA = bl(r[m]) + bh(r[m]), A = __builtin_amdgcn_rcpf(iA), k1s = -2.3022082f * A, k3s = -0.10294324f * A * A * A;
        k1v[m] = f2{k1s, k1s}; k3v[m] = f2{k3s, k3s}; iAv[m] = f2{iA, iA};
      } }
#pragma unroll
    for (int qi = 0; qi < 4; ++qi) {
      const int n = qi >> 1, sp = qi & 1;
      if (qi == 0) W2_PIECE(3, 0);
      if (qi == 1) W2_PIECE(3, 1);
      if (qi == 2) W2_PIECE(3, 2);
      if (qi == 3) W2_PIECE(3, 3);
      v4u o;
#pragma unroll
      for (int j = 0; j < 4; ++j) {
        const f2 a = {acc[n][8 * sp + 2 * j], acc[n][8 * sp + 2 * j + 1]}, k1 = k1v[n], k3 = k3v[n], iA = iAv[n];
        const f2 z = a * __builtin_elementwise_fma(a * a, k3, k1);
        const f2 e = {__builtin_amdgcn_exp2f(z.x), __builtin_amdgcn_exp2f(z.y)};
        const f2 d = __builtin_elementwise_fma(e, iA, iA);
        const f2 hv = a * f2{__builtin_amdgcn_rcpf(d.x), __builtin_amdgcn_rcpf(d.y)};
        o[j] = pk(hv.x, hv.y);
      }
      if (qi == 0) st_sc1<0>(rws, o_h, o, s_h);
      if (qi == 1) st_sc1<512>(rws, o_h, o, s_h);
      if (qi == 2) st_sc1<0>(rws, o_h, o, s_h + 114688);
      if (qi == 3) st_sc1<512>(rws, o_h, o, s_h + 114688);
      SB();
    }
    STAMP_AT(4);
  } else {
    u32 b1v;
    { const srd_t rb1 = mk_srd(p.b1 + 64 * cgp, 128); ALD16(b1v, (u32)(16 * mi + 4 * g + p4) * 2, rb1, ""); }
    static_for<45>(fill);
    VBAR(0);
    STAMP_AT(15);
    if (w < 4) {
      const v4u ones = {0x3f803f80u, 0x3f803f80u, 0x3f803f80u, 0x3f803f80u};
      f4 s0 = {0, 0, 0, 0};
      const u32 a = (w == 0 ? ta[0] : w == 1 ? ta[1] : w == 2 ? ta[2] : ta[3]) - L_SLOT - w4k;
#pragma unroll
      for (int st = 0; st < 16; ++st) s0 = mfma(trfrag(a + st * 4096, TR_HI), ones, s0);
      const float ca = (p4 & 1) ? s0[1] : s0[0], cb = (p4 & 1) ? s0[3] : s0[2], c = (p4 & 2) ? cb : ca;
      const float c1 = rest(c), c2 = rest(c1);
      PIN(b1v);
      st_sc1(rws, r16 < 4 && hm < 14 ? WS_CSUM + (u32)(64 * cgp + 16 * w + 4 * g + p4) * 16 : 0x80000000u, v4u{pkt(c, c1), pkt(c, c1), pkt(c, c2), b1v * 0x10001u});
    }
    MAKE_KV();
    VBAR(0);
    if (w == 0) { put_flag(rws, l0, WS_FST + (4 * xcd + sidx) * FLAG_STRIDE, tag); put_flag(rws, l0, WS_FKV + b * FLAG_STRIDE, tag); }
    STAMP_AT(3);
#pragma unroll
    for (int i = 1; i <= NPF; ++i) { u32 kk = ksh[i]; KEEPS(kk); TOUCH(rw2, (u32)(lane * 128), (u32)((224 * w + 8 * sidx) * 1024) + (kk << 5), 0); }
    LOAD_SSM();
    W2_STAGE(0); W2_STAGE(1); W2_STAGE(2); W2_STAGE(3);
    if (b == 255) {
      if (w < 4) wait_flags(rws, WS_FKV + tid * FLAG_STRIDE, tag, ~tag);
      BAR();
      if (w == 0) put_flag(rws, l0, WS_EPOCH, tag);
    }
    wait_flags(rws, fo1, tag, ~tag);
    w2_read(0);
    STAMP_AT(4);
    LBAR();
    asm volatile("mk_code_end2:");
    if (ni) CONV_SETUP(); else LOAD_U();
  }
#define LATE_SETUP() do { const int tja = ((224 * w) >> 7) + p4, tjb = (224 * w + 223) >> 7; \
    fo2 = WS_FH + ((tm >> 1) * 14 + (tja < tjb ? tja : tjb)) * FLAG_STRIDE; hb = WS_H + (u32)((4 * tm * 56 + 7 * w) * 1024) + l16; KEEP(fo2); KEEP(hb); } while (0)
  v4u hf[7];
  u32 hbm;
#define MROT (2 * tn)
  const int mrot = MROT;
  auto load_h = [&](int m, int s) { if (s == 0) hbm = hb + (u32)(((m + mrot) & 3) * 57344); hf[s] = ld_sc1(rws, hbm, ksh[s]); };
  f4 yacc[4];
  u32 a_x, o_y;
  if (!ni) {
    CONV_SETUP();
    const int rho = (15 - r16) & 3;
    u32 a_A = L_HC + ((mi >> 1) * 4 + rho) * HC_STRIDE + (r16 + 1 + rho + 8 * g) * 2 + (mi & 1) * 256;
    u32 a_B = L_U + (mi >> 1) * U_STRIDE + (240 - 16 * r16 + 8 * g) * 2 + (mi & 1) * 256;
    u32 a_u = L_U + ((tid >> 2) & 1) * U_STRIDE + (tid >> 3) * 64 + (tid & 3) * 16;
    KEEP(a_A); KEEP(a_B); KEEP(a_u);
    LATE_SETUP();
    WAITV(0);
    PIN(u0); PIN(u1);
    lw128(a_u, u0); lw128(a_u + 2048, u1);
    w2_read(1); w2_read(2); w2_read(3);
    v4u AD;
    { const u32 dv = (mi & 2) ? ddp >> 16 : ddp & 0xffffu; const int pos = 15 - r16 - 8 * g;
#pragma unroll
      for (int j = 0; j < 4; ++j) AD[j] = (pos >> 1) == j ? dv << ((pos & 1) * 16) : 0u; }
    u32 a_B0 = a_B - (mi & 1) * 256;
    a_x = L_X + w4k + l16;
    o_y = s_cp + WS_YT + (mi >> 1) * 4096 + (mi & 1) * 512 + r16 * 32 + hl * 16 + (g & 1) * ((mi & 1) ? 512u : 1536u);
    KEEP(a_x); KEEP(a_B0); KEEP(o_y);
    LBAR();
    STAMP_AT(5);
    u64 pv2;
    auto conv = [&](auto ROLE) {
      constexpr int role = decltype(ROLE)::value;
      constexpr ConvList CL = make_list(role);
      f4 ac[8];
      v4u fa[4][8], fb[4][8], fd[4];
      auto rd = [&](auto N) {
        constexpr int n = decltype(N)::value, kl = n / 36;
        if constexpr (n < 144) {
          if constexpr (CL.newA[n]) {
            const v2u lo = *(volatile LAS v2u*)lp(a_A + CL.kh[n] * 512 + kl * 64), hi = *(volatile LAS v2u*)lp(a_A + CL.kh[n] * 512 + kl * 64 + 8);
            fa[kl][CL.kh[n]] = v4u{lo.x, lo.y, hi.x, hi.y};
          }
          if constexpr (CL.newB[n]) fb[kl][CL.e[n]] = lr128(a_B + (7 - CL.e[n]) * 512 + kl * 64);
        } else if constexpr (n < 148) fd[n - 144] = lr128(a_B0 + (7 - TS[role][n - 144]) * 512);
      };
      static_for<CLA>(rd);
      static_for<144>([&](auto N) {
        constexpr int n = decltype(N)::value, kl = n / 36, T = CL.kh[n] + CL.e[n];
        ac[T] = mfma(fa[kl][CL.kh[n]], fb[kl][CL.e[n]], n < 8 ? f4{0, 0, 0, 0} : ac[T]);
        rd(std::integral_constant<int, n + CLA>{});
        if constexpr (n >= 9 && n < 9 + 12 * 5 && (n - 9) % 5 == 0) { constexpr int q = (n - 9) / 5; W2_PIECE(4 + q / 4, q % 4); }
        if constexpr (n == 80) LOAD_LAST();
        if constexpr (n == 100) ALD64(pv2, fo2, rws, " sc1");
        if constexpr (n >= 128 && n < 136 && (n & 1) == 0) lwf4(a_x + ((n - 128) / 2) * 1024, ac[TS[role ^ 1][(n - 128) / 2]]);
        if constexpr (n == 136) *(volatile LAS u32*)lp(L_CNT0 + 16 + w * 4) = 1u;
        SB();
      });
#pragma unroll
      for (int j = 0; j < 4; ++j) yacc[j] = mfma(AD, fd[j], ac[TS[role][j]]);
    };
    if (mi & 1) conv(std::integral_constant<int, 1>{}); else conv(std::integral_constant<int, 0>{});
    STAMP_AT(8);
    WAITV(0);
    PIN(pv2);
    wait_flags(rws, fo2, tag, pv2);
  } else {
    LBAR();
    STAMP_AT(5);
    LATE_SETUP();
    WAITV(0);
    if (is_g1) arrive_publish(L_CNT0, rws, l0, WS_FH + (ti * 14 + tj) * FLAG_STRIDE, tag);
    w2_read(1); w2_read(2); w2_read(3);
    WAITL();
    W2_STAGE(4); W2_STAGE(5); W2_STAGE(6);
    LOAD_LAST();
    STAMP_AT(8);
    wait_flags(rws, fo2, tag, ~tag);
  }
  STAMP_AT(9);
#pragma unroll
  for (int s = 0; s < 7; ++s) load_h(0, s);
  SB();
  if (!ni) {
#pragma unroll 1
    for (u32 spins = 0; spins < SPIN_CAP && *(volatile LAS u32*)lp(L_CNT0 + 16 + (w ^ 1) * 4) == 0u; ++spins) __builtin_amdgcn_s_sleep(1);
    asm volatile("" ::: "memory");
#pragma unroll
    for (int j = 0; j < 4; ++j) yacc[j] += lrf4(a_x + ((mi & 1) ? -4096 : 4096) + j * 1024);
#pragma unroll
    for (int j = 0; j < 4; j += 2) {
      const auto r0 = __builtin_amdgcn_permlane16_swap(pk(yacc[j][0], yacc[j][1]), pk(yacc[j + 1][0], yacc[j + 1][1]), false, false);
      const auto r1 = __builtin_amdgcn_permlane16_swap(pk(yacc[j][2], yacc[j][3]), pk(yacc[j + 1][2], yacc[j + 1][3]), false, false);
      if (j == 0) st_sc1<0>(rws, o_y, v4u{r0[0], r1[0], r0[1], r1[1]});
      else st_sc1<2048>(rws, o_y, v4u{r0[0], r1[0], r0[1], r1[1]});
    }
  }
  w2_read(4); w2_read(5); w2_read(6);
  STAMP_AT(10);
  const int tok = tid >> 3, chunk = tid & 7;

  u32 fo3 = WS_FY + (32 * tn + 4 * w + p4) * FLAG_STRIDE, o_yl = WS_YT + (u32)((64 * tn + tok) * 4096 + (64 * tm + 8 * chunk) * 2);
  u32 pp0 = w4k + (((lane & 48) | ((lane + 4 * hl) & 15)) << 4), yp = L_Y + (8 * chunk) * 128 + ((w ^ chunk) << 4) + (tok & 7) * 2;
  u32 rp = (((w >> 1) - mrot) & 3) * L_SLOT + (chunk >> 1) * 1024 + ((32 * (chunk & 1) + ((tok + 4 * (chunk & 3)) & 15)) << 4);
  u32 yr = L_Y + tok * 128 + ((chunk ^ w) << 4);
  KEEP(fo3); KEEP(o_yl); KEEP(pp0); KEEP(yp); KEEP(rp); KEEP(yr);
  PIN(xv); PIN(b2v); PIN(stv);
  float xx[8], xn[8], bb[8];
  v4u yv;
  {
    u64 pv3 = 0;
    f4 acc[2][4];
#pragma unroll
    for (int m = 0; m < 4; ++m) {
#pragma unroll
      for (int s = 0; s < 7; ++s) {
#pragma unroll
        for (int t = 0; t < 4; ++t) acc[m & 1][t] = mfma(w2f[s][t], hf[s], s == 0 ? f4{0, 0, 0, 0} : acc[m & 1][t]);
        if (m < 3) load_h(m + 1, s);
        if (m == 0 && s == 5 && !ni) {
          SB();
          WAITV(6);
          arrive_publish(L_CNT0 + 4, rws, l0, WS_FY + cp * FLAG_STRIDE, tag);
        }
        if (m > 0 && s >= 1 && s <= 4) lwf4(((pp0 + (m - 1) * L_SLOT) ^ (((s - 1) & 1) * 128)) + (s - 1) * 1024, acc[(m - 1) & 1][s - 1]);
        if (m == 2 && s == 0) pv3 = get_flag(rws, fo3);
        if (m == 3 && s == 1) {
          STAMP_F(28);
          wait_flags(rws, fo3, tag, pv3);
          STAMP_F(29);
          yv = ld_sc1(rws, o_yl);
        }
        if (m == 3 && s >= 3) {
          const int j = s - 3;
          xx[2 * j] = bl(xv[j]); xx[2 * j + 1] = bh(xv[j]); bb[2 * j] = bl(b2v[j]); bb[2 * j + 1] = bh(b2v[j]);
          xn[2 * j] = xx[2 * j] * asf(stv.x) + asf(stv.y); KEEP(xn[2 * j]);
          xn[2 * j + 1] = xx[2 * j + 1] * asf(stv.x) + asf(stv.y); KEEP(xn[2 * j + 1]);
          KEEP(xx[2 * j]); KEEP(xx[2 * j + 1]); KEEP(bb[2 * j]); KEEP(bb[2 * j + 1]);
        }
        SB();
      }
      STAMP_F(24 + m);
    }
#pragma unroll 1
    for (u32 spins = 0; spins < SPIN_CAP && *(volatile LAS u32*)lp(L_CNT0 + 4) < 4u; ++spins) __builtin_amdgcn_s_sleep(1);
#pragma unroll
    for (int t = 0; t < 4; ++t) lwf4(((pp0 + 3 * L_SLOT) ^ ((t & 1) * 128)) + t * 1024, acc[1][t]);
  }
  STAMP_AT(11);
#pragma unroll
  for (int j = 0; j < 4; ++j) {
    *(LAS u16*)lp(yp + (2 * j) * 128) = (u16)(yv[j] & 0xffff);
    *(LAS u16*)lp(yp + (2 * j + 1) * 128) = (u16)(yv[j] >> 16);
  }
  LBAR();
  STAMP_AT(12);
  {
    const v4u yy = lr128(yr);
    const f4 o0 = ((lrf4(rp) + lrf4(rp + 4096)) + (lrf4(rp + 8192) + lrf4(rp + 12288))) +
                  ((lrf4(rp + 16384) + lrf4(rp + 20480)) + (lrf4(rp + 24576) + lrf4(rp + 28672)));
    const f4 o1 = ((lrf4(rp + 256) + lrf4(rp + 4352)) + (lrf4(rp + 8448) + lrf4(rp + 12544))) +
                  ((lrf4(rp + 16640) + lrf4(rp + 20736)) + (lrf4(rp + 24832) + lrf4(rp + 28928)));
    float res[8];
#pragma unroll
    for (int j = 0; j < 8; ++j) {
      const float ov = (j < 4 ? o0[j & 3] : o1[j & 3]) + bb[j];
      const float sg = __builtin_amdgcn_rcpf(1.0f + __builtin_amdgcn_exp2f(ov * -1.4426950409f));
      const float yj = (j & 1) ? bh(yy[j >> 1]) : bl(yy[j >> 1]);
      res[j] = xx[j] + ((xn[j] * yj) * ov) * sg;
    }
#define OUT_AUX 16
    __builtin_amdgcn_raw_buffer_store_b128(v4u{pk(res[0], res[1]), pk(res[2], res[3]), pk(res[4], res[5]), pk(res[6], res[7])}, mk_srd(p.out, S * D * 2), o_out, 0, OUT_AUX);
  }
  asm volatile("mk_code_end:");
}
}

extern "C" void kernel_launch(void* const* d_in, const int* in_sizes, int n_in, void* d_out, int out_size, void* d_ws, size_t ws_size,
                              hipStream_t stream) {
  using namespace mk;
  Params p;
  p.x = (const u16*)d_in[0]; p.k = (const u16*)d_in[1]; p.v = (const u16*)d_in[2]; p.sk = (const u16*)d_in[3];
  p.ssm = (const u16*)d_in[4]; p.dd = (const u16*)d_in[5]; p.w1 = (const u16*)d_in[7]; p.b1 = (const u16*)d_in[8];
  p.w2 = (const u16*)d_in[9]; p.b2 = (const u16*)d_in[10];
  p.out = (u16*)d_out; p.ws = (unsigned char*)d_ws;
  (void)hipFuncSetAttribute((const void*)fused, hipFuncAttributeMaxDynamicSharedMemorySize, LDS_BYTES);
  fused<<<256, 512, LDS_BYTES, stream>>>(p);
}
```

```cpp
#include <hip/hip_runtime.h>
#include <stdint.h>
#include <type_traits>
#include <utility>

namespace mk {
typedef unsigned short u16;
typedef unsigned u32;
typedef unsigned long long u64;
typedef u32 v4u __attribute__((ext_vector_type(4)));
typedef u32 v2u __attribute__((ext_vector_type(2)));
typedef float f4 __attribute__((ext_vector_type(4)));
typedef float f2 __attribute__((ext_vector_type(2)));
typedef float f16v __attribute__((ext_vector_type(16)));
typedef short s8v __attribute__((ext_vector_type(8)));
typedef short s4v __attribute__((ext_vector_type(4)));
typedef __bf16 bf2 __attribute__((ext_vector_type(2)));
#define GAS __attribute__((address_space(1)))
#define LAS __attribute__((address_space(3)))
#define CAS __attribute__((address_space(4)))
#define DI __device__ __forceinline__
typedef __amdgpu_buffer_rsrc_t srd_t;
typedef LAS unsigned char* lptr;

constexpr int S = 2048, D = 512, F = 1792;
constexpr float EPS = 1e-5f;
constexpr u32 WS_EPOCH = 0, WS_FKV = 4096, WS_FST = 36864, WS_FH = 40960, WS_FY = 69632, WS_STATS = 131072, WS_CSUM = 196608,
              WS_KVT = 1u << 20, WS_YT = 3u << 20, WS_H = 5u << 20, WS_BYTES = 13u << 20;
constexpr u32 FLAG_STRIDE = 128;
constexpr u32 L_SLOT = 32768;
constexpr u32 L_HC = 98304, HC_STRIDE = 4160, L_U = L_HC + 8 * HC_STRIDE, U_STRIDE = 4608;
constexpr u32 L_PK = L_U + 2 * U_STRIDE, L_PV = L_PK + 72 * 128, L_PW = L_PV + 64 * 128;
constexpr u32 L_JUNK = L_PW + 1024, L_CNT0 = L_JUNK + 256, L_B2 = L_CNT0 + 256, L_TC = L_B2 + 256;
constexpr u32 L_Y = L_PK, L_X = L_PK;
constexpr u32 LDS_BYTES = 163840;
static_assert(L_JUNK + 256 <= LDS_BYTES, "LDS map");
constexpr u32 SPIN_CAP = 1u << 16;

struct Params {
  const u16 *x, *k, *v, *sk, *ssm, *dd, *w1, *b1, *w2, *b2;
  u16* out;
  unsigned char* ws;
};

DI srd_t mk_srd(const void* p, u32 bytes) { return __builtin_amdgcn_make_buffer_rsrc((void*)p, (short)0, (int)bytes, 0x00020000); }
DI float asf(u32 d) { return __builtin_bit_cast(float, d); }
DI u32 asu(float f) { return __builtin_bit_cast(u32, f); }
DI bool all_lanes(bool c) { return __builtin_amdgcn_ballot_w64(!c) == 0; }
DI float bl(u32 d) { return asf(d << 16); }
DI float bh(u32 d) { return asf(d & 0xffff0000u); }
DI u32 pk(float a, float b) { f2 t = {a, b}; return __builtin_bit_cast(u32, __builtin_convertvector(t, bf2)); }
DI float rest(float v) { return v - bh(asu(v)); }
DI u32 pkt(float lo, float hi) { return __builtin_amdgcn_perm(asu(hi), asu(lo), 0x07060302u); }
DI f4 mfma(v4u a, v4u b, f4 c) { return __builtin_amdgcn_mfma_f32_16x16x32_bf16(__builtin_bit_cast(s8v, a), __builtin_bit_cast(s8v, b), c, 0, 0, 0); }
DI f16v mfma32(v4u a, v4u b, f16v c) { return __builtin_amdgcn_mfma_f32_32x32x16_bf16(__builtin_bit_cast(s8v, a), __builtin_bit_cast(s8v, b), c, 0, 0, 0); }
DI lptr lp(u32 a) { return (lptr)(uintptr_t)a; }
DI v2u trread(u32 a) { return __builtin_bit_cast(v2u, __builtin_amdgcn_ds_read_tr16_b64_v4i16((LAS s4v*)lp(a))); }
DI v4u trfrag(u32 a, u32 hi_off) { const v2u lo = trread(a), hi = trread(a + hi_off); return v4u{lo.x, lo.y, hi.x, hi.y}; }
DI v4u lr128(u32 a) { return *(LAS v4u*)lp(a); }
DI f4 lrf4(u32 a) { return *(LAS f4*)lp(a); }
DI void lw128(u32 a, v4u v) { *(LAS v4u*)lp(a) = v; }
DI void lwf4(u32 a, f4 v) { *(LAS f4*)lp(a) = v; }
#define DMA_(OP, FL, rs, voff, sb, SI, lb, LI) do { u32 t_; \
  asm volatile("s_add_u32 m0, %1, %2\n\ts_add_u32 %0, %3, %4\n\t" OP " %5, %6, %0 offen" FL " lds" \
               : "=&s"(t_) : "s"(lb), "i"(LI), "s"(sb), "i"(SI), "v"(voff), "s"(rs) : "memory", "scc"); } while (0)
#define DFL ""
#define DMA(rs, voff, sb, SI, lb, LI) DMA_("buffer_load_dwordx4", DFL, rs, voff, sb, SI, lb, LI)
#define SSM_FL " nt"
#define DMANT(rs, voff, sb, SI, lb, LI) DMA_("buffer_load_dwordx4", " nt", rs, voff, sb, SI, lb, LI)
#define M0SET(lb, LI) asm volatile("s_add_u32 m0, %0, %1\n\ts_nop 0" ::"s"(lb), "i"(LI) : "memory", "scc")
#define DML(rs, voff, so, OFF) asm volatile("buffer_load_dwordx4 %0, %1, %2 offen offset:%3 lds" ::"v"(voff), "s"(rs), "s"(so), "i"(OFF) : "memory")
#define TOUCH(rs, voff, sb, SI) DMA_("buffer_load_dword", "", rs, voff, sb, SI, 0u, L_JUNK)
#define ALD128(d, off, rs, FL) asm volatile("buffer_load_dwordx4 %0, %1, %2, 0 offen" FL : "=v"(d) : "v"(off), "s"(rs) : "memory")
#define ALD128S(d, off, rs, so, FL) asm volatile("buffer_load_dwordx4 %0, %1, %2, %3 offen" FL : "=v"(d) : "v"(off), "s"(rs), "s"(so) : "memory")
#define ALD64(d, off, rs, FL) asm volatile("buffer_load_dwordx2 %0, %1, %2, 0 offen" FL : "=v"(d) : "v"(off), "s"(rs) : "memory")
#define ALD32(d, off, rs, FL) asm volatile("buffer_load_dword %0, %1, %2, 0 offen" FL : "=v"(d) : "v"(off), "s"(rs) : "memory")
#define ALD16(d, off, rs, FL) asm volatile("buffer_load_ushort %0, %1, %2, 0 offen" FL : "=v"(d) : "v"(off), "s"(rs) : "memory")
#define WAITV(n) asm volatile("s_waitcnt vmcnt(" #n ")" ::: "memory")
#define PIN(a) asm volatile("" : "+v"(a)::"memory")
#define KEEP(a) asm volatile("" : "+v"(a))
#define KEEPP(a) asm volatile("" : "+s"(a))
#define KEEPS(a) do { a = __builtin_amdgcn_readfirstlane(a); asm volatile("" : "+s"(a)); } while (0)
#define WAITL() asm volatile("s_waitcnt lgkmcnt(0)" ::: "memory")
#define BAR() __builtin_amdgcn_s_barrier()
#define VBAR(n) do { __builtin_amdgcn_s_waitcnt(0xC07F); asm volatile("s_waitcnt vmcnt(" #n ") lgkmcnt(0)\n\ts_barrier" ::: "memory"); } while (0)
#define LBAR() do { __builtin_amdgcn_s_waitcnt(0xC07F); asm volatile("s_waitcnt lgkmcnt(0)\n\ts_barrier" ::: "memory"); } while (0)
#define SB() __builtin_amdgcn_sched_barrier(0)
DI void wave_sum2(float& a, float& b) {
  const auto x = __builtin_amdgcn_permlane32_swap(asu(a), asu(b), false, false);
  float v = asf(x[0]) + asf(x[1]);
  const auto y = __builtin_amdgcn_permlane16_swap(asu(v), asu(v), false, false);
  v = asf(y[0]) + asf(y[1]);
  v += asf(__builtin_amdgcn_update_dpp(0u, asu(v), 0xB1, 0xF, 0xF, true));
  v += asf(__builtin_amdgcn_update_dpp(0u, asu(v), 0x4E, 0xF, 0xF, true));
  v += asf(__builtin_amdgcn_update_dpp(0u, asu(v), 0x141, 0xF, 0xF, true));
  v += asf(__builtin_amdgcn_update_dpp(0u, asu(v), 0x140, 0xF, 0xF, true));
  const auto z = __builtin_amdgcn_permlane32_swap(asu(v), asu(v), false, false);
  a = asf(z[0]); b = asf(z[1]);
}
DI v4u ld_sc1(srd_t rs, u32 off, u32 soff = 0) { return __builtin_amdgcn_raw_buffer_load_b128(rs, off, soff, 16); }
template <int IMM = 0> DI void st_sc1(srd_t rs, u32 off, v4u v, u32 soff = 0) {
  asm volatile("buffer_store_dwordx4 %0, %1, %2, %3 offen offset:%4 sc1\n\ts_nop 1" ::"v"(v), "v"(off), "s"(rs), "s"(soff), "i"(IMM) : "memory");
}
DI void put_flag(srd_t rs, u32 l0, u32 off, u64 tag) { __builtin_amdgcn_raw_buffer_store_b64(__builtin_bit_cast(v2u, tag), rs, l0, off, 16); }
DI u32 get_flag(srd_t rs, u32 off) { return __builtin_amdgcn_raw_buffer_load_b32(rs, off, 0, 16); }
DI void wait_flags(srd_t rs, u32 off, u32 tagv, u32 v) {
  if (!all_lanes(v == tagv)) {
#pragma unroll 1
    for (u32 spins = 0; spins < SPIN_CAP; ++spins) {
      u32 x;
      asm volatile("buffer_load_dword %0, %1, %2, 0 offen sc1\n\ts_waitcnt vmcnt(0)" : "=&v"(x) : "v"(off), "s"(rs) : "memory");
      if (all_lanes(x == tagv)) break;
      __builtin_amdgcn_s_sleep(1);
    }
  }
  asm volatile("" ::: "memory");
}
DI void arrive_publish(u32 cnt, srd_t rs, u32 l0, u32 off, u64 tag) {
  u32 old;
  asm volatile("s_mov_b64 exec, 1\n\tds_add_rtn_u32 %0, %1, %2\n\ts_mov_b64 exec, -1\n\ts_waitcnt lgkmcnt(0)" : "=&v"(old) : "v"(cnt), "v"(1u) : "memory");
  if (__builtin_amdgcn_readfirstlane(old) == 3u) put_flag(rs, l0, off, tag);
}
#define STAMP_AT(i)
#define STAMP_F(i)
struct ConvList { int kh[144], e[144]; bool newA[144], newB[144]; };
constexpr int KEEP_MASK[2] = {0x99, 0x66};
constexpr int TS[2][4] = {{0, 3, 4, 7}, {1, 2, 5, 6}};
constexpr ConvList make_list(int role) {
  ConvList L = {};
  int n = 0;
  for (int kl = 0; kl < 4; ++kl) {
    bool seen[16] = {};
    for (int part = 0; part < (kl == 3 ? 2 : 1); ++part) {
      const int mask = kl < 3 ? 0xff : KEEP_MASK[part ? role : role ^ 1];
      for (int kh = 0; kh < 8; ++kh)
        for (int T = 7; T >= kh; --T) {
          if (!((mask >> T) & 1)) continue;
          const int e = T - kh;
          L.kh[n] = kh; L.e[n] = e; L.newA[n] = !seen[kh]; L.newB[n] = !seen[8 + e];
          seen[kh] = seen[8 + e] = true;
          ++n;
        }
    }
  }
  return L;
}
template <class F, int... I> DI void static_for_(F&& f, std::integer_sequence<int, I...>) { (f(std::integral_constant<int, I>{}), ...); }
template <int N, class F> DI void static_for(F&& f) { static_for_(f, std::make_integer_sequence<int, N>{}); }
#define CLA 10
#define NPF 0
#define ZX 1
#define ZW1 1
#define ZW2 1

__global__ void __launch_bounds__(512, 2) fused(Params p) {
  const int tid = __builtin_amdgcn_workitem_id_x(), lane = tid & 63, w = __builtin_amdgcn_readfirstlane(tid >> 6);
  const int b = __builtin_amdgcn_workgroup_id_x(), xcd = b & 7, idx = b >> 3;
  const int g = lane >> 4, r16 = lane & 15, q4 = r16 >> 2, p4 = lane & 3, rr = lane >> 3;
  STAMP_AT(0);
  if (w == 0) {
    u64 pc; u32 len;
    asm volatile("mk_code_begin:\n\ts_getpc_b64 %0\n\ts_mov_b32 %1, mk_code_end-mk_code_begin\n\ts_max_i32 %1, %1, mk_code_end2-mk_code_begin" : "=s"(pc), "=s"(len) :: "scc");
    const srd_t rc = mk_srd((const void*)pc, len);
    TOUCH(rc, (u32)(lane * 128), (u32)((idx & 3) * 8192), 0);
  }
  SB();
  KEEPP(p.ws); KEEPP(p.out); KEEPP(p.w2); KEEPP(p.b2); KEEPP(p.k);
  const int cp = 32 * xcd + idx;
  const bool is_g1 = idx < 28;
  const srd_t rx = mk_srd(p.x, S * D * 2 * ZX), rw1 = mk_srd(p.w1 - 1024, D * F * 2 * ZW1 + 2048);
  const u32 w1k = w * 1024, w4k = w * 4096;
  v4u xr;
  const int stok = 256 * xcd + 8 * idx + w;
  ALD128(xr, (u32)(stok * 1024 + lane * 16), rx, "");
  SB();
  const int sidx = idx & 3, ti = 4 * (xcd >> 1) + sidx, t1 = idx >> 2, tj = 7 * (xcd & 1) + t1;
  const int hm = 4 * (xcd >> 1) + sidx, cgp = 14 * (xcd & 1) + (hm < 14 ? hm : 0);
  const u32 chsw = (u32)(((lane & 7) ^ (rr & 6)) << 4);
  const u32 xvo = (u32)((128 * ti + 16 * w + rr) * 1024 + (((lane & 7) ^ g) << 4)), xvo1 = (xvo ^ 64) + 7168;
  const u32 wvo = (u32)((8 * w + (is_g1 ? g : rr)) * 3584 + (is_g1 ? tj * 256 : cgp * 128)) + (is_g1 ? (u32)((r16 ^ (g << 2)) << 4) : chsw), wvo1 = wvo + 13312;
#define ROT1 ((4 * ((t1 + sidx) & 1) + xcd) & 7)
  u32 sox = (u32)(ROT1) * 128, sow = sox * 1792;
#define NEXT_CHUNK() do { sox = (sox + 128) & 1023; sow = sox * 1792; } while (0)
#define PIECE(c, i) do { \
    if ((i) == 0) { M0SET(w4k, ((c) % 3) * L_SLOT); DML(rx, xvo, sox, 0); } \
    if ((i) == 1) DML(rx, xvo1, sox, 1024); \
    if ((i) == 2) DML(rw1, wvo, sow, 2048); \
    if ((i) == 3) { DML(rw1, wvo1, sow, 3072); NEXT_CHUNK(); } } while (0)
#define HPIECE(c) DMA(rw1, wvo, 0u, (c) * 64 * 3584 + 2048, w1k, (c) * 8192)
  if (is_g1) { PIECE(0, 0); PIECE(0, 1); PIECE(0, 2); PIECE(0, 3); }
  else { HPIECE(0); HPIECE(1); HPIECE(2); HPIECE(3); }
  SB();

  u64 tag = *(const CAS u64*)(p.ws + WS_EPOCH);
  u32 ddp = *(const CAS u32*)(p.dd + 2 * cp);
  v2u hq = *(const CAS v2u*)(p.ssm + cp * 4096 + ((w & 3) ? w * 512 - 4 : 0));
  SB();
  const srd_t rws = mk_srd(p.ws, WS_BYTES);
  const int tm = 4 * xcd + sidx;
  const int tn = (t1 + 4 - (int)(((uintptr_t)p.w2 >> 7) & 7)) & 7;
  const int mi = w & 3, ni = w >> 2, hl = lane >> 5, l31 = lane & 31;
  u32 l0 = lane == 0 ? 0u : 0x80000000u, l01 = lane < 2 ? lane * 16 : 0x80000000u;
  u32 s_st = WS_STATS + (u32)stok * 32;
  const int m2 = w & 1, nq = w >> 1;
  u32 xa[2][4], wa[2];
#pragma unroll
  for (int ks = 0; ks < 4; ++ks) { xa[0][ks] = (4 * m2 + (g & 1)) * 4096 + r16 * 128 + (((2 * ks + hl) ^ (r16 >> 1)) << 4); xa[1][ks] = xa[0][ks] + 2 * L_SLOT; }
  wa[0] = 2048 + hl * 4096 + q4 * 256 + (((4 * nq + 2 * (g & 1) + (p4 >> 1)) ^ (q4 << 2)) << 4) + 8 * (p4 & 1);
  wa[1] = wa[0] + 2 * L_SLOT;
  u32 kvo = (u32)(rr * 1024 + (((lane & 7) ^ (rr & 2)) << 4)), kso = (u32)((64 * idx + 8 * w) * 1024 + xcd * 128);
  KEEP(l0); KEEP(l01); KEEPS(s_st); KEEP(kvo); KEEPS(kso);
#pragma unroll
  for (int i = 0; i < 4; ++i) { KEEP(xa[0][i]); KEEP(xa[1][i]); }
  KEEP(wa[0]); KEEP(wa[1]);
#define STAGE_K() do { const srd_t rk = mk_srd(p.k, S * D * 2); DMANT(rk, kvo, kso, 0, w1k, L_PK + 1024); \
    if (w == 0) DMANT(rk, kvo + kso - 8192u, 0u, 0, 0u, L_PK); } while (0)
#define STAGE_V() do { const srd_t rv = mk_srd(p.v, S * D * 2); DMANT(rv, kvo, kso, 0, w1k, L_PV); \
    if (w == 1) { const srd_t rsk = mk_srd(p.sk, D * 4 * 2); DMA(rsk, (u32)(lane * 16), (u32)(xcd * 512), 0, 0u, L_PW); } \
    if (w == 2) { const srd_t rb2 = mk_srd(p.b2, D * 2); DMA_("buffer_load_dword", "", rb2, (u32)(lane * 4), (u32)(tn * 128), 0, 0u, L_B2); } } while (0)
  constexpr u32 TR_HI = 1024;
  const srd_t rw2 = mk_srd(p.w2, F * D * 2 * ZW2), rssm = mk_srd(p.ssm + cp * 4096, 8192);
  u32 a_kv, a_tap, o_kv, s_kv, w2vo[4], w2base, ksh[7], s_t0, ta[4], t16, a_hc, fo1, fo2, o_xe, o_we, o_out, o_h, s_h, hb, s_cp, tagv;
  v2u ck[3], cv[2], ctw;
  float cw[4], kk[12], vv[8], co[8];
  const u32 l16 = lane * 16;
  auto fill = [&](auto KK) {
    constexpr int K = decltype(KK)::value;
    if constexpr (K == 200) { s_t0 = (u32)((224 * t1 + 8 * sidx) * 1024) + (ksh[0] << 5); KEEPS(s_t0); }
    if constexpr (K == 201) { a_kv = L_PK + 512 + ni * 4096 + (8 * g + q4) * 128 + (((2 * mi + (p4 >> 1)) ^ (q4 & 2)) << 4) + 8 * (p4 & 1); KEEP(a_kv); }
    if constexpr (K == 202) { a_tap = L_PW + mi * 128 + r16 * 8; KEEP(a_tap);
#pragma unroll
      for (int i = 0; i < 4; ++i) { w2vo[i] = (u32)(rr * 1024 + i * 7168) + chsw; KEEP(w2vo[i]); } }
    if constexpr (K == 203) { o_kv = (u32)((r16 >> 1) * 8192 + (r16 & 1) * 64 + (3 - g) * 16); KEEP(o_kv); }
    if constexpr (K == 204) { s_kv = WS_KVT + (u32)((32 * xcd + 8 * mi) * 8192 + (63 - 2 * idx - ni) * 128); KEEPS(s_kv); }
    if constexpr (K == 205) {
      if (w == 2) lw128(L_U + hl * U_STRIDE + 4096 + l31 * 16, v4u{0, 0, 0, 0});
      if (w == 3) lw128(L_CNT0 + (lane & 1) * 16, v4u{0, 0, 0, 0});
    }
    if constexpr (K == 9) { const int ia = 8 * sidx * 8 + xcd, ib = (16 * (ti & 1) + 8 * m2) * 8 + (ti >> 1);
                            fo1 = ((lane & (is_g1 ? 8 : 0)) ? ib : ia) + 8 * (lane & 7); KEEP(fo1); }
    if constexpr (K == 13) { const u32 fkv = (31 - 4 * mi - p4 - 4 * (lane & 4)) * 8 + xcd; fo1 = (lane & 16) ? fkv : fo1; KEEP(fo1); }
    if constexpr (K == 17) { const int m = 2 * t1 + ni; const u32 fc = (WS_FST - WS_FKV) / FLAG_STRIDE + ((m >> 2) * 2 + (xcd & 1)) * 4 + (m & 3);
                             fo1 = (lane & 24) == (is_g1 ? 24 : 64) ? fc : fo1; fo1 = WS_FKV + fo1 * FLAG_STRIDE + 4 * hl; KEEP(fo1); }
    if constexpr (K == 21) { tagv = hl ? (u32)(tag >> 32) : (u32)tag; KEEP(tagv); }
    if constexpr (K == 29) { t16 = tid * 16; o_out = (u32)((64 * tm + 8 * w) * 1024 + 128 * tn) + rr * 1024 + (lane & 7) * 16; KEEP(t16); KEEP(o_out); }
    if constexpr (K == 33) { o_xe = WS_STATS + (u32)((128 * ti + 64 * m2) * 32) + lane * 32; o_we = WS_CSUM + (u32)((128 * tj + 32 * nq) * 16) + l31 * 16; KEEP(o_xe); KEEP(o_we); }
    if constexpr (K == 37) { const int trow = 16 * hl + 4 * (g & 1) + q4; ta[0] = L_SLOT + w4k + trow * 128 + (((p4 >> 1) ^ (trow & 6)) << 4) + 8 * (p4 & 1); KEEP(ta[0]); }
    if constexpr (K == 41) { ta[1] = ta[0] ^ 32; ta[2] = ta[0] ^ 64; ta[3] = ta[0] ^ 96; KEEP(ta[1]); KEEP(ta[2]); KEEP(ta[3]);
                             a_hc = L_HC + ni * (4 * HC_STRIDE - 4096) + 32 + t16; KEEP(a_hc); }
    if constexpr (K == 45) { o_h = (u32)((g & 1) * 57344 + r16 * 16 + hl * 256); KEEP(o_h); }
    if constexpr (K == 49) { s_h = WS_H + (u32)(((8 * ti + 4 * m2) * 56 + 4 * tj + nq) * 1024); KEEPS(s_h); }
    if constexpr (K == 100) {
      ctw = *(LAS v2u*)lp(a_tap);
      ck[0] = trread(a_kv); ck[1] = trread(a_kv + 512); ck[2] = trread(a_kv + 1024);
      cv[0] = trread(a_kv + (L_PV - L_PK - 512)); cv[1] = trread(a_kv + (L_PV - L_PK));
    }
    if constexpr (K == 102) {
      cw[0] = bl(ctw.x); cw[1] = bh(ctw.x); cw[2] = bl(ctw.y); cw[3] = bh(ctw.y);
      kk[1] = bh(ck[0].x); kk[2] = bl(ck[0].y); kk[3] = bh(ck[0].y);
#pragma unroll
      for (int i = 0; i < 4; ++i) KEEP(cw[i]);
#pragma unroll
      for (int i = 1; i < 4; ++i) KEEP(kk[i]);
    }
    if constexpr (K >= 103 && K < 111) {
      constexpr int j = K - 103;
      const u32 kd = ck[1 + j / 4][(j / 2) & 1], vd = cv[j / 4][(j / 2) & 1];
      kk[j + 4] = (j & 1) ? bh(kd) : bl(kd); vv[j] = (j & 1) ? bh(vd) : bl(vd);
      asm volatile("v_mul_f32 %0, %1, %5\n\tv_fmac_f32 %0, %2, %6\n\tv_fmac_f32 %0, %3, %7\n\tv_fmac_f32 %0, %4, %8\n\tv_mul_f32 %0, %0, %9"
                   : "=&v"(co[j]) : "v"(kk[j + 1]), "v"(kk[j + 2]), "v"(kk[j + 3]), "v"(kk[j + 4]), "v"(cw[3]), "v"(cw[2]), "v"(cw[1]), "v"(cw[0]), "v"(vv[j]));
    }
    if constexpr (K == 111) st_sc1(rws, o_kv, v4u{pk(co[7], co[6]), pk(co[5], co[4]), pk(co[3], co[2]), pk(co[1], co[0])}, s_kv);
  };
  SB();
  BAR();
  STAGE_K(); STAGE_V();
  { int t = xcd;
    KEEPS(t);
#pragma unroll
    for (int s = 0; s < 7; ++s) { t = t >= 7 ? t - 7 : t; ksh[s] = (u32)t * 1024; KEEPS(ksh[s]); ++t; } }
  w2base = (u32)(224 * w * 1024 + tn * 128);
  s_cp = (u32)cp * 8192;
  KEEPS(w2base); KEEPS(s_cp);
  static_for<6>([&](auto I) { fill(std::integral_constant<int, 200 + decltype(I)::value>{}); });
  SB();
  WAITV(6);
  PIN(xr);
  STAMP_AT(1);
  {
    float s1 = 0.f, s2 = 0.f;
    u32 one2 = 0x3f803f80u;
    KEEP(one2);
#pragma unroll
    for (int j = 0; j < 4; ++j) {
      const u32 xj = xr[j];
      const bf2 v = __builtin_bit_cast(bf2, xj);
      s1 = __builtin_amdgcn_fdot2_f32_bf16(v, __builtin_bit_cast(bf2, one2), s1, false);
      s2 = __builtin_amdgcn_fdot2_f32_bf16(v, v, s2, false);
    }
    wave_sum2(s1, s2);
    const float mean = s1 * (1.0f / D), var = __builtin_fmaxf(s2 * (1.0f / D) - mean * mean, 0.f);
    const float iA = __builtin_amdgcn_sqrtf(var + EPS * (var + EPS));
    const float m0 = -mean, m1 = rest(m0), m2 = rest(m1);
    u32 d0 = pkt(m0, m0), d1 = pkt(m1, m1);
    const u32 mv = pk(mean, var);
    float rq = __builtin_amdgcn_rcpf(bl(pk(__builtin_amdgcn_sqrtf(bl(pk(bh(mv) + EPS, 0.f))), 0.f))), mq = -bl(mv) * rq;
    KEEP(d0); KEEP(d1); KEEP(rq); KEEP(mq);
    const bool l1 = lane & 1;
    st_sc1(rws, l01, v4u{l1 ? asu(rq) : d0, l1 ? asu(mq) : d1, pkt(m2, m0), pkt(iA, rest(iA))}, s_st);
  }
  tag += 0x9E3779B97F4A7C15ull;
  SB();
  if (is_g1) { PIECE(1, 0); PIECE(1, 1); PIECE(1, 2); PIECE(1, 3); }
  else { HPIECE(4); HPIECE(5); HPIECE(6); HPIECE(7); }
  v4u hd;
  ALD128(hd, (u32)(tid * 16), rssm, SSM_FL);
  if (w == 6) TOUCH(rw2, (u32)(lane * 128), s_t0, 0);
#define MAKE_KV() static_for<12>([&](auto I) { fill(std::integral_constant<int, 100 + decltype(I)::value>{}); })
  SB();
  STAMP_AT(2);
  v4u w2f[7][4];
  u32 sw2;
#define W2SLOT(s) ((s) == 0 ? 3 : ((s) + 1) % 3)
#define W2_PIECE(s, i) do { if ((i) == 0) { sw2 = w2base + (ksh[s] << 5); M0SET(w4k, W2SLOT(s) * L_SLOT); } DML(rw2, w2vo[i], sw2, (i) * 1024); } while (0)
#define W2_STAGE(s) do { W2_PIECE(s, 0); W2_PIECE(s, 1); W2_PIECE(s, 2); W2_PIECE(s, 3); } while (0)
  v4u xv; v2u stv;
  v4u u0, u1;
#define LOAD_U() do { ALD128S(u0, t16, rws, s_cp + WS_KVT, " sc1"); ALD128S(u1, t16, rws, s_cp + WS_KVT + 4096, " sc1"); } while (0)
#define LOAD_LAST() ALD128(xv, o_out, rx, "")
#define LOAD_TC() ALD64(stv, WS_STATS + 16 + (64 * tm + lane) * 32, rws, " sc1")
  auto w2_read = [&](int s) {
#pragma unroll
    for (int t = 0; t < 4; ++t) w2f[s][t] = trfrag(ta[t] + W2SLOT(s) * L_SLOT - L_SLOT, TR_HI);
  };
  auto write_hc = [&](int rho) {
    const u32 p0 = __builtin_amdgcn_update_dpp(mi ? hq.x : 0u, hd.z, 0x138, 0xF, 0xF, false), p1 = __builtin_amdgcn_update_dpp(mi ? hq.y : 0u, hd.w, 0x138, 0xF, 0xF, false);
    const u32 a0 = __builtin_amdgcn_alignbit(p1, p0, 16), a1 = __builtin_amdgcn_alignbit(hd.x, p1, 16), a2 = __builtin_amdgcn_alignbit(hd.y, hd.x, 16),
              a3 = __builtin_amdgcn_alignbit(hd.z, hd.y, 16), a4 = __builtin_amdgcn_alignbit(hd.w, hd.z, 16);
    if (rho == 0) lw128(a_hc, hd);
    if (rho == 1) lw128(a_hc + HC_STRIDE, v4u{a1, a2, a3, a4});
    if (rho == 2) lw128(a_hc + 2 * HC_STRIDE, v4u{p1, hd.x, hd.y, hd.z});
    if (rho == 3) lw128(a_hc + 3 * HC_STRIDE, v4u{a0, a1, a2, a3});
  };

#define CONV_SETUP() do { PIN(hd); write_hc(0); write_hc(1); write_hc(2); write_hc(3); \
    if (w == 4) lw128(L_HC + rr * HC_STRIDE + (lane & 1) * 16, v4u{0, 0, 0, 0}); } while (0)

  if (__builtin_expect(is_g1, 1)) {
    f16v acc[2];
#pragma unroll
    for (int i = 0; i < 16; ++i) { acc[0][i] = 0.f; acc[1][i] = 0.f; }
    v4u xf[2][2][2], wf[2][2];
#define REQ(dc, i) do { if ((dc) >= 8) W2_PIECE((dc) & 3, i); else PIECE(dc, i); } while (0)
#define STEP(st, dc, d0, nd) do { \
      constexpr int hs_ = (st) & 1, hn_ = hs_ ^ 1, sl_ = (((st) + 1) >> 1) % 3, sb_ = sl_ >> 1; constexpr u32 so_ = (sl_ & 1) * L_SLOT; \
      constexpr bool rd_ = (st) < 15, mm_ = (st) >= 0; \
      _Pragma("unroll") for (int i_ = 0; i_ < 4; ++i_) { \
        if (mm_) acc[i_ & 1] = mfma32(wf[hs_][i_ >> 1], xf[hs_][i_ >> 1][i_ & 1], acc[i_ & 1]); \
        if (rd_) { \
          if (!(i_ & 1)) wf[hn_][i_ >> 1] = trfrag(wa[sb_] + so_ + (2 * hn_ + (i_ >> 1)) * 8192, 1024); \
          else { xf[hn_][i_ >> 1][0] = lr128(xa[sb_][2 * hn_ + (i_ >> 1)] + so_); xf[hn_][i_ >> 1][1] = lr128(xa[sb_][2 * hn_ + (i_ >> 1)] + so_ + 8192); } } \
        if ((nd) == 4) { if (i_ == 0) REQ(dc, 0); if (i_ == 1) REQ(dc, 1); if (i_ == 2) REQ(dc, 2); if (i_ == 3) REQ(dc, 3); } \
        if ((nd) == 2) { if (i_ == 0) REQ(dc, d0); if (i_ == 2) REQ(dc, (d0) + 1); } \
        if (mm_) { if (i_ == 0) fill(std::integral_constant<int, 4 * (st)>{}); if (i_ == 1) fill(std::integral_constant<int, 4 * (st) + 1>{}); \
                   if (i_ == 2) fill(std::integral_constant<int, 4 * (st) + 2>{}); if (i_ == 3) fill(std::integral_constant<int, 4 * (st) + 3>{}); } \
        SB(); } } while (0)
#define ITER(c) do { STAMP_F(16 + (c)); STEP(2 * (c) + 1, (c) + 3, 0, 2); STEP(2 * (c) + 2, (c) + 3, 2, 2); } while (0)
    u32 pv; v4u xe, we;
    VBAR(8);
    STAMP_AT(15);
    STEP(-1, 0, 0, 0);
    STEP(0, 2, 0, 4);
    VBAR(10);
    STAMP_F(30);
    MAKE_KV();
    SB();
    VBAR(6);
    ITER(0);
    VBAR(5);
    ITER(1); W2_PIECE(0, 0); W2_PIECE(0, 1);
    VBAR(6);
    if (w == 0) put_flag(rws, l0, WS_FKV + b * FLAG_STRIDE, tag);
    ITER(2); M0SET(w4k, 3 * L_SLOT); W2_PIECE(0, 2); W2_PIECE(0, 3);
    VBAR(8);
    ITER(3);
    VBAR(6);
    ITER(4);
    ALD32(pv, fo1, rws, " sc1");
    VBAR(5);
    STAMP_F(21);
    STEP(11, 9, 0, 2); STEP(12, 9, 2, 2);
    VBAR(4);
    STAMP_F(22);
    PIN(pv);
    wait_flags(rws, fo1, tagv, pv);
    ALD128(xe, o_xe, rws, " sc1");
    ALD128(we, o_we, rws, " sc1");
    if (!ni) LOAD_U();
    if (w == 3) LOAD_TC();
    STEP(13, 0, 0, 0); w2_read(0); STEP(14, 10, 0, 2);
    LBAR();
    STAMP_F(23);
    STEP(15, 10, 2, 2);
    if (ni) WAITV(4); else WAITV(6);
    PIN(xe); PIN(we);
    {
      const v4u z = {0, 0, 0, 0};
      acc[0] = mfma32(we, hl ? z : xe, acc[0]);
      acc[1] = mfma32(we, hl ? xe : z, acc[1]);
    }
    STAMP_AT(3);
    if (ni) { CONV_SETUP(); SB(); }
    f2 k1v[2], k3v[2], iAv[2];
    { const auto r = __builtin_amdgcn_permlane32_swap(xe.w, xe.w, false, false);
#pragma unroll
      for (int m = 0; m < 2; ++m) {
        const float iA = bl(r[m]) + bh(r[m]), A = __builtin_amdgcn_rcpf(iA), k1s = -2.3022082f * A, k3s = -0.10294324f * A * A * A;
        k1v[m] = f2{k1s, k1s}; k3v[m] = f2{k3s, k3s}; iAv[m] = f2{iA, iA};
      } }
#pragma unroll
    for (int qi = 0; qi < 4; ++qi) {
      const int n = qi >> 1, sp = qi & 1;
      if (qi == 0) W2_PIECE(3, 0);
      if (qi == 1) W2_PIECE(3, 1);
      if (qi == 2) W2_PIECE(3, 2);
      if (qi == 3) W2_PIECE(3, 3);
      v4u o;
#pragma unroll
      for (int j = 0; j < 4; ++j) {
        const f2 a = {acc[n][8 * sp + 2 * j], acc[n][8 * sp + 2 * j + 1]}, k1 = k1v[n], k3 = k3v[n], iA = iAv[n];
        const f2 z = a * __builtin_elementwise_fma(a * a, k3, k1);
        const f2 e = {__builtin_amdgcn_exp2f(z.x), __builtin_amdgcn_exp2f(z.y)};
        const f2 d = __builtin_elementwise_fma(e, iA, iA);
        const f2 hv = a * f2{__builtin_amdgcn_rcpf(d.x), __builtin_amdgcn_rcpf(d.y)};
        o[j] = pk(hv.x, hv.y);
      }
      if (qi == 0) st_sc1<0>(rws, o_h, o, s_h);
      if (qi == 1) st_sc1<512>(rws, o_h, o, s_h);
      if (qi == 2) st_sc1<0>(rws, o_h, o, s_h + 114688);
      if (qi == 3) st_sc1<512>(rws, o_h, o, s_h + 114688);
      SB();
    }
    STAMP_AT(4);
  } else {
    u32 b1v;
    { const srd_t rb1 = mk_srd(p.b1 + 64 * cgp, 128); ALD16(b1v, (u32)(16 * mi + 4 * g + p4) * 2, rb1, ""); }
    static_for<45>(fill);
    VBAR(0);
    STAMP_AT(15);
    if (w < 4) {
      const v4u ones = {0x3f803f80u, 0x3f803f80u, 0x3f803f80u, 0x3f803f80u};
      f4 s0 = {0, 0, 0, 0};
      const u32 a = (w == 0 ? ta[0] : w == 1 ? ta[1] : w == 2 ? ta[2] : ta[3]) - L_SLOT - w4k;
#pragma unroll
      for (int st = 0; st < 16; ++st) s0 = mfma(trfrag(a + st * 4096, TR_HI), ones, s0);
      const float ca = (p4 & 1) ? s0[1] : s0[0], cb = (p4 & 1) ? s0[3] : s0[2], c = (p4 & 2) ? cb : ca;
      const float c1 = rest(c), c2 = rest(c1);
      PIN(b1v);
      st_sc1(rws, r16 < 4 && hm < 14 ? WS_CSUM + (u32)(64 * cgp + 16 * w + 4 * g + p4) * 16 : 0x80000000u, v4u{pkt(c, c1), pkt(c, c1), pkt(c, c2), b1v * 0x10001u});
    }
    MAKE_KV();
    VBAR(0);
    if (w == 0) { put_flag(rws, l0, WS_FST + (4 * xcd + sidx) * FLAG_STRIDE, tag); put_flag(rws, l0, WS_FKV + b * FLAG_STRIDE, tag); }
    STAMP_AT(3);
#pragma unroll
    for (int i = 1; i <= NPF; ++i) { u32 kk = ksh[i]; KEEPS(kk); TOUCH(rw2, (u32)(lane * 128), (u32)((224 * w + 8 * sidx) * 1024) + (kk << 5), 0); }
    W2_STAGE(0); W2_STAGE(1); W2_STAGE(2); W2_STAGE(3);
    if (b == 255) {
      wait_flags(rws, WS_FKV + (32 * w + l31) * FLAG_STRIDE + 4 * hl, tagv, ~tagv);
      BAR();
      if (w == 0) put_flag(rws, l0, WS_EPOCH, tag);
    }
    wait_flags(rws, fo1, tagv, ~tagv);
    w2_read(0);
    STAMP_AT(4);
    LBAR();
    asm volatile("mk_code_end2:");
    if (ni) CONV_SETUP(); else LOAD_U();
    if (w == 3) LOAD_TC();
  }
#define LATE_SETUP() do { const int tja = ((224 * w) >> 7) + p4, tjb = (224 * w + 223) >> 7; \
    fo2 = WS_FH + ((tm >> 1) * 14 + (tja < tjb ? tja : tjb)) * FLAG_STRIDE + 4 * hl; hb = WS_H + (u32)((4 * tm * 56 + 7 * w) * 1024) + l16; KEEP(fo2); KEEP(hb); } while (0)
  v4u hf[7];
  u32 hbm;
#define MROT (2 * tn)
  const int mrot = MROT;
  auto load_h = [&](int m, int s) { if (s == 0) hbm = hb + (u32)(((m + mrot) & 3) * 57344); hf[s] = ld_sc1(rws, hbm, ksh[s]); };
  f4 yacc[4];
  u32 a_x, o_y;
  if (!ni) {
    CONV_SETUP();
    const int rho = (15 - r16) & 3;
    u32 a_A = L_HC + ((mi >> 1) * 4 + rho) * HC_STRIDE + (r16 + 1 + rho + 8 * g) * 2 + (mi & 1) * 256;
    u32 a_B = L_U + (mi >> 1) * U_STRIDE + (240 - 16 * r16 + 8 * g) * 2 + (mi & 1) * 256;
    u32 a_u = L_U + ((tid >> 2) & 1) * U_STRIDE + (tid >> 3) * 64 + (tid & 3) * 16;
    KEEP(a_A); KEEP(a_B); KEEP(a_u);
    LATE_SETUP();
    WAITV(0);
    PIN(u0); PIN(u1);
    lw128(a_u, u0); lw128(a_u + 2048, u1);
    if (w == 3) { PIN(stv); *(LAS v2u*)lp(L_TC + lane * 8) = stv; }
    w2_read(1); w2_read(2); w2_read(3);
    v4u AD;
    { const u32 dv = (mi & 2) ? ddp >> 16 : ddp & 0xffffu; const int pos = 15 - r16 - 8 * g;
#pragma unroll
      for (int j = 0; j < 4; ++j) AD[j] = (pos >> 1) == j ? dv << ((pos & 1) * 16) : 0u; }
    u32 a_B0 = a_B - (mi & 1) * 256;
    a_x = L_X + w4k + l16;
    o_y = s_cp + WS_YT + (mi >> 1) * 4096 + (mi & 1) * 512 + r16 * 32 + hl * 16 + (g & 1) * ((mi & 1) ? 512u : 1536u);
    KEEP(a_x); KEEP(a_B0); KEEP(o_y);
    LBAR();
    STAMP_AT(5);
    u32 pv2;
    auto conv = [&](auto ROLE) {
      constexpr int role = decltype(ROLE)::value;
      constexpr ConvList CL = make_list(role);
      f4 ac[8];
      v4u fa[4][8], fb[4][8], fd[4];
      auto rd = [&](auto N) {
        constexpr int n = decltype(N)::value, kl = n / 36;
        if constexpr (n < 144) {
          if constexpr (CL.newA[n]) {
            const v2u lo = *(volatile LAS v2u*)lp(a_A + CL.kh[n] * 512 + kl * 64), hi = *(volatile LAS v2u*)lp(a_A + CL.kh[n] * 512 + kl * 64 + 8);
            fa[kl][CL.kh[n]] = v4u{lo.x, lo.y, hi.x, hi.y};
          }
          if constexpr (CL.newB[n]) fb[kl][CL.e[n]] = lr128(a_B + (7 - CL.e[n]) * 512 + kl * 64);
        } else if constexpr (n < 148) fd[n - 144] = lr128(a_B0 + (7 - TS[role][n - 144]) * 512);
      };
      static_for<CLA>(rd);
      static_for<144>([&](auto N) {
        constexpr int n = decltype(N)::value, kl = n / 36, T = CL.kh[n] + CL.e[n];
        ac[T] = mfma(fa[kl][CL.kh[n]], fb[kl][CL.e[n]], n < 8 ? f4{0, 0, 0, 0} : ac[T]);
        rd(std::integral_constant<int, n + CLA>{});
        if constexpr (n >= 9 && n < 9 + 12 * 5 && (n - 9) % 5 == 0) { constexpr int q = (n - 9) / 5; W2_PIECE(4 + q / 4, q % 4); }
        if constexpr (n == 80) LOAD_LAST();
        if constexpr (n == 100) ALD32(pv2, fo2, rws, " sc1");
        if constexpr (n >= 128 && n < 136 && (n & 1) == 0) lwf4(a_x + ((n - 128) / 2) * 1024, ac[TS[role ^ 1][(n - 128) / 2]]);
        if constexpr (n == 136) *(volatile LAS u32*)lp(L_CNT0 + 16 + w * 4) = 1u;
        SB();
      });
#pragma unroll
      for (int j = 0; j < 4; ++j) yacc[j] = mfma(AD, fd[j], ac[TS[role][j]]);
    };
    if (mi & 1) conv(std::integral_constant<int, 1>{}); else conv(std::integral_constant<int, 0>{});
    STAMP_AT(8);
    WAITV(0);
    PIN(pv2);
    wait_flags(rws, fo2, tagv, pv2);
  } else {
    LBAR();
    STAMP_AT(5);
    LATE_SETUP();
    WAITV(0);
    if (is_g1) arrive_publish(L_CNT0, rws, l0, WS_FH + (ti * 14 + tj) * FLAG_STRIDE, tag);
    w2_read(1); w2_read(2); w2_read(3);
    WAITL();
    W2_STAGE(4); W2_STAGE(5); W2_STAGE(6);
    LOAD_LAST();
    STAMP_AT(8);
    wait_flags(rws, fo2, tagv, ~tagv);
  }
  STAMP_AT(9);
#pragma unroll
  for (int s = 0; s < 7; ++s) load_h(0, s);
  SB();
  if (!ni) {
#pragma unroll 1
    for (u32 spins = 0; spins < SPIN_CAP && *(volatile LAS u32*)lp(L_CNT0 + 16 + (w ^ 1) * 4) == 0u; ++spins) __builtin_amdgcn_s_sleep(1);
    asm volatile("" ::: "memory");
#pragma unroll
    for (int j = 0; j < 4; ++j) yacc[j] += lrf4(a_x + ((mi & 1) ? -4096 : 4096) + j * 1024);
#pragma unroll
    for (int j = 0; j < 4; j += 2) {
      const auto r0 = __builtin_amdgcn_permlane16_swap(pk(yacc[j][0], yacc[j][1]), pk(yacc[j + 1][0], yacc[j + 1][1]), false, false);
      const auto r1 = __builtin_amdgcn_permlane16_swap(pk(yacc[j][2], yacc[j][3]), pk(yacc[j + 1][2], yacc[j + 1][3]), false, false);
      if (j == 0) st_sc1<0>(rws, o_y, v4u{r0[0], r1[0], r0[1], r1[1]});
      else st_sc1<2048>(rws, o_y, v4u{r0[0], r1[0], r0[1], r1[1]});
    }
  }
  w2_read(4); w2_read(5); w2_read(6);
  STAMP_AT(10);
  const int tok = tid >> 3, chunk = tid & 7;

  u32 fo3 = WS_FY + (32 * tn + 4 * w + p4) * FLAG_STRIDE + 4 * hl, o_yl = WS_YT + (u32)((64 * tn + tok) * 4096 + (64 * tm + 8 * chunk) * 2);
  u32 pp0 = w4k + (((lane & 48) | ((lane + 4 * hl) & 15)) << 4), yp = L_Y + (8 * chunk) * 128 + ((w ^ chunk) << 4) + (tok & 7) * 2;
  u32 rp = (((w >> 1) - mrot) & 3) * L_SLOT + (chunk >> 1) * 1024 + ((32 * (chunk & 1) + ((tok + 4 * (chunk & 3)) & 15)) << 4);
  u32 yr = L_Y + tok * 128 + ((chunk ^ w) << 4);
  KEEP(fo3); KEEP(o_yl); KEEP(pp0); KEEP(yp); KEEP(rp); KEEP(yr);
  PIN(xv);
  v4u b2v; v2u tc; u32 cdone;
  float xx[8], xn[8], bb[8];
  v4u yv;
  {
    u32 pv3 = 0;
    f4 acc[2][4];
#pragma unroll
    for (int m = 0; m < 4; ++m) {
#pragma unroll
      for (int s = 0; s < 7; ++s) {
#pragma unroll
        for (int t = 0; t < 4; ++t) acc[m & 1][t] = mfma(w2f[s][t], hf[s], s == 0 ? f4{0, 0, 0, 0} : acc[m & 1][t]);
        if (m < 3) load_h(m + 1, s);
        if (m == 0 && s == 5 && !ni) {
          SB();
          WAITV(6);
          arrive_publish(L_CNT0 + 4, rws, l0, WS_FY + cp * FLAG_STRIDE, tag);
        }
        if (m > 0 && s >= 1 && s <= 4) lwf4(((pp0 + (m - 1) * L_SLOT) ^ (((s - 1) & 1) * 128)) + (s - 1) * 1024, acc[(m - 1) & 1][s - 1]);
        if (m == 2 && s == 0) pv3 = get_flag(rws, fo3);
        if (m == 3 && s == 1) {
          STAMP_F(28);
          wait_flags(rws, fo3, tagv, pv3);
          STAMP_F(29);
          yv = ld_sc1(rws, o_yl);
        }
        if (m == 3 && s == 2) { b2v = lr128(L_B2 + chunk * 16); tc = *(LAS v2u*)lp(L_TC + tok * 8); cdone = *(volatile LAS u32*)lp(L_CNT0 + 4); }
        if (m == 3 && s >= 3) {
          const int j = s - 3;
          xx[2 * j] = bl(xv[j]); xx[2 * j + 1] = bh(xv[j]); bb[2 * j] = bl(b2v[j]); bb[2 * j + 1] = bh(b2v[j]);
          xn[2 * j] = xx[2 * j] * asf(tc.x) + asf(tc.y); KEEP(xn[2 * j]);
          xn[2 * j + 1] = xx[2 * j + 1] * asf(tc.x) + asf(tc.y); KEEP(xn[2 * j + 1]);
          KEEP(xx[2 * j]); KEEP(xx[2 * j + 1]); KEEP(bb[2 * j]); KEEP(bb[2 * j + 1]);
        }
        SB();
      }
      STAMP_F(24 + m);
    }
    if (__builtin_amdgcn_readfirstlane(cdone) < 4u) {
#pragma unroll 1
      for (u32 spins = 0; spins < SPIN_CAP && *(volatile LAS u32*)lp(L_CNT0 + 4) < 4u; ++spins) __builtin_amdgcn_s_sleep(1);
    }
    asm volatile("" ::: "memory");
#pragma unroll
    for (int t = 0; t < 4; ++t) lwf4(((pp0 + 3 * L_SLOT) ^ ((t & 1) * 128)) + t * 1024, acc[1][t]);
  }
  STAMP_AT(11);
#pragma unroll
  for (int j = 0; j < 4; ++j) {
    *(LAS u16*)lp(yp + (2 * j) * 128) = (u16)(yv[j] & 0xffff);
    *(LAS u16*)lp(yp + (2 * j + 1) * 128) = (u16)(yv[j] >> 16);
  }
  LBAR();
  STAMP_AT(12);
  {
    const v4u yy = lr128(yr);
    const f4 o0 = ((lrf4(rp) + lrf4(rp + 4096)) + (lrf4(rp + 8192) + lrf4(rp + 12288))) +
                  ((lrf4(rp + 16384) + lrf4(rp + 20480)) + (lrf4(rp + 24576) + lrf4(rp + 28672)));
    const f4 o1 = ((lrf4(rp + 256) + lrf4(rp + 4352)) + (lrf4(rp + 8448) + lrf4(rp + 12544))) +
                  ((lrf4(rp + 16640) + lrf4(rp + 20736)) + (lrf4(rp + 24832) + lrf4(rp + 28928)));
    float res[8];
#pragma unroll
    for (int j = 0; j < 8; ++j) {
      const float ov = (j < 4 ? o0[j & 3] : o1[j & 3]) + bb[j];
      const float sg = __builtin_amdgcn_rcpf(1.0f + __builtin_amdgcn_exp2f(ov * -1.4426950409f));
      const float yj = (j & 1) ? bh(yy[j >> 1]) : bl(yy[j >> 1]);
      res[j] = xx[j] + ((xn[j] * yj) * ov) * sg;
    }
#define OUT_AUX 16
    __builtin_amdgcn_raw_buffer_store_b128(v4u{pk(res[0], res[1]), pk(res[2], res[3]), pk(res[4], res[5]), pk(res[6], res[7])}, mk_srd(p.out, S * D * 2), o_out, 0, OUT_AUX);
  }
  asm volatile("mk_code_end:");
}
}

extern "C" void kernel_launch(void* const* d_in, const int* in_sizes, int n_in, void* d_out, int out_size, void* d_ws, size_t ws_size,
                              hipStream_t stream) {
  using namespace mk;
  Params p;
  p.x = (const u16*)d_in[0]; p.k = (const u16*)d_in[1]; p.v = (const u16*)d_in[2]; p.sk = (const u16*)d_in[3];
  p.ssm = (const u16*)d_in[4]; p.dd = (const u16*)d_in[5]; p.w1 = (const u16*)d_in[7]; p.b1 = (const u16*)d_in[8];
  p.w2 = (const u16*)d_in[9]; p.b2 = (const u16*)d_in[10];
  p.out = (u16*)d_out; p.ws = (unsigned char*)d_ws;
  (void)hipFuncSetAttribute((const void*)fused, hipFuncAttributeMaxDynamicSharedMemorySize, LDS_BYTES);
  fused<<<256, 512, LDS_BYTES, stream>>>(p);
}
```

```cpp
#include <hip/hip_runtime.h>
#include <stdint.h>
#define XFL " sc1"
#define WFL " sc1"
#define W2FL " sc1"
#define XRFL ""
#define NO_T0
#define ROT1 xcd
#include <type_traits>
#include <utility>

namespace mk {
typedef unsigned short u16;
typedef unsigned u32;
typedef unsigned long long u64;
typedef u32 v4u __attribute__((ext_vector_type(4)));
typedef u32 v2u __attribute__((ext_vector_type(2)));
typedef float f4 __attribute__((ext_vector_type(4)));
typedef float f2 __attribute__((ext_vector_type(2)));
typedef float f16v __attribute__((ext_vector_type(16)));
typedef short s8v __attribute__((ext_vector_type(8)));
typedef short s4v __attribute__((ext_vector_type(4)));
typedef __bf16 bf2 __attribute__((ext_vector_type(2)));
#define GAS __attribute__((address_space(1)))
#define LAS __attribute__((address_space(3)))
#define CAS __attribute__((address_space(4)))
#define DI __device__ __forceinline__
typedef __amdgpu_buffer_rsrc_t srd_t;
typedef LAS unsigned char* lptr;

constexpr int S = 2048, D = 512, F = 1792;
constexpr float EPS = 1e-5f;
constexpr u32 WS_EPOCH = 0, WS_FKV = 4096, WS_FST = 36864, WS_FH = 40960, WS_FY = 69632, WS_STATS = 131072, WS_CSUM = 196608,
              WS_KVT = 1u << 20, WS_YT = 3u << 20, WS_H = 5u << 20, WS_BYTES = 13u << 20;
constexpr u32 FLAG_STRIDE = 128;
constexpr u32 L_SLOT = 32768;
constexpr u32 L_HC = 98304, HC_STRIDE = 4160, L_U = L_HC + 8 * HC_STRIDE, U_STRIDE = 4608;
constexpr u32 L_PK = L_U + 2 * U_STRIDE, L_PV = L_PK + 72 * 128, L_PW = L_PV + 64 * 128;
constexpr u32 L_JUNK = L_PW + 1024, L_CNT0 = L_JUNK + 256, L_B2 = L_CNT0 + 256, L_TC = L_B2 + 256;
constexpr u32 L_Y = L_PK, L_X = L_PK;
constexpr u32 LDS_BYTES = 163840;
static_assert(L_JUNK + 256 <= LDS_BYTES, "LDS map");
constexpr u32 SPIN_CAP = 1u << 16;

struct Params {
  const u16 *x, *k, *v, *sk, *ssm, *dd, *w1, *b1, *w2, *b2;
  u16* out;
  unsigned char* ws;
};

DI srd_t mk_srd(const void* p, u32 bytes) { return __builtin_amdgcn_make_buffer_rsrc((void*)p, (short)0, (int)bytes, 0x00020000); }
DI float asf(u32 d) { return __builtin_bit_cast(float, d); }
DI u32 asu(float f) { return __builtin_bit_cast(u32, f); }
DI bool all_lanes(bool c) { return __builtin_amdgcn_ballot_w64(!c) == 0; }
DI float bl(u32 d) { return asf(d << 16); }
DI float bh(u32 d) { return asf(d & 0xffff0000u); }
DI u32 pk(float a, float b) { f2 t = {a, b}; return __builtin_bit_cast(u32, __builtin_convertvector(t, bf2)); }
DI float rest(float v) { return v - bh(asu(v)); }
DI u32 pkt(float lo, float hi) { return __builtin_amdgcn_perm(asu(hi), asu(lo), 0x07060302u); }
DI f4 mfma(v4u a, v4u b, f4 c) { return __builtin_amdgcn_mfma_f32_16x16x32_bf16(__builtin_bit_cast(s8v, a), __builtin_bit_cast(s8v, b), c, 0, 0, 0); }
DI f16v mfma32(v4u a, v4u b, f16v c) { return __builtin_amdgcn_mfma_f32_32x32x16_bf16(__builtin_bit_cast(s8v, a), __builtin_bit_cast(s8v, b), c, 0, 0, 0); }
DI lptr lp(u32 a) { return (lptr)(uintptr_t)a; }
DI v2u trread(u32 a) { return __builtin_bit_cast(v2u, __builtin_amdgcn_ds_read_tr16_b64_v4i16((LAS s4v*)lp(a))); }
DI v4u trfrag(u32 a, u32 hi_off) { const v2u lo = trread(a), hi = trread(a + hi_off); return v4u{lo.x, lo.y, hi.x, hi.y}; }
DI v4u lr128(u32 a) { return *(LAS v4u*)lp(a); }
DI f4 lrf4(u32 a) { return *(LAS f4*)lp(a); }
DI void lw128(u32 a, v4u v) { *(LAS v4u*)lp(a) = v; }
DI void lwf4(u32 a, f4 v) { *(LAS f4*)lp(a) = v; }
#define DMA_(OP, FL, rs, voff, sb, SI, lb, LI) do { u32 t_; \
  asm volatile("s_add_u32 m0, %1, %2\n\ts_add_u32 %0, %3, %4\n\t" OP " %5, %6, %0 offen" FL " lds" \
               : "=&s"(t_) : "s"(lb), "i"(LI), "s"(sb), "i"(SI), "v"(voff), "s"(rs) : "memory", "scc"); } while (0)
#define DFL ""
#define DMA(rs, voff, sb, SI, lb, LI) DMA_("buffer_load_dwordx4", DFL, rs, voff, sb, SI, lb, LI)
#define SSM_FL " nt"
#define DMANT(rs, voff, sb, SI, lb, LI) DMA_("buffer_load_dwordx4", " nt", rs, voff, sb, SI, lb, LI)
#define M0SET(lb, LI) asm volatile("s_add_u32 m0, %0, %1\n\ts_nop 0" ::"s"(lb), "i"(LI) : "memory", "scc")
#define DMLF(FL, rs, voff, so, OFF) asm volatile("buffer_load_dwordx4 %0, %1, %2 offen offset:%3" FL " lds" ::"v"(voff), "s"(rs), "s"(so), "i"(OFF) : "memory")
#define DML(rs, voff, so, OFF) DMLF("", rs, voff, so, OFF)
#define TOUCH(rs, voff, sb, SI) DMA_("buffer_load_dword", "", rs, voff, sb, SI, 0u, L_JUNK)
#define ALD128(d, off, rs, FL) asm volatile("buffer_load_dwordx4 %0, %1, %2, 0 offen" FL : "=v"(d) : "v"(off), "s"(rs) : "memory")
#define ALD128S(d, off, rs, so, FL) asm volatile("buffer_load_dwordx4 %0, %1, %2, %3 offen" FL : "=v"(d) : "v"(off), "s"(rs), "s"(so) : "memory")
#define ALD64(d, off, rs, FL) asm volatile("buffer_load_dwordx2 %0, %1, %2, 0 offen" FL : "=v"(d) : "v"(off), "s"(rs) : "memory")
#define ALD32(d, off, rs, FL) asm volatile("buffer_load_dword %0, %1, %2, 0 offen" FL : "=v"(d) : "v"(off), "s"(rs) : "memory")
#define ALD16(d, off, rs, FL) asm volatile("buffer_load_ushort %0, %1, %2, 0 offen" FL : "=v"(d) : "v"(off), "s"(rs) : "memory")
#define WAITV(n) asm volatile("s_waitcnt vmcnt(" #n ")" ::: "memory")
#define PIN(a) asm volatile("" : "+v"(a)::"memory")
#define KEEP(a) asm volatile("" : "+v"(a))
#define KEEPP(a) asm volatile("" : "+s"(a))
#define KEEPS(a) do { a = __builtin_amdgcn_readfirstlane(a); asm volatile("" : "+s"(a)); } while (0)
#define WAITL() asm volatile("s_waitcnt lgkmcnt(0)" ::: "memory")
#define BAR() __builtin_amdgcn_s_barrier()
#define VBAR(n) do { __builtin_amdgcn_s_waitcnt(0xC07F); asm volatile("s_waitcnt vmcnt(" #n ") lgkmcnt(0)\n\ts_barrier" ::: "memory"); } while (0)
#define LBAR() do { __builtin_amdgcn_s_waitcnt(0xC07F); asm volatile("s_waitcnt lgkmcnt(0)\n\ts_barrier" ::: "memory"); } while (0)
#define SB() __builtin_amdgcn_sched_barrier(0)
DI void wave_sum2(float& a, float& b) {
  const auto x = __builtin_amdgcn_permlane32_swap(asu(a), asu(b), false, false);
  float v = asf(x[0]) + asf(x[1]);
  const auto y = __builtin_amdgcn_permlane16_swap(asu(v), asu(v), false, false);
  v = asf(y[0]) + asf(y[1]);
  v += asf(__builtin_amdgcn_update_dpp(0u, asu(v), 0xB1, 0xF, 0xF, true));
  v += asf(__builtin_amdgcn_update_dpp(0u, asu(v), 0x4E, 0xF, 0xF, true));
  v += asf(__builtin_amdgcn_update_dpp(0u, asu(v), 0x141, 0xF, 0xF, true));
  v += asf(__builtin_amdgcn_update_dpp(0u, asu(v), 0x140, 0xF, 0xF, true));
  const auto z = __builtin_amdgcn_permlane32_swap(asu(v), asu(v), false, false);
  a = asf(z[0]); b = asf(z[1]);
}
DI v4u ld_sc1(srd_t rs, u32 off, u32 soff = 0) { return __builtin_amdgcn_raw_buffer_load_b128(rs, off, soff, 16); }
template <int IMM = 0> DI void st_sc1(srd_t rs, u32 off, v4u v, u32 soff = 0) {
  asm volatile("buffer_store_dwordx4 %0, %1, %2, %3 offen offset:%4 sc1\n\ts_nop 1" ::"v"(v), "v"(off), "s"(rs), "s"(soff), "i"(IMM) : "memory");
}
DI void put_flag(srd_t rs, u32 l0, u32 off, u64 tag) { __builtin_amdgcn_raw_buffer_store_b64(__builtin_bit_cast(v2u, tag), rs, l0, off, 16); }
DI u32 get_flag(srd_t rs, u32 off) { return __builtin_amdgcn_raw_buffer_load_b32(rs, off, 0, 16); }
DI void wait_flags(srd_t rs, u32 off, u32 tagv, u32 v) {
  if (!all_lanes(v == tagv)) {
#pragma unroll 1
    for (u32 spins = 0; spins < SPIN_CAP; ++spins) {
      u32 x;
      asm volatile("buffer_load_dword %0, %1, %2, 0 offen sc1\n\ts_waitcnt vmcnt(0)" : "=&v"(x) : "v"(off), "s"(rs) : "memory");
      if (all_lanes(x == tagv)) break;
      __builtin_amdgcn_s_sleep(1);
    }
  }
  asm volatile("" ::: "memory");
}
DI void arrive_publish(u32 cnt, srd_t rs, u32 l0, u32 off, u64 tag) {
  u32 old;
  asm volatile("s_mov_b64 exec, 1\n\tds_add_rtn_u32 %0, %1, %2\n\ts_mov_b64 exec, -1\n\ts_waitcnt lgkmcnt(0)" : "=&v"(old) : "v"(cnt), "v"(1u) : "memory");
  if (__builtin_amdgcn_readfirstlane(old) == 3u) put_flag(rs, l0, off, tag);
}
#define STAMP_AT(i)
#define STAMP_F(i)
struct ConvList { int kh[144], e[144]; bool newA[144], newB[144]; };
constexpr int KEEP_MASK[2] = {0x99, 0x66};
constexpr int TS[2][4] = {{0, 3, 4, 7}, {1, 2, 5, 6}};
constexpr ConvList make_list(int role) {
  ConvList L = {};
  int n = 0;
  for (int kl = 0; kl < 4; ++kl) {
    bool seen[16] = {};
    for (int part = 0; part < (kl == 3 ? 2 : 1); ++part) {
      const int mask = kl < 3 ? 0xff : KEEP_MASK[part ? role : role ^ 1];
      for (int kh = 0; kh < 8; ++kh)
        for (int T = 7; T >= kh; --T) {
          if (!((mask >> T) & 1)) continue;
          const int e = T - kh;
          L.kh[n] = kh; L.e[n] = e; L.newA[n] = !seen[kh]; L.newB[n] = !seen[8 + e];
          seen[kh] = seen[8 + e] = true;
          ++n;
        }
    }
  }
  return L;
}
template <class F, int... I> DI void static_for_(F&& f, std::integer_sequence<int, I...>) { (f(std::integral_constant<int, I>{}), ...); }
template <int N, class F> DI void static_for(F&& f) { static_for_(f, std::make_integer_sequence<int, N>{}); }
#define CLA 10
#define NPF 0
#define ZX 1
#define ZW1 1
#define ZW2 1

__global__ void __launch_bounds__(512, 2) fused(Params p) {
  const int tid = __builtin_amdgcn_workitem_id_x(), lane = tid & 63, w = __builtin_amdgcn_readfirstlane(tid >> 6);
  const int b = __builtin_amdgcn_workgroup_id_x(), xcd = b & 7, idx = b >> 3;
  const int g = lane >> 4, r16 = lane & 15, q4 = r16 >> 2, p4 = lane & 3, rr = lane >> 3;
  STAMP_AT(0);
  if (w == 0) {
    u64 pc; u32 len;
    asm volatile("mk_code_begin:\n\ts_getpc_b64 %0\n\ts_mov_b32 %1, mk_code_end-mk_code_begin\n\ts_max_i32 %1, %1, mk_code_end2-mk_code_begin" : "=s"(pc), "=s"(len) :: "scc");
    const srd_t rc = mk_srd((const void*)pc, len);
    TOUCH(rc, (u32)(lane * 128), (u32)((idx & 3) * 8192), 0);
  }
  SB();
  KEEPP(p.ws); KEEPP(p.out); KEEPP(p.w2); KEEPP(p.b2); KEEPP(p.k);
  const int cp = 32 * xcd + idx;
  const bool is_g1 = idx < 28;
  const srd_t rx = mk_srd(p.x, S * D * 2 * ZX), rw1 = mk_srd(p.w1 - 1024, D * F * 2 * ZW1 + 2048);
  const u32 w1k = w * 1024, w4k = w * 4096;
  v4u xr;
  const int stok = 256 * xcd + 8 * idx + w;
  ALD128(xr, (u32)(stok * 1024 + lane * 16), rx, XRFL);
  SB();
  const int sidx = idx & 3, ti = 4 * (xcd >> 1) + sidx, t1 = idx >> 2, tj = 7 * (xcd & 1) + t1;
  const int hm = 4 * (xcd >> 1) + sidx, cgp = 14 * (xcd & 1) + (hm < 14 ? hm : 0);
  const u32 chsw = (u32)(((lane & 7) ^ (rr & 6)) << 4);
  const u32 xvo = (u32)((128 * ti + 16 * w + rr) * 1024 + (((lane & 7) ^ g) << 4)), xvo1 = (xvo ^ 64) + 7168;
  const u32 wvo = (u32)((8 * w + (is_g1 ? g : rr)) * 3584 + (is_g1 ? tj * 256 : cgp * 128)) + (is_g1 ? (u32)((r16 ^ (g << 2)) << 4) : chsw), wvo1 = wvo + 13312;
  u32 sox = (u32)(ROT1) * 128, sow = sox * 1792;
#define NEXT_CHUNK() do { sox = (sox + 128) & 1023; sow = sox * 1792; } while (0)
#define PIECE(c, i) do { \
    if ((i) == 0) { M0SET(w4k, ((c) % 3) * L_SLOT); DMLF(XFL, rx, xvo, sox, 0); } \
    if ((i) == 1) DMLF(XFL, rx, xvo1, sox, 1024); \
    if ((i) == 2) DMLF(WFL, rw1, wvo, sow, 2048); \
    if ((i) == 3) { DMLF(WFL, rw1, wvo1, sow, 3072); NEXT_CHUNK(); } } while (0)
#define HPIECE(c) DMA(rw1, wvo, 0u, (c) * 64 * 3584 + 2048, w1k, (c) * 8192)
  if (is_g1) { PIECE(0, 0); PIECE(0, 1); PIECE(0, 2); PIECE(0, 3); }
  else { HPIECE(0); HPIECE(1); HPIECE(2); HPIECE(3); }
  SB();

  u64 tag = *(const CAS u64*)(p.ws + WS_EPOCH);
  u32 ddp = *(const CAS u32*)(p.dd + 2 * cp);
  v2u hq = *(const CAS v2u*)(p.ssm + cp * 4096 + ((w & 3) ? w * 512 - 4 : 0));
  SB();
  const srd_t rws = mk_srd(p.ws, WS_BYTES);
  const int tm = 4 * xcd + sidx;
  const int tn = (t1 + 4 - (int)(((uintptr_t)p.w2 >> 7) & 7)) & 7;
  const int mi = w & 3, ni = w >> 2, hl = lane >> 5, l31 = lane & 31;
  u32 l0 = lane == 0 ? 0u : 0x80000000u, l01 = lane < 2 ? lane * 16 : 0x80000000u;
  u32 s_st = WS_STATS + (u32)stok * 32;
  const int m2 = w & 1, nq = w >> 1;
  u32 xa[2][4], wa[2];
#pragma unroll
  for (int ks = 0; ks < 4; ++ks) { xa[0][ks] = (4 * m2 + (g & 1)) * 4096 + r16 * 128 + (((2 * ks + hl) ^ (r16 >> 1)) << 4); xa[1][ks] = xa[0][ks] + 2 * L_SLOT; }
  wa[0] = 2048 + hl * 4096 + q4 * 256 + (((4 * nq + 2 * (g & 1) + (p4 >> 1)) ^ (q4 << 2)) << 4) + 8 * (p4 & 1);
  wa[1] = wa[0] + 2 * L_SLOT;
  u32 kvo = (u32)(rr * 1024 + (((lane & 7) ^ (rr & 2)) << 4)), kso = (u32)((64 * idx + 8 * w) * 1024 + xcd * 128);
  KEEP(l0); KEEP(l01); KEEPS(s_st); KEEP(kvo); KEEPS(kso);
#pragma unroll
  for (int i = 0; i < 4; ++i) { KEEP(xa[0][i]); KEEP(xa[1][i]); }
  KEEP(wa[0]); KEEP(wa[1]);
#define STAGE_K() do { const srd_t rk = mk_srd(p.k, S * D * 2); DMANT(rk, kvo, kso, 0, w1k, L_PK + 1024); \
    if (w == 0) DMANT(rk, kvo + kso - 8192u, 0u, 0, 0u, L_PK); } while (0)
#define STAGE_V() do { const srd_t rv = mk_srd(p.v, S * D * 2); DMANT(rv, kvo, kso, 0, w1k, L_PV); \
    if (w == 1) { const srd_t rsk = mk_srd(p.sk, D * 4 * 2); DMA(rsk, (u32)(lane * 16), (u32)(xcd * 512), 0, 0u, L_PW); } \
    if (w == 2) { const srd_t rb2 = mk_srd(p.b2, D * 2); DMA_("buffer_load_dword", "", rb2, (u32)(lane * 4), (u32)(tn * 128), 0, 0u, L_B2); } } while (0)
  constexpr u32 TR_HI = 1024;
  const srd_t rw2 = mk_srd(p.w2, F * D * 2 * ZW2), rssm = mk_srd(p.ssm + cp * 4096, 8192);
  u32 a_kv, a_tap, o_kv, s_kv, w2vo[4], w2base, ksh[7], s_t0, ta[4], t16, a_hc, fo1, fo2, o_xe, o_we, o_out, o_h, s_h, hb, s_cp, tagv;
  v2u ck[3], cv[2], ctw;
  float cw[4], kk[12], vv[8], co[8];
  const u32 l16 = lane * 16;
  auto fill = [&](auto KK) {
    constexpr int K = decltype(KK)::value;
    if constexpr (K == 200) { s_t0 = (u32)((224 * t1 + 8 * sidx) * 1024) + (ksh[0] << 5); KEEPS(s_t0); }
    if constexpr (K == 201) { a_kv = L_PK + 512 + ni * 4096 + (8 * g + q4) * 128 + (((2 * mi + (p4 >> 1)) ^ (q4 & 2)) << 4) + 8 * (p4 & 1); KEEP(a_kv); }
    if constexpr (K == 202) { a_tap = L_PW + mi * 128 + r16 * 8; KEEP(a_tap);
#pragma unroll
      for (int i = 0; i < 4; ++i) { w2vo[i] = (u32)(rr * 1024 + i * 7168) + chsw; KEEP(w2vo[i]); } }
    if constexpr (K == 203) { o_kv = (u32)((r16 >> 1) * 8192 + (r16 & 1) * 64 + (3 - g) * 16); KEEP(o_kv); }
    if constexpr (K == 204) { s_kv = WS_KVT + (u32)((32 * xcd + 8 * mi) * 8192 + (63 - 2 * idx - ni) * 128); KEEPS(s_kv); }
    if constexpr (K == 205) {
      if (w == 2) lw128(L_U + hl * U_STRIDE + 4096 + l31 * 16, v4u{0, 0, 0, 0});
      if (w == 3) lw128(L_CNT0 + (lane & 1) * 16, v4u{0, 0, 0, 0});
    }
    if constexpr (K == 9) { const int ia = 8 * sidx * 8 + xcd, ib = (16 * (ti & 1) + 8 * m2) * 8 + (ti >> 1);
                            fo1 = ((lane & (is_g1 ? 8 : 0)) ? ib : ia) + 8 * (lane & 7); KEEP(fo1); }
    if constexpr (K == 13) { const u32 fkv = (31 - 4 * mi - p4 - 4 * (lane & 4)) * 8 + xcd; fo1 = (lane & 16) ? fkv : fo1; KEEP(fo1); }
    if constexpr (K == 17) { const int m = 2 * t1 + ni; const u32 fc = (WS_FST - WS_FKV) / FLAG_STRIDE + ((m >> 2) * 2 + (xcd & 1)) * 4 + (m & 3);
                             fo1 = (lane & 24) == (is_g1 ? 24 : 64) ? fc : fo1; fo1 = WS_FKV + fo1 * FLAG_STRIDE + 4 * hl; KEEP(fo1); }
    if constexpr (K == 21) { tagv = hl ? (u32)(tag >> 32) : (u32)tag; KEEP(tagv); }
    if constexpr (K == 29) { t16 = tid * 16; o_out = (u32)((64 * tm + 8 * w) * 1024 + 128 * tn) + rr * 1024 + (lane & 7) * 16; KEEP(t16); KEEP(o_out); }
    if constexpr (K == 33) { o_xe = WS_STATS + (u32)((128 * ti + 64 * m2) * 32) + lane * 32; o_we = WS_CSUM + (u32)((128 * tj + 32 * nq) * 16) + l31 * 16; KEEP(o_xe); KEEP(o_we); }
    if constexpr (K == 37) { const int trow = 16 * hl + 4 * (g & 1) + q4; ta[0] = L_SLOT + w4k + trow * 128 + (((p4 >> 1) ^ (trow & 6)) << 4) + 8 * (p4 & 1); KEEP(ta[0]); }
    if constexpr (K == 41) { ta[1] = ta[0] ^ 32; ta[2] = ta[0] ^ 64; ta[3] = ta[0] ^ 96; KEEP(ta[1]); KEEP(ta[2]); KEEP(ta[3]);
                             a_hc = L_HC + ni * (4 * HC_STRIDE - 4096) + 32 + t16; KEEP(a_hc); }
    if constexpr (K == 45) { o_h = (u32)((g & 1) * 57344 + r16 * 16 + hl * 256); KEEP(o_h); }
    if constexpr (K == 49) { s_h = WS_H + (u32)(((8 * ti + 4 * m2) * 56 + 4 * tj + nq) * 1024); KEEPS(s_h); }
    if constexpr (K == 100) {
      ctw = *(LAS v2u*)lp(a_tap);
      ck[0] = trread(a_kv); ck[1] = trread(a_kv + 512); ck[2] = trread(a_kv + 1024);
      cv[0] = trread(a_kv + (L_PV - L_PK - 512)); cv[1] = trread(a_kv + (L_PV - L_PK));
    }
    if constexpr (K == 102) {
      cw[0] = bl(ctw.x); cw[1] = bh(ctw.x); cw[2] = bl(ctw.y); cw[3] = bh(ctw.y);
      kk[1] = bh(ck[0].x); kk[2] = bl(ck[0].y); kk[3] = bh(ck[0].y);
#pragma unroll
      for (int i = 0; i < 4; ++i) KEEP(cw[i]);
#pragma unroll
      for (int i = 1; i < 4; ++i) KEEP(kk[i]);
    }
    if constexpr (K >= 103 && K < 111) {
      constexpr int j = K - 103;
      const u32 kd = ck[1 + j / 4][(j / 2) & 1], vd = cv[j / 4][(j / 2) & 1];
      kk[j + 4] = (j & 1) ? bh(kd) : bl(kd); vv[j] = (j & 1) ? bh(vd) : bl(vd);
      asm volatile("v_mul_f32 %0, %1, %5\n\tv_fmac_f32 %0, %2, %6\n\tv_fmac_f32 %0, %3, %7\n\tv_fmac_f32 %0, %4, %8\n\tv_mul_f32 %0, %0, %9"
                   : "=&v"(co[j]) : "v"(kk[j + 1]), "v"(kk[j + 2]), "v"(kk[j + 3]), "v"(kk[j + 4]), "v"(cw[3]), "v"(cw[2]), "v"(cw[1]), "v"(cw[0]), "v"(vv[j]));
    }
    if constexpr (K == 111) st_sc1(rws, o_kv, v4u{pk(co[7], co[6]), pk(co[5], co[4]), pk(co[3], co[2]), pk(co[1], co[0])}, s_kv);
  };
  SB();
  BAR();
  STAGE_K(); STAGE_V();
  { int t = xcd;
    KEEPS(t);
#pragma unroll
    for (int s = 0; s < 7; ++s) { t = t >= 7 ? t - 7 : t; ksh[s] = (u32)t * 1024; KEEPS(ksh[s]); ++t; } }
  w2base = (u32)(224 * w * 1024 + tn * 128);
  s_cp = (u32)cp * 8192;
  KEEPS(w2base); KEEPS(s_cp);
  static_for<6>([&](auto I) { fill(std::integral_constant<int, 200 + decltype(I)::value>{}); });
  SB();
  WAITV(6);
  PIN(xr);
  STAMP_AT(1);
  {
    float s1 = 0.f, s2 = 0.f;
    u32 one2 = 0x3f803f80u;
    KEEP(one2);
#pragma unroll
    for (int j = 0; j < 4; ++j) {
      const u32 xj = xr[j];
      const bf2 v = __builtin_bit_cast(bf2, xj);
      s1 = __builtin_amdgcn_fdot2_f32_bf16(v, __builtin_bit_cast(bf2, one2), s1, false);
      s2 = __builtin_amdgcn_fdot2_f32_bf16(v, v, s2, false);
    }
    wave_sum2(s1, s2);
    const float mean = s1 * (1.0f / D), var = __builtin_fmaxf(s2 * (1.0f / D) - mean * mean, 0.f);
    const float iA = __builtin_amdgcn_sqrtf(var + EPS * (var + EPS));
    const float m0 = -mean, m1 = rest(m0), m2 = rest(m1);
    u32 d0 = pkt(m0, m0), d1 = pkt(m1, m1);
    const u32 mv = pk(mean, var);
    float rq = __builtin_amdgcn_rcpf(bl(pk(__builtin_amdgcn_sqrtf(bl(pk(bh(mv) + EPS, 0.f))), 0.f))), mq = -bl(mv) * rq;
    KEEP(d0); KEEP(d1); KEEP(rq); KEEP(mq);
    const bool l1 = lane & 1;
    st_sc1(rws, l01, v4u{l1 ? asu(rq) : d0, l1 ? asu(mq) : d1, pkt(m2, m0), pkt(iA, rest(iA))}, s_st);
  }
  tag += 0x9E3779B97F4A7C15ull;
  SB();
  if (is_g1) { PIECE(1, 0); PIECE(1, 1); PIECE(1, 2); PIECE(1, 3); }
  else { HPIECE(4); HPIECE(5); HPIECE(6); HPIECE(7); }
  v4u hd;
  ALD128(hd, (u32)(tid * 16), rssm, SSM_FL);
#define MAKE_KV() static_for<12>([&](auto I) { fill(std::integral_constant<int, 100 + decltype(I)::value>{}); })
  SB();
  STAMP_AT(2);
  v4u w2f[7][4];
  u32 sw2;
#define W2SLOT(s) ((s) == 0 ? 3 : ((s) + 1) % 3)
#define W2_PIECE(s, i) do { if ((i) == 0) { sw2 = w2base + (ksh[s] << 5); M0SET(w4k, W2SLOT(s) * L_SLOT); } DMLF(W2FL, rw2, w2vo[i], sw2, (i) * 1024); } while (0)
#define W2_STAGE(s) do { W2_PIECE(s, 0); W2_PIECE(s, 1); W2_PIECE(s, 2); W2_PIECE(s, 3); } while (0)
  v4u xv; v2u stv;
  v4u u0, u1;
#define LOAD_U() do { ALD128S(u0, t16, rws, s_cp + WS_KVT, " sc1"); ALD128S(u1, t16, rws, s_cp + WS_KVT + 4096, " sc1"); } while (0)
#define LOAD_LAST() ALD128(xv, o_out, rx, "")
#define LOAD_TC() ALD64(stv, WS_STATS + 16 + (64 * tm + lane) * 32, rws, " sc1")
  auto w2_read = [&](int s) {
#pragma unroll
    for (int t = 0; t < 4; ++t) w2f[s][t] = trfrag(ta[t] + W2SLOT(s) * L_SLOT - L_SLOT, TR_HI);
  };
  auto write_hc = [&](int rho) {
    const u32 p0 = __builtin_amdgcn_update_dpp(mi ? hq.x : 0u, hd.z, 0x138, 0xF, 0xF, false), p1 = __builtin_amdgcn_update_dpp(mi ? hq.y : 0u, hd.w, 0x138, 0xF, 0xF, false);
    const u32 a0 = __builtin_amdgcn_alignbit(p1, p0, 16), a1 = __builtin_amdgcn_alignbit(hd.x, p1, 16), a2 = __builtin_amdgcn_alignbit(hd.y, hd.x, 16),
              a3 = __builtin_amdgcn_alignbit(hd.z, hd.y, 16), a4 = __builtin_amdgcn_alignbit(hd.w, hd.z, 16);
    if (rho == 0) lw128(a_hc, hd);
    if (rho == 1) lw128(a_hc + HC_STRIDE, v4u{a1, a2, a3, a4});
    if (rho == 2) lw128(a_hc + 2 * HC_STRIDE, v4u{p1, hd.x, hd.y, hd.z});
    if (rho == 3) lw128(a_hc + 3 * HC_STRIDE, v4u{a0, a1, a2, a3});
  };

#define CONV_SETUP() do { PIN(hd); write_hc(0); write_hc(1); write_hc(2); write_hc(3); \
    if (w == 4) lw128(L_HC + rr * HC_STRIDE + (lane & 1) * 16, v4u{0, 0, 0, 0}); } while (0)

  if (__builtin_expect(is_g1, 1)) {
    f16v acc[2];
#pragma unroll
    for (int i = 0; i < 16; ++i) { acc[0][i] = 0.f; acc[1][i] = 0.f; }
    v4u xf[2][2][2], wf[2][2];
#define REQ(dc, i) do { if ((dc) >= 8) W2_PIECE((dc) & 3, i); else PIECE(dc, i); } while (0)
#define STEP(st, dc, d0, nd) do { \
      constexpr int hs_ = (st) & 1, hn_ = hs_ ^ 1, sl_ = (((st) + 1) >> 1) % 3, sb_ = sl_ >> 1; constexpr u32 so_ = (sl_ & 1) * L_SLOT; \
      constexpr bool rd_ = (st) < 15, mm_ = (st) >= 0; \
      _Pragma("unroll") for (int i_ = 0; i_ < 4; ++i_) { \
        if (mm_) acc[i_ & 1] = mfma32(wf[hs_][i_ >> 1], xf[hs_][i_ >> 1][i_ & 1], acc[i_ & 1]); \
        if (rd_) { \
          if (!(i_ & 1)) wf[hn_][i_ >> 1] = trfrag(wa[sb_] + so_ + (2 * hn_ + (i_ >> 1)) * 8192, 1024); \
          else { xf[hn_][i_ >> 1][0] = lr128(xa[sb_][2 * hn_ + (i_ >> 1)] + so_); xf[hn_][i_ >> 1][1] = lr128(xa[sb_][2 * hn_ + (i_ >> 1)] + so_ + 8192); } } \
        if ((nd) == 4) { if (i_ == 0) REQ(dc, 0); if (i_ == 1) REQ(dc, 1); if (i_ == 2) REQ(dc, 2); if (i_ == 3) REQ(dc, 3); } \
        if ((nd) == 2) { if (i_ == 0) REQ(dc, d0); if (i_ == 2) REQ(dc, (d0) + 1); } \
        if (mm_) { if (i_ == 0) fill(std::integral_constant<int, 4 * (st)>{}); if (i_ == 1) fill(std::integral_constant<int, 4 * (st) + 1>{}); \
                   if (i_ == 2) fill(std::integral_constant<int, 4 * (st) + 2>{}); if (i_ == 3) fill(std::integral_constant<int, 4 * (st) + 3>{}); } \
        SB(); } } while (0)
#define ITER(c) do { STAMP_F(16 + (c)); STEP(2 * (c) + 1, (c) + 3, 0, 2); STEP(2 * (c) + 2, (c) + 3, 2, 2); } while (0)
    u32 pv; v4u xe, we;
    VBAR(8);
    STAMP_AT(15);
    STEP(-1, 0, 0, 0);
    STEP(0, 2, 0, 4);
    VBAR(10);
    STAMP_F(30);
    MAKE_KV();
    SB();
    VBAR(6);
    ITER(0);
    VBAR(5);
    ITER(1); W2_PIECE(0, 0); W2_PIECE(0, 1);
    VBAR(6);
    if (w == 0) put_flag(rws, l0, WS_FKV + b * FLAG_STRIDE, tag);
    ITER(2); M0SET(w4k, 3 * L_SLOT); W2_PIECE(0, 2); W2_PIECE(0, 3);
    VBAR(8);
    ITER(3);
    VBAR(6);
    ITER(4);
    ALD32(pv, fo1, rws, " sc1");
    VBAR(5);
    STAMP_F(21);
    STEP(11, 9, 0, 2); STEP(12, 9, 2, 2);
    VBAR(4);
    STAMP_F(22);
    PIN(pv);
    wait_flags(rws, fo1, tagv, pv);
    ALD128(xe, o_xe, rws, " sc1");
    ALD128(we, o_we, rws, " sc1");
    if (!ni) LOAD_U();
    if (w == 3) LOAD_TC();
    STEP(13, 0, 0, 0); w2_read(0); STEP(14, 10, 0, 2);
    LBAR();
    STAMP_F(23);
    STEP(15, 10, 2, 2);
    if (ni) WAITV(4); else WAITV(6);
    PIN(xe); PIN(we);
    {
      const v4u z = {0, 0, 0, 0};
      acc[0] = mfma32(we, hl ? z : xe, acc[0]);
      acc[1] = mfma32(we, hl ? xe : z, acc[1]);
    }
    STAMP_AT(3);
    if (ni) { CONV_SETUP(); SB(); }
    f2 k1v[2], k3v[2], iAv[2];
    { const auto r = __builtin_amdgcn_permlane32_swap(xe.w, xe.w, false, false);
#pragma unroll
      for (int m = 0; m < 2; ++m) {
        const float iA = bl(r[m]) + bh(r[m]), A = __builtin_amdgcn_rcpf(iA), k1s = -2.3022082f * A, k3s = -0.10294324f * A * A * A;
        k1v[m] = f2{k1s, k1s}; k3v[m] = f2{k3s, k3s}; iAv[m] = f2{iA, iA};
      } }
#pragma unroll
    for (int qi = 0; qi < 4; ++qi) {
      const int n = qi >> 1, sp = qi & 1;
      if (qi == 0) W2_PIECE(3, 0);
      if (qi == 1) W2_PIECE(3, 1);
      if (qi == 2) W2_PIECE(3, 2);
      if (qi == 3) W2_PIECE(3, 3);
      v4u o;
#pragma unroll
      for (int j = 0; j < 4; ++j) {
        const f2 a = {acc[n][8 * sp + 2 * j], acc[n][8 * sp + 2 * j + 1]}, k1 = k1v[n], k3 = k3v[n], iA = iAv[n];
        const f2 z = a * __builtin_elementwise_fma(a * a, k3, k1);
        const f2 e = {__builtin_amdgcn_exp2f(z.x), __builtin_amdgcn_exp2f(z.y)};
        const f2 d = __builtin_elementwise_fma(e, iA, iA);
        const f2 hv = a * f2{__builtin_amdgcn_rcpf(d.x), __builtin_amdgcn_rcpf(d.y)};
        o[j] = pk(hv.x, hv.y);
      }
      if (qi == 0) st_sc1<0>(rws, o_h, o, s_h);
      if (qi == 1) st_sc1<512>(rws, o_h, o, s_h);
      if (qi == 2) st_sc1<0>(rws, o_h, o, s_h + 114688);
      if (qi == 3) st_sc1<512>(rws, o_h, o, s_h + 114688);
      SB();
    }
    STAMP_AT(4);
  } else {
    u32 b1v;
    { const srd_t rb1 = mk_srd(p.b1 + 64 * cgp, 128); ALD16(b1v, (u32)(16 * mi + 4 * g + p4) * 2, rb1, ""); }
    static_for<45>(fill);
    VBAR(0);
    STAMP_AT(15);
    if (w < 4) {
      const v4u ones = {0x3f803f80u, 0x3f803f80u, 0x3f803f80u, 0x3f803f80u};
      f4 s0 = {0, 0, 0, 0};
      const u32 a = (w == 0 ? ta[0] : w == 1 ? ta[1] : w == 2 ? ta[2] : ta[3]) - L_SLOT - w4k;
#pragma unroll
      for (int st = 0; st < 16; ++st) s0 = mfma(trfrag(a + st * 4096, TR_HI), ones, s0);
      const float ca = (p4 & 1) ? s0[1] : s0[0], cb = (p4 & 1) ? s0[3] : s0[2], c = (p4 & 2) ? cb : ca;
      const float c1 = rest(c), c2 = rest(c1);
      PIN(b1v);
      st_sc1(rws, r16 < 4 && hm < 14 ? WS_CSUM + (u32)(64 * cgp + 16 * w + 4 * g + p4) * 16 : 0x80000000u, v4u{pkt(c, c1), pkt(c, c1), pkt(c, c2), b1v * 0x10001u});
    }
    MAKE_KV();
    VBAR(0);
    if (w == 0) { put_flag(rws, l0, WS_FST + (4 * xcd + sidx) * FLAG_STRIDE, tag); put_flag(rws, l0, WS_FKV + b * FLAG_STRIDE, tag); }
    STAMP_AT(3);
#pragma unroll
    for (int i = 1; i <= NPF; ++i) { u32 kk = ksh[i]; KEEPS(kk); TOUCH(rw2, (u32)(lane * 128), (u32)((224 * w + 8 * sidx) * 1024) + (kk << 5), 0); }
    W2_STAGE(0); W2_STAGE(1); W2_STAGE(2); W2_STAGE(3);
    if (b == 255) {
      wait_flags(rws, WS_FKV + (32 * w + l31) * FLAG_STRIDE + 4 * hl, tagv, ~tagv);
      BAR();
      if (w == 0) put_flag(rws, l0, WS_EPOCH, tag);
    }
    wait_flags(rws, fo1, tagv, ~tagv);
    w2_read(0);
    STAMP_AT(4);
    LBAR();
    asm volatile("mk_code_end2:");
    if (ni) CONV_SETUP(); else LOAD_U();
    if (w == 3) LOAD_TC();
  }
#define LATE_SETUP() do { const int tja = ((224 * w) >> 7) + p4, tjb = (224 * w + 223) >> 7; \
    fo2 = WS_FH + ((tm >> 1) * 14 + (tja < tjb ? tja : tjb)) * FLAG_STRIDE + 4 * hl; hb = WS_H + (u32)((4 * tm * 56 + 7 * w) * 1024) + l16; KEEP(fo2); KEEP(hb); } while (0)
  v4u hf[7];
  u32 hbm;
#define MROT (2 * tn)
  const int mrot = MROT;
  auto load_h = [&](int m, int s) { if (s == 0) hbm = hb + (u32)(((m + mrot) & 3) * 57344); hf[s] = ld_sc1(rws, hbm, ksh[s]); };
  f4 yacc[4];
  u32 a_x, o_y;
  if (!ni) {
    CONV_SETUP();
    const int rho = (15 - r16) & 3;
    u32 a_A = L_HC + ((mi >> 1) * 4 + rho) * HC_STRIDE + (r16 + 1 + rho + 8 * g) * 2 + (mi & 1) * 256;
    u32 a_B = L_U + (mi >> 1) * U_STRIDE + (240 - 16 * r16 + 8 * g) * 2 + (mi & 1) * 256;
    u32 a_u = L_U + ((tid >> 2) & 1) * U_STRIDE + (tid >> 3) * 64 + (tid & 3) * 16;
    KEEP(a_A); KEEP(a_B); KEEP(a_u);
    LATE_SETUP();
    WAITV(0);
    PIN(u0); PIN(u1);
    lw128(a_u, u0); lw128(a_u + 2048, u1);
    if (w == 3) { PIN(stv); *(LAS v2u*)lp(L_TC + lane * 8) = stv; }
    w2_read(1); w2_read(2); w2_read(3);
    v4u AD;
    { const u32 dv = (mi & 2) ? ddp >> 16 : ddp & 0xffffu; const int pos = 15 - r16 - 8 * g;
#pragma unroll
      for (int j = 0; j < 4; ++j) AD[j] = (pos >> 1) == j ? dv << ((pos & 1) * 16) : 0u; }
    u32 a_B0 = a_B - (mi & 1) * 256;
    a_x = L_X + w4k + l16;
    o_y = s_cp + WS_YT + (mi >> 1) * 4096 + (mi & 1) * 512 + r16 * 32 + hl * 16 + (g & 1) * ((mi & 1) ? 512u : 1536u);
    KEEP(a_x); KEEP(a_B0); KEEP(o_y);
    LBAR();
    STAMP_AT(5);
    u32 pv2;
    auto conv = [&](auto ROLE) {
      constexpr int role = decltype(ROLE)::value;
      constexpr ConvList CL = make_list(role);
      f4 ac[8];
      v4u fa[4][8], fb[4][8], fd[4];
      auto rd = [&](auto N) {
        constexpr int n = decltype(N)::value, kl = n / 36;
        if constexpr (n < 144) {
          if constexpr (CL.newA[n]) {
            const v2u lo = *(volatile LAS v2u*)lp(a_A + CL.kh[n] * 512 + kl * 64), hi = *(volatile LAS v2u*)lp(a_A + CL.kh[n] * 512 + kl * 64 + 8);
            fa[kl][CL.kh[n]] = v4u{lo.x, lo.y, hi.x, hi.y};
          }
          if constexpr (CL.newB[n]) fb[kl][CL.e[n]] = lr128(a_B + (7 - CL.e[n]) * 512 + kl * 64);
        } else if constexpr (n < 148) fd[n - 144] = lr128(a_B0 + (7 - TS[role][n - 144]) * 512);
      };
      static_for<CLA>(rd);
      static_for<144>([&](auto N) {
        constexpr int n = decltype(N)::value, kl = n / 36, T = CL.kh[n] + CL.e[n];
        ac[T] = mfma(fa[kl][CL.kh[n]], fb[kl][CL.e[n]], n < 8 ? f4{0, 0, 0, 0} : ac[T]);
        rd(std::integral_constant<int, n + CLA>{});
        if constexpr (n >= 9 && n < 9 + 12 * 5 && (n - 9) % 5 == 0) { constexpr int q = (n - 9) / 5; W2_PIECE(4 + q / 4, q % 4); }
        if constexpr (n == 80) LOAD_LAST();
        if constexpr (n == 100) ALD32(pv2, fo2, rws, " sc1");
        if constexpr (n >= 128 && n < 136 && (n & 1) == 0) lwf4(a_x + ((n - 128) / 2) * 1024, ac[TS[role ^ 1][(n - 128) / 2]]);
        if constexpr (n == 136) *(volatile LAS u32*)lp(L_CNT0 + 16 + w * 4) = 1u;
        SB();
      });
#pragma unroll
      for (int j = 0; j < 4; ++j) yacc[j] = mfma(AD, fd[j], ac[TS[role][j]]);
    };
    if (mi & 1) conv(std::integral_constant<int, 1>{}); else conv(std::integral_constant<int, 0>{});
    STAMP_AT(8);
    WAITV(0);
    PIN(pv2);
    wait_flags(rws, fo2, tagv, pv2);
  } else {
    LBAR();
    STAMP_AT(5);
    LATE_SETUP();
    WAITV(0);
    if (is_g1) arrive_publish(L_CNT0, rws, l0, WS_FH + (ti * 14 + tj) * FLAG_STRIDE, tag);
    w2_read(1); w2_read(2); w2_read(3);
    WAITL();
    W2_STAGE(4); W2_STAGE(5); W2_STAGE(6);
    LOAD_LAST();
    STAMP_AT(8);
    wait_flags(rws, fo2, tagv, ~tagv);
  }
  STAMP_AT(9);
#pragma unroll
  for (int s = 0; s < 7; ++s) load_h(0, s);
  SB();
  if (!ni) {
#pragma unroll 1
    for (u32 spins = 0; spins < SPIN_CAP && *(volatile LAS u32*)lp(L_CNT0 + 16 + (w ^ 1) * 4) == 0u; ++spins) __builtin_amdgcn_s_sleep(1);
    asm volatile("" ::: "memory");
#pragma unroll
    for (int j = 0; j < 4; ++j) yacc[j] += lrf4(a_x + ((mi & 1) ? -4096 : 4096) + j * 1024);
#pragma unroll
    for (int j = 0; j < 4; j += 2) {
      const auto r0 = __builtin_amdgcn_permlane16_swap(pk(yacc[j][0], yacc[j][1]), pk(yacc[j + 1][0], yacc[j + 1][1]), false, false);
      const auto r1 = __builtin_amdgcn_permlane16_swap(pk(yacc[j][2], yacc[j][3]), pk(yacc[j + 1][2], yacc[j + 1][3]), false, false);
      if (j == 0) st_sc1<0>(rws, o_y, v4u{r0[0], r1[0], r0[1], r1[1]});
      else st_sc1<2048>(rws, o_y, v4u{r0[0], r1[0], r0[1], r1[1]});
    }
  }
  w2_read(4); w2_read(5); w2_read(6);
  STAMP_AT(10);
  const int tok = tid >> 3, chunk = tid & 7;

  u32 fo3 = WS_FY + (32 * tn + 4 * w + p4) * FLAG_STRIDE + 4 * hl, o_yl = WS_YT + (u32)((64 * tn + tok) * 4096 + (64 * tm + 8 * chunk) * 2);
  u32 pp0 = w4k + (((lane & 48) | ((lane + 4 * hl) & 15)) << 4), yp = L_Y + (8 * chunk) * 128 + ((w ^ chunk) << 4) + (tok & 7) * 2;
  u32 rp = (((w >> 1) - mrot) & 3) * L_SLOT + (chunk >> 1) * 1024 + ((32 * (chunk & 1) + ((tok + 4 * (chunk & 3)) & 15)) << 4);
  u32 yr = L_Y + tok * 128 + ((chunk ^ w) << 4);
  KEEP(fo3); KEEP(o_yl); KEEP(pp0); KEEP(yp); KEEP(rp); KEEP(yr);
  PIN(xv);
  v4u b2v; v2u tc; u32 cdone;
  float xx[8], xn[8], bb[8];
  v4u yv;
  {
    u32 pv3 = 0;
    f4 acc[2][4];
#pragma unroll
    for (int m = 0; m < 4; ++m) {
#pragma unroll
      for (int s = 0; s < 7; ++s) {
#pragma unroll
        for (int t = 0; t < 4; ++t) acc[m & 1][t] = mfma(w2f[s][t], hf[s], s == 0 ? f4{0, 0, 0, 0} : acc[m & 1][t]);
        if (m < 3) load_h(m + 1, s);
        if (m == 0 && s == 5 && !ni) {
          SB();
          WAITV(6);
          arrive_publish(L_CNT0 + 4, rws, l0, WS_FY + cp * FLAG_STRIDE, tag);
        }
        if (m > 0 && s >= 1 && s <= 4) lwf4(((pp0 + (m - 1) * L_SLOT) ^ (((s - 1) & 1) * 128)) + (s - 1) * 1024, acc[(m - 1) & 1][s - 1]);
        if (m == 2 && s == 0) pv3 = get_flag(rws, fo3);
        if (m == 3 && s == 1) {
          STAMP_F(28);
          wait_flags(rws, fo3, tagv, pv3);
          STAMP_F(29);
          yv = ld_sc1(rws, o_yl);
        }
        if (m == 3 && s == 2) { b2v = lr128(L_B2 + chunk * 16); tc = *(LAS v2u*)lp(L_TC + tok * 8); cdone = *(volatile LAS u32*)lp(L_CNT0 + 4); }
        if (m == 3 && s >= 3) {
          const int j = s - 3;
          xx[2 * j] = bl(xv[j]); xx[2 * j + 1] = bh(xv[j]); bb[2 * j] = bl(b2v[j]); bb[2 * j + 1] = bh(b2v[j]);
          xn[2 * j] = xx[2 * j] * asf(tc.x) + asf(tc.y); KEEP(xn[2 * j]);
          xn[2 * j + 1] = xx[2 * j + 1] * asf(tc.x) + asf(tc.y); KEEP(xn[2 * j + 1]);
          KEEP(xx[2 * j]); KEEP(xx[2 * j + 1]); KEEP(bb[2 * j]); KEEP(bb[2 * j + 1]);
        }
        SB();
      }
      STAMP_F(24 + m);
    }
    if (__builtin_amdgcn_readfirstlane(cdone) < 4u) {
#pragma unroll 1
      for (u32 spins = 0; spins < SPIN_CAP && *(volatile LAS u32*)lp(L_CNT0 + 4) < 4u; ++spins) __builtin_amdgcn_s_sleep(1);
    }
    asm volatile("" ::: "memory");
#pragma unroll
    for (int t = 0; t < 4; ++t) lwf4(((pp0 + 3 * L_SLOT) ^ ((t & 1) * 128)) + t * 1024, acc[1][t]);
  }
  STAMP_AT(11);
#pragma unroll
  for (int j = 0; j < 4; ++j) {
    *(LAS u16*)lp(yp + (2 * j) * 128) = (u16)(yv[j] & 0xffff);
    *(LAS u16*)lp(yp + (2 * j + 1) * 128) = (u16)(yv[j] >> 16);
  }
  LBAR();
  STAMP_AT(12);
  {
    const v4u yy = lr128(yr);
    const f4 o0 = ((lrf4(rp) + lrf4(rp + 4096)) + (lrf4(rp + 8192) + lrf4(rp + 12288))) +
                  ((lrf4(rp + 16384) + lrf4(rp + 20480)) + (lrf4(rp + 24576) + lrf4(rp + 28672)));
    const f4 o1 = ((lrf4(rp + 256) + lrf4(rp + 4352)) + (lrf4(rp + 8448) + lrf4(rp + 12544))) +
                  ((lrf4(rp + 16640) + lrf4(rp + 20736)) + (lrf4(rp + 24832) + lrf4(rp + 28928)));
    float res[8];
#pragma unroll
    for (int j = 0; j < 8; ++j) {
      const float ov = (j < 4 ? o0[j & 3] : o1[j & 3]) + bb[j];
      const float sg = __builtin_amdgcn_rcpf(1.0f + __builtin_amdgcn_exp2f(ov * -1.4426950409f));
      const float yj = (j & 1) ? bh(yy[j >> 1]) : bl(yy[j >> 1]);
      res[j] = xx[j] + ((xn[j] * yj) * ov) * sg;
    }
#define OUT_AUX 16
    __builtin_amdgcn_raw_buffer_store_b128(v4u{pk(res[0], res[1]), pk(res[2], res[3]), pk(res[4], res[5]), pk(res[6], res[7])}, mk_srd(p.out, S * D * 2), o_out, 0, OUT_AUX);
  }
  asm volatile("mk_code_end:");
}
}

extern "C" void kernel_launch(void* const* d_in, const int* in_sizes, int n_in, void* d_out, int out_size, void* d_ws, size_t ws_size,
                              hipStream_t stream) {
  using namespace mk;
  Params p;
  p.x = (const u16*)d_in[0]; p.k = (const u16*)d_in[1]; p.v = (const u16*)d_in[2]; p.sk = (const u16*)d_in[3];
  p.ssm = (const u16*)d_in[4]; p.dd = (const u16*)d_in[5]; p.w1 = (const u16*)d_in[7]; p.b1 = (const u16*)d_in[8];
  p.w2 = (const u16*)d_in[9]; p.b2 = (const u16*)d_in[10];
  p.out = (u16*)d_out; p.ws = (unsigned char*)d_ws;
  (void)hipFuncSetAttribute((const void*)fused, hipFuncAttributeMaxDynamicSharedMemorySize, LDS_BYTES);
  fused<<<256, 512, LDS_BYTES, stream>>>(p);
}
```

```cpp
#include <hip/hip_runtime.h>
#include <stdint.h>
#define EARLY 0
#include <type_traits>
#include <utility>

namespace mk {
typedef unsigned short u16;
typedef unsigned u32;
typedef unsigned long long u64;
typedef u32 v4u __attribute__((ext_vector_type(4)));
typedef u32 v2u __attribute__((ext_vector_type(2)));
typedef float f4 __attribute__((ext_vector_type(4)));
typedef float f2 __attribute__((ext_vector_type(2)));
typedef float f16v __attribute__((ext_vector_type(16)));
typedef short s8v __attribute__((ext_vector_type(8)));
typedef short s4v __attribute__((ext_vector_type(4)));
typedef __bf16 bf2 __attribute__((ext_vector_type(2)));
#define GAS __attribute__((address_space(1)))
#define LAS __attribute__((address_space(3)))
#define CAS __attribute__((address_space(4)))
#define DI __device__ __forceinline__
typedef __amdgpu_buffer_rsrc_t srd_t;
typedef LAS unsigned char* lptr;

constexpr int S = 2048, D = 512, F = 1792;
constexpr float EPS = 1e-5f;
constexpr u32 WS_EPOCH = 0, WS_FKV = 4096, WS_FST = 36864, WS_FH = 40960, WS_FY = 69632, WS_STATS = 131072, WS_CSUM = 196608,
              WS_KVT = 1u << 20, WS_YT = 3u << 20, WS_H = 5u << 20, WS_BYTES = 13u << 20;
constexpr u32 FLAG_STRIDE = 128;
constexpr u32 L_SLOT = 32768;
constexpr u32 L_HC = 98304, HC_STRIDE = 4160, L_U = L_HC + 8 * HC_STRIDE, U_STRIDE = 4608;
constexpr u32 L_PK = L_U + 2 * U_STRIDE, L_PV = L_PK + 72 * 128, L_PW = L_PV + 64 * 128;
constexpr u32 L_JUNK = L_PW + 1024, L_CNT0 = L_JUNK + 256, L_B2 = L_CNT0 + 256, L_TC = L_B2 + 256;
constexpr u32 L_Y = L_PK, L_X = L_PK;
constexpr u32 LDS_BYTES = 163840;
static_assert(L_JUNK + 256 <= LDS_BYTES, "LDS map");
constexpr u32 SPIN_CAP = 1u << 16;

struct Params {
  const u16 *x, *k, *v, *sk, *ssm, *dd, *w1, *b1, *w2, *b2;
  u16* out;
  unsigned char* ws;
};

DI srd_t mk_srd(const void* p, u32 bytes) { return __builtin_amdgcn_make_buffer_rsrc((void*)p, (short)0, (int)bytes, 0x00020000); }
DI float asf(u32 d) { return __builtin_bit_cast(float, d); }
DI u32 asu(float f) { return __builtin_bit_cast(u32, f); }
DI bool all_lanes(bool c) { return __builtin_amdgcn_ballot_w64(!c) == 0; }
DI float bl(u32 d) { return asf(d << 16); }
DI float bh(u32 d) { return asf(d & 0xffff0000u); }
DI u32 pk(float a, float b) { f2 t = {a, b}; return __builtin_bit_cast(u32, __builtin_convertvector(t, bf2)); }
DI float rest(float v) { return v - bh(asu(v)); }
DI u32 pkt(float lo, float hi) { return __builtin_amdgcn_perm(asu(hi), asu(lo), 0x07060302u); }
DI f4 mfma(v4u a, v4u b, f4 c) { return __builtin_amdgcn_mfma_f32_16x16x32_bf16(__builtin_bit_cast(s8v, a), __builtin_bit_cast(s8v, b), c, 0, 0, 0); }
DI f16v mfma32(v4u a, v4u b, f16v c) { return __builtin_amdgcn_mfma_f32_32x32x16_bf16(__builtin_bit_cast(s8v, a), __builtin_bit_cast(s8v, b), c, 0, 0, 0); }
DI lptr lp(u32 a) { return (lptr)(uintptr_t)a; }
DI v2u trread(u32 a) { return __builtin_bit_cast(v2u, __builtin_amdgcn_ds_read_tr16_b64_v4i16((LAS s4v*)lp(a))); }
DI v4u trfrag(u32 a, u32 hi_off) { const v2u lo = trread(a), hi = trread(a + hi_off); return v4u{lo.x, lo.y, hi.x, hi.y}; }
DI v4u lr128(u32 a) { return *(LAS v4u*)lp(a); }
DI f4 lrf4(u32 a) { return *(LAS f4*)lp(a); }
DI void lw128(u32 a, v4u v) { *(LAS v4u*)lp(a) = v; }
DI void lwf4(u32 a, f4 v) { *(LAS f4*)lp(a) = v; }
#define DMA_(OP, FL, rs, voff, sb, SI, lb, LI) do { u32 t_; \
  asm volatile("s_add_u32 m0, %1, %2\n\ts_add_u32 %0, %3, %4\n\t" OP " %5, %6, %0 offen" FL " lds" \
               : "=&s"(t_) : "s"(lb), "i"(LI), "s"(sb), "i"(SI), "v"(voff), "s"(rs) : "memory", "scc"); } while (0)
#define DFL ""
#define DMA(rs, voff, sb, SI, lb, LI) DMA_("buffer_load_dwordx4", DFL, rs, voff, sb, SI, lb, LI)
#define SSM_FL " nt"
#define DMANT(rs, voff, sb, SI, lb, LI) DMA_("buffer_load_dwordx4", " nt", rs, voff, sb, SI, lb, LI)
#define M0SET(lb, LI) asm volatile("s_add_u32 m0, %0, %1\n\ts_nop 0" ::"s"(lb), "i"(LI) : "memory", "scc")
#define DML(rs, voff, so, OFF) asm volatile("buffer_load_dwordx4 %0, %1, %2 offen offset:%3 lds" ::"v"(voff), "s"(rs), "s"(so), "i"(OFF) : "memory")
#define TOUCH(rs, voff, sb, SI) DMA_("buffer_load_dword", "", rs, voff, sb, SI, 0u, L_JUNK)
#define ALD128(d, off, rs, FL) asm volatile("buffer_load_dwordx4 %0, %1, %2, 0 offen" FL : "=v"(d) : "v"(off), "s"(rs) : "memory")
#define ALD128S(d, off, rs, so, FL) asm volatile("buffer_load_dwordx4 %0, %1, %2, %3 offen" FL : "=v"(d) : "v"(off), "s"(rs), "s"(so) : "memory")
#define ALD64(d, off, rs, FL) asm volatile("buffer_load_dwordx2 %0, %1, %2, 0 offen" FL : "=v"(d) : "v"(off), "s"(rs) : "memory")
#define ALD32(d, off, rs, FL) asm volatile("buffer_load_dword %0, %1, %2, 0 offen" FL : "=v"(d) : "v"(off), "s"(rs) : "memory")
#define ALD16(d, off, rs, FL) asm volatile("buffer_load_ushort %0, %1, %2, 0 offen" FL : "=v"(d) : "v"(off), "s"(rs) : "memory")
#define WAITV(n) asm volatile("s_waitcnt vmcnt(" #n ")" ::: "memory")
#define PIN(a) asm volatile("" : "+v"(a)::"memory")
#define KEEP(a) asm volatile("" : "+v"(a))
#define KEEPP(a) asm volatile("" : "+s"(a))
#define KEEPS(a) do { a = __builtin_amdgcn_readfirstlane(a); asm volatile("" : "+s"(a)); } while (0)
#define WAITL() asm volatile("s_waitcnt lgkmcnt(0)" ::: "memory")
#define BAR() __builtin_amdgcn_s_barrier()
#define VBAR(n) do { __builtin_amdgcn_s_waitcnt(0xC07F); asm volatile("s_waitcnt vmcnt(" #n ") lgkmcnt(0)\n\ts_barrier" ::: "memory"); } while (0)
#define LBAR() do { __builtin_amdgcn_s_waitcnt(0xC07F); asm volatile("s_waitcnt lgkmcnt(0)\n\ts_barrier" ::: "memory"); } while (0)
#define SB() __builtin_amdgcn_sched_barrier(0)
DI void wave_sum2(float& a, float& b) {
  const auto x = __builtin_amdgcn_permlane32_swap(asu(a), asu(b), false, false);
  float v = asf(x[0]) + asf(x[1]);
  const auto y = __builtin_amdgcn_permlane16_swap(asu(v), asu(v), false, false);
  v = asf(y[0]) + asf(y[1]);
  v += asf(__builtin_amdgcn_update_dpp(0u, asu(v), 0xB1, 0xF, 0xF, true));
  v += asf(__builtin_amdgcn_update_dpp(0u, asu(v), 0x4E, 0xF, 0xF, true));
  v += asf(__builtin_amdgcn_update_dpp(0u, asu(v), 0x141, 0xF, 0xF, true));
  v += asf(__builtin_amdgcn_update_dpp(0u, asu(v), 0x140, 0xF, 0xF, true));
  const auto z = __builtin_amdgcn_permlane32_swap(asu(v), asu(v), false, false);
  a = asf(z[0]); b = asf(z[1]);
}
DI v4u ld_sc1(srd_t rs, u32 off, u32 soff = 0) { return __builtin_amdgcn_raw_buffer_load_b128(rs, off, soff, 16); }
template <int IMM = 0> DI void st_sc1(srd_t rs, u32 off, v4u v, u32 soff = 0) {
  asm volatile("buffer_store_dwordx4 %0, %1, %2, %3 offen offset:%4 sc1\n\ts_nop 1" ::"v"(v), "v"(off), "s"(rs), "s"(soff), "i"(IMM) : "memory");
}
DI void put_flag(srd_t rs, u32 l0, u32 off, u64 tag) { __builtin_amdgcn_raw_buffer_store_b64(__builtin_bit_cast(v2u, tag), rs, l0, off, 16); }
DI u32 get_flag(srd_t rs, u32 off) { return __builtin_amdgcn_raw_buffer_load_b32(rs, off, 0, 16); }
DI void wait_flags(srd_t rs, u32 off, u32 tagv, u32 v) {
  if (!all_lanes(v == tagv)) {
#pragma unroll 1
    for (u32 spins = 0; spins < SPIN_CAP; ++spins) {
      u32 x;
      asm volatile("buffer_load_dword %0, %1, %2, 0 offen sc1\n\ts_waitcnt vmcnt(0)" : "=&v"(x) : "v"(off), "s"(rs) : "memory");
      if (all_lanes(x == tagv)) break;
      __builtin_amdgcn_s_sleep(1);
    }
  }
  asm volatile("" ::: "memory");
}
DI void arrive_publish(u32 cnt, srd_t rs, u32 l0, u32 off, u64 tag) {
  u32 old;
  asm volatile("s_mov_b64 exec, 1\n\tds_add_rtn_u32 %0, %1, %2\n\ts_mov_b64 exec, -1\n\ts_waitcnt lgkmcnt(0)" : "=&v"(old) : "v"(cnt), "v"(1u) : "memory");
  if (__builtin_amdgcn_readfirstlane(old) == 3u) put_flag(rs, l0, off, tag);
}
#define STAMP_AT(i)
#define STAMP_F(i)
struct ConvList { int kh[144], e[144]; bool newA[144], newB[144]; };
constexpr int KEEP_MASK[2] = {0x99, 0x66};
constexpr int TS[2][4] = {{0, 3, 4, 7}, {1, 2, 5, 6}};
constexpr ConvList make_list(int role) {
  ConvList L = {};
  int n = 0;
  for (int kl = 0; kl < 4; ++kl) {
    bool seen[16] = {};
    for (int part = 0; part < (kl == 3 ? 2 : 1); ++part) {
      const int mask = kl < 3 ? 0xff : KEEP_MASK[part ? role : role ^ 1];
      for (int kh = 0; kh < 8; ++kh)
        for (int T = 7; T >= kh; --T) {
          if (!((mask >> T) & 1)) continue;
          const int e = T - kh;
          L.kh[n] = kh; L.e[n] = e; L.newA[n] = !seen[kh]; L.newB[n] = !seen[8 + e];
          seen[kh] = seen[8 + e] = true;
          ++n;
        }
    }
  }
  return L;
}
template <class F, int... I> DI void static_for_(F&& f, std::integer_sequence<int, I...>) { (f(std::integral_constant<int, I>{}), ...); }
template <int N, class F> DI void static_for(F&& f) { static_for_(f, std::make_integer_sequence<int, N>{}); }
#define CLA 10
#define NPF 0
#define ZX 1
#define ZW1 1
#define ZW2 1

__global__ void __launch_bounds__(512, 2) fused(Params p) {
  const int tid = __builtin_amdgcn_workitem_id_x(), lane = tid & 63, w = __builtin_amdgcn_readfirstlane(tid >> 6);
  const int b = __builtin_amdgcn_workgroup_id_x(), xcd = b & 7, idx = b >> 3;
  const int g = lane >> 4, r16 = lane & 15, q4 = r16 >> 2, p4 = lane & 3, rr = lane >> 3;
  STAMP_AT(0);
  if (w == 0) {
    u64 pc; u32 len;
    asm volatile("mk_code_begin:\n\ts_getpc_b64 %0\n\ts_mov_b32 %1, mk_code_end-mk_code_begin\n\ts_max_i32 %1, %1, mk_code_end2-mk_code_begin" : "=s"(pc), "=s"(len) :: "scc");
    const srd_t rc = mk_srd((const void*)pc, len);
    TOUCH(rc, (u32)(lane * 128), (u32)((idx & 3) * 8192), 0);
  }
  SB();
  const int cp = 32 * xcd + idx;
  const bool is_g1 = idx < 28;
  u32 w1k = w * 1024, w4k = w * 4096;
  const int stok = 256 * xcd + 8 * idx + w;
  const int sidx = idx & 3, ti = 4 * (xcd >> 1) + sidx, t1 = idx >> 2, tj = 7 * (xcd & 1) + t1;
  const int hm = 4 * (xcd >> 1) + sidx, cgp = 14 * (xcd & 1) + (hm < 14 ? hm : 0);
  const int tm = 4 * xcd + sidx, tn = t1 ^ 4;
  const int mi = w & 3, ni = w >> 2, hl = lane >> 5, l31 = lane & 31;
  const int m2 = w & 1, nq = w >> 1;
  const u32 chsw = (u32)(((lane & 7) ^ (rr & 6)) << 4);
  u32 xro = (u32)(stok * 1024 + lane * 16);
  u32 xvo = (u32)((128 * ti + 16 * w + rr) * 1024 + (((lane & 7) ^ g) << 4)), xvo1 = (xvo ^ 64) + 7168;
  u32 wvo = (u32)((8 * w + (is_g1 ? g : rr)) * 3584 + (is_g1 ? tj * 256 : cgp * 128)) + (is_g1 ? (u32)((r16 ^ (g << 2)) << 4) : chsw), wvo1 = wvo + 13312;
#define ROT1 ((4 * ((t1 + sidx) & 1) + xcd) & 7)
  u32 sox = (u32)(ROT1) * 128, sow = sox * 1792;
  u32 kvo = (u32)(rr * 1024 + (((lane & 7) ^ (rr & 2)) << 4)), kso = (u32)((64 * idx + 8 * w) * 1024 + xcd * 128);
  u32 l0 = lane == 0 ? 0u : 0x80000000u, l01 = lane < 2 ? lane * 16 : 0x80000000u;
  u32 s_st = WS_STATS + (u32)stok * 32;
  u32 xa[2][4], wa[2];
#pragma unroll
  for (int ks = 0; ks < 4; ++ks) { xa[0][ks] = (4 * m2 + (g & 1)) * 4096 + r16 * 128 + (((2 * ks + hl) ^ (r16 >> 1)) << 4); xa[1][ks] = xa[0][ks] + 2 * L_SLOT; }
  wa[0] = 2048 + hl * 4096 + q4 * 256 + (((4 * nq + 2 * (g & 1) + (p4 >> 1)) ^ (q4 << 2)) << 4) + 8 * (p4 & 1);
  wa[1] = wa[0] + 2 * L_SLOT;
  KEEP(xro); KEEP(xvo); KEEP(xvo1); KEEP(wvo); KEEP(wvo1); KEEPS(sox); KEEPS(sow); KEEPS(w1k); KEEPS(w4k);
  KEEP(l0); KEEP(l01); KEEPS(s_st); KEEP(kvo); KEEPS(kso); KEEP(wa[0]); KEEP(wa[1]);
#pragma unroll
  for (int i = 0; i < 4; ++i) { KEEP(xa[0][i]); KEEP(xa[1][i]); }
  SB();
  KEEPP(p.ws); KEEPP(p.out); KEEPP(p.w2); KEEPP(p.b2); KEEPP(p.k); KEEPP(p.x); KEEPP(p.w1);
  const srd_t rx = mk_srd(p.x, S * D * 2 * ZX), rw1 = mk_srd(p.w1 - 1024, D * F * 2 * ZW1 + 2048);
  v4u xr;
  ALD128(xr, xro, rx, "");
#define NEXT_CHUNK() do { sox = (sox + 128) & 1023; sow = sox * 1792; } while (0)
#define PIECE(c, i) do { \
    if ((i) == 0) { M0SET(w4k, ((c) % 3) * L_SLOT); DML(rx, xvo, sox, 0); } \
    if ((i) == 1) DML(rx, xvo1, sox, 1024); \
    if ((i) == 2) DML(rw1, wvo, sow, 2048); \
    if ((i) == 3) { DML(rw1, wvo1, sow, 3072); NEXT_CHUNK(); } } while (0)
#define HPIECE(c) DMA(rw1, wvo, 0u, (c) * 64 * 3584 + 2048, w1k, (c) * 8192)
  if (is_g1) { PIECE(0, 0); PIECE(0, 1); PIECE(0, 2); PIECE(0, 3); }
  else { HPIECE(0); HPIECE(1); HPIECE(2); HPIECE(3); }
  SB();
  u64 tag = *(const CAS u64*)(p.ws + WS_EPOCH);
  u32 ddp = *(const CAS u32*)(p.dd + 2 * cp);
  v2u hq = *(const CAS v2u*)(p.ssm + cp * 4096 + ((w & 3) ? w * 512 - 4 : 0));
  SB();
  const srd_t rws = mk_srd(p.ws, WS_BYTES);
#define STAGE_K() do { const srd_t rk = mk_srd(p.k, S * D * 2); DMANT(rk, kvo, kso, 0, w1k, L_PK + 1024); \
    if (w == 0) DMANT(rk, kvo + kso - 8192u, 0u, 0, 0u, L_PK); } while (0)
#define STAGE_V() do { const srd_t rv = mk_srd(p.v, S * D * 2); DMANT(rv, kvo, kso, 0, w1k, L_PV); \
    if (w == 1) { const srd_t rsk = mk_srd(p.sk, D * 4 * 2); DMA(rsk, (u32)(lane * 16), (u32)(xcd * 512), 0, 0u, L_PW); } \
    if (w == 2) { const srd_t rb2 = mk_srd(p.b2, D * 2); DMA_("buffer_load_dword", "", rb2, (u32)(lane * 4), (u32)(tn * 128), 0, 0u, L_B2); } } while (0)
  constexpr u32 TR_HI = 1024;
  const srd_t rw2 = mk_srd(p.w2, F * D * 2 * ZW2), rssm = mk_srd(p.ssm + cp * 4096, 8192);
  u32 a_kv, a_tap, o_kv, s_kv, w2vo[4], w2base, ksh[7], s_t0, ta[4], t16, a_hc, fo1, fo2, o_xe, o_we, o_out, o_h, s_h, hb, s_cp, tagv;
  v2u ck[3], cv[2], ctw;
  float cw[4], kk[12], vv[8], co[8];
  const u32 l16 = lane * 16;
  auto fill = [&](auto KK) {
    constexpr int K = decltype(KK)::value;
    if constexpr (K == 200) { s_t0 = (u32)((224 * t1 + 8 * sidx) * 1024) + (ksh[0] << 5); KEEPS(s_t0); }
    if constexpr (K == 201) { a_kv = L_PK + 512 + ni * 4096 + (8 * g + q4) * 128 + (((2 * mi + (p4 >> 1)) ^ (q4 & 2)) << 4) + 8 * (p4 & 1); KEEP(a_kv); }
    if constexpr (K == 202) { a_tap = L_PW + mi * 128 + r16 * 8; KEEP(a_tap);
#pragma unroll
      for (int i = 0; i < 4; ++i) { w2vo[i] = (u32)(rr * 1024 + i * 7168) + chsw; KEEP(w2vo[i]); } }
    if constexpr (K == 203) { o_kv = (u32)((r16 >> 1) * 8192 + (r16 & 1) * 64 + (3 - g) * 16); KEEP(o_kv); }
    if constexpr (K == 204) { s_kv = WS_KVT + (u32)((32 * xcd + 8 * mi) * 8192 + (63 - 2 * idx - ni) * 128); KEEPS(s_kv); }
    if constexpr (K == 205) {
      if (w == 2) lw128(L_U + hl * U_STRIDE + 4096 + l31 * 16, v4u{0, 0, 0, 0});
      if (w == 3) lw128(L_CNT0 + (lane & 1) * 16, v4u{0, 0, 0, 0});
    }
    if constexpr (K == 9) { const int ia = 8 * sidx * 8 + xcd, ib = (16 * (ti & 1) + 8 * m2) * 8 + (ti >> 1);
                            fo1 = ((lane & (is_g1 ? 8 : 0)) ? ib : ia) + 8 * (lane & 7); KEEP(fo1); }
    if constexpr (K == 13) { const u32 fkv = (31 - 4 * mi - p4 - 4 * (lane & 4)) * 8 + xcd; fo1 = (lane & 16) ? fkv : fo1; KEEP(fo1); }
    if constexpr (K == 17) { const int m = 2 * t1 + ni; const u32 fc = (WS_FST - WS_FKV) / FLAG_STRIDE + ((m >> 2) * 2 + (xcd & 1)) * 4 + (m & 3);
                             fo1 = (lane & 24) == (is_g1 ? 24 : 64) ? fc : fo1; fo1 = WS_FKV + fo1 * FLAG_STRIDE + 4 * hl; KEEP(fo1); }
    if constexpr (K == 21) { tagv = hl ? (u32)(tag >> 32) : (u32)tag; KEEP(tagv); }
    if constexpr (K == 29) { t16 = tid * 16; o_out = (u32)((64 * tm + 8 * w) * 1024 + 128 * tn) + rr * 1024 + (lane & 7) * 16; KEEP(t16); KEEP(o_out); }
    if constexpr (K == 33) { o_xe = WS_STATS + (u32)((128 * ti + 64 * m2) * 32) + lane * 32; o_we = WS_CSUM + (u32)((128 * tj + 32 * nq) * 16) + l31 * 16; KEEP(o_xe); KEEP(o_we); }
    if constexpr (K == 37) { const int trow = 16 * hl + 4 * (g & 1) + q4; ta[0] = L_SLOT + w4k + trow * 128 + (((p4 >> 1) ^ (trow & 6)) << 4) + 8 * (p4 & 1); KEEP(ta[0]); }
    if constexpr (K == 41) { ta[1] = ta[0] ^ 32; ta[2] = ta[0] ^ 64; ta[3] = ta[0] ^ 96; KEEP(ta[1]); KEEP(ta[2]); KEEP(ta[3]);
                             a_hc = L_HC + ni * (4 * HC_STRIDE - 4096) + 32 + t16; KEEP(a_hc); }
    if constexpr (K == 45) { o_h = (u32)((g & 1) * 57344 + r16 * 16 + hl * 256); KEEP(o_h); }
    if constexpr (K == 49) { s_h = WS_H + (u32)(((8 * ti + 4 * m2) * 56 + 4 * tj + nq) * 1024); KEEPS(s_h); }
    if constexpr (K == 100) {
      ctw = *(LAS v2u*)lp(a_tap);
      ck[0] = trread(a_kv); ck[1] = trread(a_kv + 512); ck[2] = trread(a_kv + 1024);
      cv[0] = trread(a_kv + (L_PV - L_PK - 512)); cv[1] = trread(a_kv + (L_PV - L_PK));
    }
    if constexpr (K == 102) {
      cw[0] = bl(ctw.x); cw[1] = bh(ctw.x); cw[2] = bl(ctw.y); cw[3] = bh(ctw.y);
      kk[1] = bh(ck[0].x); kk[2] = bl(ck[0].y); kk[3] = bh(ck[0].y);
#pragma unroll
      for (int i = 0; i < 4; ++i) KEEP(cw[i]);
#pragma unroll
      for (int i = 1; i < 4; ++i) KEEP(kk[i]);
    }
    if constexpr (K >= 103 && K < 111) {
      constexpr int j = K - 103;
      const u32 kd = ck[1 + j / 4][(j / 2) & 1], vd = cv[j / 4][(j / 2) & 1];
      kk[j + 4] = (j & 1) ? bh(kd) : bl(kd); vv[j] = (j & 1) ? bh(vd) : bl(vd);
      asm volatile("v_mul_f32 %0, %1, %5\n\tv_fmac_f32 %0, %2, %6\n\tv_fmac_f32 %0, %3, %7\n\tv_fmac_f32 %0, %4, %8\n\tv_mul_f32 %0, %0, %9"
                   : "=&v"(co[j]) : "v"(kk[j + 1]), "v"(kk[j + 2]), "v"(kk[j + 3]), "v"(kk[j + 4]), "v"(cw[3]), "v"(cw[2]), "v"(cw[1]), "v"(cw[0]), "v"(vv[j]));
    }
    if constexpr (K == 111) st_sc1(rws, o_kv, v4u{pk(co[7], co[6]), pk(co[5], co[4]), pk(co[3], co[2]), pk(co[1], co[0])}, s_kv);
  };
  SB();
  { int t = xcd;
    KEEPS(t);
#pragma unroll
    for (int s = 0; s < 7; ++s) { t = t >= 7 ? t - 7 : t; ksh[s] = (u32)t * 1024; KEEPS(ksh[s]); ++t; } }
  w2base = (u32)(224 * w * 1024 + tn * 128);
  s_cp = (u32)cp * 8192;
  KEEPS(w2base); KEEPS(s_cp);
  static_for<6>([&](auto I) { fill(std::integral_constant<int, 200 + decltype(I)::value>{}); });
  SB();
  BAR();
  STAGE_K(); STAGE_V();
  SB();
  WAITV(6);
  PIN(xr);
  STAMP_AT(1);
  {
    float s1 = 0.f, s2 = 0.f;
    u32 one2 = 0x3f803f80u;
    KEEP(one2);
#pragma unroll
    for (int j = 0; j < 4; ++j) {
      const u32 xj = xr[j];
      const bf2 v = __builtin_bit_cast(bf2, xj);
      s1 = __builtin_amdgcn_fdot2_f32_bf16(v, __builtin_bit_cast(bf2, one2), s1, false);
      s2 = __builtin_amdgcn_fdot2_f32_bf16(v, v, s2, false);
    }
    wave_sum2(s1, s2);
    const float mean = s1 * (1.0f / D), var = __builtin_fmaxf(s2 * (1.0f / D) - mean * mean, 0.f);
    const float iA = __builtin_amdgcn_sqrtf(var + EPS * (var + EPS));
    const float m0 = -mean, m1 = rest(m0), m2 = rest(m1);
    u32 d0 = pkt(m0, m0), d1 = pkt(m1, m1);
    const u32 mv = pk(mean, var);
    float rq = __builtin_amdgcn_rcpf(bl(pk(__builtin_amdgcn_sqrtf(bl(pk(bh(mv) + EPS, 0.f))), 0.f))), mq = -bl(mv) * rq;
    KEEP(d0); KEEP(d1); KEEP(rq); KEEP(mq);
    const bool l1 = lane & 1;
    st_sc1(rws, l01, v4u{l1 ? asu(rq) : d0, l1 ? asu(mq) : d1, pkt(m2, m0), pkt(iA, rest(iA))}, s_st);
  }
  SB();
  if (is_g1) { PIECE(1, 0); PIECE(1, 1); PIECE(1, 2); PIECE(1, 3); }
  else { HPIECE(4); HPIECE(5); HPIECE(6); HPIECE(7); }
  v4u hd;
  ALD128(hd, (u32)(tid * 16), rssm, SSM_FL);
  if (w == 6) TOUCH(rw2, (u32)(lane * 128), s_t0, 0);
#define MAKE_KV() static_for<12>([&](auto I) { fill(std::integral_constant<int, 100 + decltype(I)::value>{}); })
  SB();
  tag += 0x9E3779B97F4A7C15ull;
  STAMP_AT(2);
  v4u w2f[7][4];
  u32 sw2;
#define W2SLOT(s) ((s) == 0 ? 3 : ((s) + 1) % 3)
#define W2_PIECE(s, i) do { if ((i) == 0) { sw2 = w2base + (ksh[s] << 5); M0SET(w4k, W2SLOT(s) * L_SLOT); } DML(rw2, w2vo[i], sw2, (i) * 1024); } while (0)
#define W2_STAGE(s) do { W2_PIECE(s, 0); W2_PIECE(s, 1); W2_PIECE(s, 2); W2_PIECE(s, 3); } while (0)
  v4u xv; v2u stv;
  v4u u0, u1;
#define LOAD_U() do { ALD128S(u0, t16, rws, s_cp + WS_KVT, " sc1"); ALD128S(u1, t16, rws, s_cp + WS_KVT + 4096, " sc1"); } while (0)
#define LOAD_LAST() ALD128(xv, o_out, rx, "")
#define LOAD_TC() ALD64(stv, WS_STATS + 16 + (64 * tm + lane) * 32, rws, " sc1")
  auto w2_read = [&](int s) {
#pragma unroll
    for (int t = 0; t < 4; ++t) w2f[s][t] = trfrag(ta[t] + W2SLOT(s) * L_SLOT - L_SLOT, TR_HI);
  };
  auto write_hc = [&](int rho) {
    const u32 p0 = __builtin_amdgcn_update_dpp(mi ? hq.x : 0u, hd.z, 0x138, 0xF, 0xF, false), p1 = __builtin_amdgcn_update_dpp(mi ? hq.y : 0u, hd.w, 0x138, 0xF, 0xF, false);
    const u32 a0 = __builtin_amdgcn_alignbit(p1, p0, 16), a1 = __builtin_amdgcn_alignbit(hd.x, p1, 16), a2 = __builtin_amdgcn_alignbit(hd.y, hd.x, 16),
              a3 = __builtin_amdgcn_alignbit(hd.z, hd.y, 16), a4 = __builtin_amdgcn_alignbit(hd.w, hd.z, 16);
    if (rho == 0) lw128(a_hc, hd);
    if (rho == 1) lw128(a_hc + HC_STRIDE, v4u{a1, a2, a3, a4});
    if (rho == 2) lw128(a_hc + 2 * HC_STRIDE, v4u{p1, hd.x, hd.y, hd.z});
    if (rho == 3) lw128(a_hc + 3 * HC_STRIDE, v4u{a0, a1, a2, a3});
  };

#define CONV_SETUP() do { PIN(hd); write_hc(0); write_hc(1); write_hc(2); write_hc(3); \
    if (w == 4) lw128(L_HC + rr * HC_STRIDE + (lane & 1) * 16, v4u{0, 0, 0, 0}); } while (0)

  if (__builtin_expect(is_g1, 1)) {
    f16v acc[2];
#pragma unroll
    for (int i = 0; i < 16; ++i) { acc[0][i] = 0.f; acc[1][i] = 0.f; }
    v4u xf[2][2][2], wf[2][2];
#define REQ(dc, i) do { if ((dc) >= 8) W2_PIECE((dc) & 3, i); else PIECE(dc, i); } while (0)
#define STEP(st, dc, d0, nd) do { \
      constexpr int hs_ = (st) & 1, hn_ = hs_ ^ 1, sl_ = (((st) + 1) >> 1) % 3, sb_ = sl_ >> 1; constexpr u32 so_ = (sl_ & 1) * L_SLOT; \
      constexpr bool rd_ = (st) < 15, mm_ = (st) >= 0; \
      _Pragma("unroll") for (int i_ = 0; i_ < 4; ++i_) { \
        if (mm_) acc[i_ & 1] = mfma32(wf[hs_][i_ >> 1], xf[hs_][i_ >> 1][i_ & 1], acc[i_ & 1]); \
        if (rd_) { \
          if (!(i_ & 1)) wf[hn_][i_ >> 1] = trfrag(wa[sb_] + so_ + (2 * hn_ + (i_ >> 1)) * 8192, 1024); \
          else { xf[hn_][i_ >> 1][0] = lr128(xa[sb_][2 * hn_ + (i_ >> 1)] + so_); xf[hn_][i_ >> 1][1] = lr128(xa[sb_][2 * hn_ + (i_ >> 1)] + so_ + 8192); } } \
        if ((nd) == 4) { if (i_ == 0) REQ(dc, 0); if (i_ == 1) REQ(dc, 1); if (i_ == 2) REQ(dc, 2); if (i_ == 3) REQ(dc, 3); } \
        if ((nd) == 2) { if (i_ == 0) REQ(dc, d0); if (i_ == 2) REQ(dc, (d0) + 1); } \
        if (mm_ && !EARLY) { if (i_ == 0) fill(std::integral_constant<int, 4 * (st)>{}); if (i_ == 1) fill(std::integral_constant<int, 4 * (st) + 1>{}); \
                   if (i_ == 2) fill(std::integral_constant<int, 4 * (st) + 2>{}); if (i_ == 3) fill(std::integral_constant<int, 4 * (st) + 3>{}); } \
        SB(); } } while (0)
#define ITER(c) do { STAMP_F(16 + (c)); STEP(2 * (c) + 1, (c) + 3, 0, 2); STEP(2 * (c) + 2, (c) + 3, 2, 2); } while (0)
    u32 pv; v4u xe, we;
    VBAR(8);
    STAMP_AT(15);
    STEP(-1, 0, 0, 0);
    STEP(0, 2, 0, 4);
    VBAR(10);
    STAMP_F(30);
    MAKE_KV();
    SB();
    VBAR(6);
    ITER(0);
    VBAR(5);
    ITER(1); W2_PIECE(0, 0); W2_PIECE(0, 1);
    VBAR(6);
    if (w == 0) put_flag(rws, l0, WS_FKV + b * FLAG_STRIDE, tag);
    ITER(2); M0SET(w4k, 3 * L_SLOT); W2_PIECE(0, 2); W2_PIECE(0, 3);
    VBAR(8);
    ITER(3);
    VBAR(6);
    ITER(4);
    ALD32(pv, fo1, rws, " sc1");
    VBAR(5);
    STAMP_F(21);
    STEP(11, 9, 0, 2); STEP(12, 9, 2, 2);
    VBAR(4);
    STAMP_F(22);
    PIN(pv);
    wait_flags(rws, fo1, tagv, pv);
    ALD128(xe, o_xe, rws, " sc1");
    ALD128(we, o_we, rws, " sc1");
    if (!ni) LOAD_U();
    if (w == 3) LOAD_TC();
    STEP(13, 0, 0, 0); w2_read(0); STEP(14, 10, 0, 2);
    LBAR();
    STAMP_F(23);
    STEP(15, 10, 2, 2);
    if (ni) WAITV(4); else WAITV(6);
    PIN(xe); PIN(we);
    {
      const v4u z = {0, 0, 0, 0};
      acc[0] = mfma32(we, hl ? z : xe, acc[0]);
      acc[1] = mfma32(we, hl ? xe : z, acc[1]);
    }
    STAMP_AT(3);
    if (ni) { CONV_SETUP(); SB(); }
    f2 k1v[2], k3v[2], iAv[2];
    { const auto r = __builtin_amdgcn_permlane32_swap(xe.w, xe.w, false, false);
#pragma unroll
      for (int m = 0; m < 2; ++m) {
        const float iA = bl(r[m]) + bh(r[m]), A = __builtin_amdgcn_rcpf(iA), k1s = -2.3022082f * A, k3s = -0.10294324f * A * A * A;
        k1v[m] = f2{k1s, k1s}; k3v[m] = f2{k3s, k3s}; iAv[m] = f2{iA, iA};
      } }
#pragma unroll
    for (int qi = 0; qi < 4; ++qi) {
      const int n = qi >> 1, sp = qi & 1;
      if (qi == 0) W2_PIECE(3, 0);
      if (qi == 1) W2_PIECE(3, 1);
      if (qi == 2) W2_PIECE(3, 2);
      if (qi == 3) W2_PIECE(3, 3);
      v4u o;
#pragma unroll
      for (int j = 0; j < 4; ++j) {
        const f2 a = {acc[n][8 * sp + 2 * j], acc[n][8 * sp + 2 * j + 1]}, k1 = k1v[n], k3 = k3v[n], iA = iAv[n];
        const f2 z = a * __builtin_elementwise_fma(a * a, k3, k1);
        const f2 e = {__builtin_amdgcn_exp2f(z.x), __builtin_amdgcn_exp2f(z.y)};
        const f2 d = __builtin_elementwise_fma(e, iA, iA);
        const f2 hv = a * f2{__builtin_amdgcn_rcpf(d.x), __builtin_amdgcn_rcpf(d.y)};
        o[j] = pk(hv.x, hv.y);
      }
      if (qi == 0) st_sc1<0>(rws, o_h, o, s_h);
      if (qi == 1) st_sc1<512>(rws, o_h, o, s_h);
      if (qi == 2) st_sc1<0>(rws, o_h, o, s_h + 114688);
      if (qi == 3) st_sc1<512>(rws, o_h, o, s_h + 114688);
      SB();
    }
    STAMP_AT(4);
  } else {
    u32 b1v;
    { const srd_t rb1 = mk_srd(p.b1 + 64 * cgp, 128); ALD16(b1v, (u32)(16 * mi + 4 * g + p4) * 2, rb1, ""); }
    static_for<45>(fill);
    VBAR(0);
    STAMP_AT(15);
    if (w < 4) {
      const v4u ones = {0x3f803f80u, 0x3f803f80u, 0x3f803f80u, 0x3f803f80u};
      f4 s0 = {0, 0, 0, 0};
      const u32 a = (w == 0 ? ta[0] : w == 1 ? ta[1] : w == 2 ? ta[2] : ta[3]) - L_SLOT - w4k;
#pragma unroll
      for (int st = 0; st < 16; ++st) s0 = mfma(trfrag(a + st * 4096, TR_HI), ones, s0);
      const float ca = (p4 & 1) ? s0[1] : s0[0], cb = (p4 & 1) ? s0[3] : s0[2], c = (p4 & 2) ? cb : ca;
      const float c1 = rest(c), c2 = rest(c1);
      PIN(b1v);
      st_sc1(rws, r16 < 4 && hm < 14 ? WS_CSUM + (u32)(64 * cgp + 16 * w + 4 * g + p4) * 16 : 0x80000000u, v4u{pkt(c, c1), pkt(c, c1), pkt(c, c2), b1v * 0x10001u});
    }
    MAKE_KV();
    VBAR(0);
    if (w == 0) { put_flag(rws, l0, WS_FST + (4 * xcd + sidx) * FLAG_STRIDE, tag); put_flag(rws, l0, WS_FKV + b * FLAG_STRIDE, tag); }
    STAMP_AT(3);
#pragma unroll
    for (int i = 1; i <= NPF; ++i) { u32 kk = ksh[i]; KEEPS(kk); TOUCH(rw2, (u32)(lane * 128), (u32)((224 * w + 8 * sidx) * 1024) + (kk << 5), 0); }
    W2_STAGE(0); W2_STAGE(1); W2_STAGE(2); W2_STAGE(3);
    if (b == 255) {
      wait_flags(rws, WS_FKV + (32 * w + l31) * FLAG_STRIDE + 4 * hl, tagv, ~tagv);
      BAR();
      if (w == 0) put_flag(rws, l0, WS_EPOCH, tag);
    }
    wait_flags(rws, fo1, tagv, ~tagv);
    w2_read(0);
    STAMP_AT(4);
    LBAR();
    asm volatile("mk_code_end2:");
    if (ni) CONV_SETUP(); else LOAD_U();
    if (w == 3) LOAD_TC();
  }
#define LATE_SETUP() do { const int tja = ((224 * w) >> 7) + p4, tjb = (224 * w + 223) >> 7; \
    fo2 = WS_FH + ((tm >> 1) * 14 + (tja < tjb ? tja : tjb)) * FLAG_STRIDE + 4 * hl; hb = WS_H + (u32)((4 * tm * 56 + 7 * w) * 1024) + l16; KEEP(fo2); KEEP(hb); } while (0)
  v4u hf[7];
  u32 hbm;
#define MROT (2 * tn)
  const int mrot = MROT;
  auto load_h = [&](int m, int s) { if (s == 0) hbm = hb + (u32)(((m + mrot) & 3) * 57344); hf[s] = ld_sc1(rws, hbm, ksh[s]); };
  f4 yacc[4];
  u32 a_x, o_y;
  if (!ni) {
    CONV_SETUP();
    const int rho = (15 - r16) & 3;
    u32 a_A = L_HC + ((mi >> 1) * 4 + rho) * HC_STRIDE + (r16 + 1 + rho + 8 * g) * 2 + (mi & 1) * 256;
    u32 a_B = L_U + (mi >> 1) * U_STRIDE + (240 - 16 * r16 + 8 * g) * 2 + (mi & 1) * 256;
    u32 a_u = L_U + ((tid >> 2) & 1) * U_STRIDE + (tid >> 3) * 64 + (tid & 3) * 16;
    KEEP(a_A); KEEP(a_B); KEEP(a_u);
    LATE_SETUP();
    WAITV(0);
    PIN(u0); PIN(u1);
    lw128(a_u, u0); lw128(a_u + 2048, u1);
    if (w == 3) { PIN(stv); *(LAS v2u*)lp(L_TC + lane * 8) = stv; }
    w2_read(1); w2_read(2); w2_read(3);
    v4u AD;
    { const u32 dv = (mi & 2) ? ddp >> 16 : ddp & 0xffffu; const int pos = 15 - r16 - 8 * g;
#pragma unroll
      for (int j = 0; j < 4; ++j) AD[j] = (pos >> 1) == j ? dv << ((pos & 1) * 16) : 0u; }
    u32 a_B0 = a_B - (mi & 1) * 256;
    a_x = L_X + w4k + l16;
    o_y = s_cp + WS_YT + (mi >> 1) * 4096 + (mi & 1) * 512 + r16 * 32 + hl * 16 + (g & 1) * ((mi & 1) ? 512u : 1536u);
    KEEP(a_x); KEEP(a_B0); KEEP(o_y);
    LBAR();
    STAMP_AT(5);
    u32 pv2;
    auto conv = [&](auto ROLE) {
      constexpr int role = decltype(ROLE)::value;
      constexpr ConvList CL = make_list(role);
      f4 ac[8];
      v4u fa[4][8], fb[4][8], fd[4];
      auto rd = [&](auto N) {
        constexpr int n = decltype(N)::value, kl = n / 36;
        if constexpr (n < 144) {
          if constexpr (CL.newA[n]) {
            const v2u lo = *(volatile LAS v2u*)lp(a_A + CL.kh[n] * 512 + kl * 64), hi = *(volatile LAS v2u*)lp(a_A + CL.kh[n] * 512 + kl * 64 + 8);
            fa[kl][CL.kh[n]] = v4u{lo.x, lo.y, hi.x, hi.y};
          }
          if constexpr (CL.newB[n]) fb[kl][CL.e[n]] = lr128(a_B + (7 - CL.e[n]) * 512 + kl * 64);
        } else if constexpr (n < 148) fd[n - 144] = lr128(a_B0 + (7 - TS[role][n - 144]) * 512);
      };
      static_for<CLA>(rd);
      static_for<144>([&](auto N) {
        constexpr int n = decltype(N)::value, kl = n / 36, T = CL.kh[n] + CL.e[n];
        ac[T] = mfma(fa[kl][CL.kh[n]], fb[kl][CL.e[n]], n < 8 ? f4{0, 0, 0, 0} : ac[T]);
        rd(std::integral_constant<int, n + CLA>{});
        if constexpr (n >= 9 && n < 9 + 12 * 5 && (n - 9) % 5 == 0) { constexpr int q = (n - 9) / 5; W2_PIECE(4 + q / 4, q % 4); }
        if constexpr (n == 80) LOAD_LAST();
        if constexpr (n == 100) ALD32(pv2, fo2, rws, " sc1");
        if constexpr (n >= 128 && n < 136 && (n & 1) == 0) lwf4(a_x + ((n - 128) / 2) * 1024, ac[TS[role ^ 1][(n - 128) / 2]]);
        if constexpr (n == 136) *(volatile LAS u32*)lp(L_CNT0 + 16 + w * 4) = 1u;
        SB();
      });
#pragma unroll
      for (int j = 0; j < 4; ++j) yacc[j] = mfma(AD, fd[j], ac[TS[role][j]]);
    };
    if (mi & 1) conv(std::integral_constant<int, 1>{}); else conv(std::integral_constant<int, 0>{});
    STAMP_AT(8);
    WAITV(0);
    PIN(pv2);
    wait_flags(rws, fo2, tagv, pv2);
  } else {
    LBAR();
    STAMP_AT(5);
    LATE_SETUP();
    WAITV(0);
    if (is_g1) arrive_publish(L_CNT0, rws, l0, WS_FH + (ti * 14 + tj) * FLAG_STRIDE, tag);
    w2_read(1); w2_read(2); w2_read(3);
    WAITL();
    W2_STAGE(4); W2_STAGE(5); W2_STAGE(6);
    LOAD_LAST();
    STAMP_AT(8);
    wait_flags(rws, fo2, tagv, ~tagv);
  }
  STAMP_AT(9);
#pragma unroll
  for (int s = 0; s < 7; ++s) load_h(0, s);
  SB();
  if (!ni) {
#pragma unroll 1
    for (u32 spins = 0; spins < SPIN_CAP && *(volatile LAS u32*)lp(L_CNT0 + 16 + (w ^ 1) * 4) == 0u; ++spins) __builtin_amdgcn_s_sleep(1);
    asm volatile("" ::: "memory");
#pragma unroll
    for (int j = 0; j < 4; ++j) yacc[j] += lrf4(a_x + ((mi & 1) ? -4096 : 4096) + j * 1024);
#pragma unroll
    for (int j = 0; j < 4; j += 2) {
      const auto r0 = __builtin_amdgcn_permlane16_swap(pk(yacc[j][0], yacc[j][1]), pk(yacc[j + 1][0], yacc[j + 1][1]), false, false);
      const auto r1 = __builtin_amdgcn_permlane16_swap(pk(yacc[j][2], yacc[j][3]), pk(yacc[j + 1][2], yacc[j + 1][3]), false, false);
      if (j == 0) st_sc1<0>(rws, o_y, v4u{r0[0], r1[0], r0[1], r1[1]});
      else st_sc1<2048>(rws, o_y, v4u{r0[0], r1[0], r0[1], r1[1]});
    }
  }
  w2_read(4); w2_read(5); w2_read(6);
  STAMP_AT(10);
  const int tok = tid >> 3, chunk = tid & 7;

  u32 fo3 = WS_FY + (32 * tn + 4 * w + p4) * FLAG_STRIDE + 4 * hl, o_yl = WS_YT + (u32)((64 * tn + tok) * 4096 + (64 * tm + 8 * chunk) * 2);
  u32 pp0 = w4k + (((lane & 48) | ((lane + 4 * hl) & 15)) << 4), yp = L_Y + (8 * chunk) * 128 + ((w ^ chunk) << 4) + (tok & 7) * 2;
  u32 rp = (((w >> 1) - mrot) & 3) * L_SLOT + (chunk >> 1) * 1024 + ((32 * (chunk & 1) + ((tok + 4 * (chunk & 3)) & 15)) << 4);
  u32 yr = L_Y + tok * 128 + ((chunk ^ w) << 4);
  KEEP(fo3); KEEP(o_yl); KEEP(pp0); KEEP(yp); KEEP(rp); KEEP(yr);
  PIN(xv);
  v4u b2v; v2u tc; u32 cdone;
  float xx[8], xn[8], bb[8];
  v4u yv;
  {
    u32 pv3 = 0;
    f4 acc[2][4];
#pragma unroll
    for (int m = 0; m < 4; ++m) {
#pragma unroll
      for (int s = 0; s < 7; ++s) {
#pragma unroll
        for (int t = 0; t < 4; ++t) acc[m & 1][t] = mfma(w2f[s][t], hf[s], s == 0 ? f4{0, 0, 0, 0} : acc[m & 1][t]);
        if (m < 3) load_h(m + 1, s);
        if (m == 0 && s == 5 && !ni) {
          SB();
          WAITV(6);
          arrive_publish(L_CNT0 + 4, rws, l0, WS_FY + cp * FLAG_STRIDE, tag);
        }
        if (m > 0 && s >= 1 && s <= 4) lwf4(((pp0 + (m - 1) * L_SLOT) ^ (((s - 1) & 1) * 128)) + (s - 1) * 1024, acc[(m - 1) & 1][s - 1]);
        if (m == 2 && s == 0) pv3 = get_flag(rws, fo3);
        if (m == 3 && s == 1) {
          STAMP_F(28);
          wait_flags(rws, fo3, tagv, pv3);
          STAMP_F(29);
          yv = ld_sc1(rws, o_yl);
        }
        if (m == 3 && s == 2) { b2v = lr128(L_B2 + chunk * 16); tc = *(LAS v2u*)lp(L_TC + tok * 8); cdone = *(volatile LAS u32*)lp(L_CNT0 + 4); }
        if (m == 3 && s >= 3) {
          const int j = s - 3;
          xx[2 * j] = bl(xv[j]); xx[2 * j + 1] = bh(xv[j]); bb[2 * j] = bl(b2v[j]); bb[2 * j + 1] = bh(b2v[j]);
          xn[2 * j] = xx[2 * j] * asf(tc.x) + asf(tc.y); KEEP(xn[2 * j]);
          xn[2 * j + 1] = xx[2 * j + 1] * asf(tc.x) + asf(tc.y); KEEP(xn[2 * j + 1]);
          KEEP(xx[2 * j]); KEEP(xx[2 * j + 1]); KEEP(bb[2 * j]); KEEP(bb[2 * j + 1]);
        }
        SB();
      }
      STAMP_F(24 + m);
    }
    if (__builtin_amdgcn_readfirstlane(cdone) < 4u) {
#pragma unroll 1
      for (u32 spins = 0; spins < SPIN_CAP && *(volatile LAS u32*)lp(L_CNT0 + 4) < 4u; ++spins) __builtin_amdgcn_s_sleep(1);
    }
    asm volatile("" ::: "memory");
#pragma unroll
    for (int t = 0; t < 4; ++t) lwf4(((pp0 + 3 * L_SLOT) ^ ((t & 1) * 128)) + t * 1024, acc[1][t]);
  }
  STAMP_AT(11);
#pragma unroll
  for (int j = 0; j < 4; ++j) {
    *(LAS u16*)lp(yp + (2 * j) * 128) = (u16)(yv[j] & 0xffff);
    *(LAS u16*)lp(yp + (2 * j + 1) * 128) = (u16)(yv[j] >> 16);
  }
  LBAR();
  STAMP_AT(12);
  {
    const v4u yy = lr128(yr);
    const f4 o0 = ((lrf4(rp) + lrf4(rp + 4096)) + (lrf4(rp + 8192) + lrf4(rp + 12288))) +
                  ((lrf4(rp + 16384) + lrf4(rp + 20480)) + (lrf4(rp + 24576) + lrf4(rp + 28672)));
    const f4 o1 = ((lrf4(rp + 256) + lrf4(rp + 4352)) + (lrf4(rp + 8448) + lrf4(rp + 12544))) +
                  ((lrf4(rp + 16640) + lrf4(rp + 20736)) + (lrf4(rp + 24832) + lrf4(rp + 28928)));
    float res[8];
#pragma unroll
    for (int j = 0; j < 8; ++j) {
      const float ov = (j < 4 ? o0[j & 3] : o1[j & 3]) + bb[j];
      const float sg = __builtin_amdgcn_rcpf(1.0f + __builtin_amdgcn_exp2f(ov * -1.4426950409f));
      const float yj = (j & 1) ? bh(yy[j >> 1]) : bl(yy[j >> 1]);
      res[j] = xx[j] + ((xn[j] * yj) * ov) * sg;
    }
#define OUT_AUX 16
    __builtin_amdgcn_raw_buffer_store_b128(v4u{pk(res[0], res[1]), pk(res[2], res[3]), pk(res[4], res[5]), pk(res[6], res[7])}, mk_srd(p.out, S * D * 2), o_out, 0, OUT_AUX);
  }
  asm volatile("mk_code_end:");
}
}

extern "C" void kernel_launch(void* const* d_in, const int* in_sizes, int n_in, void* d_out, int out_size, void* d_ws, size_t ws_size,
                              hipStream_t stream) {
  using namespace mk;
  Params p;
  p.x = (const u16*)d_in[0]; p.k = (const u16*)d_in[1]; p.v = (const u16*)d_in[2]; p.sk = (const u16*)d_in[3];
  p.ssm = (const u16*)d_in[4]; p.dd = (const u16*)d_in[5]; p.w1 = (const u16*)d_in[7]; p.b1 = (const u16*)d_in[8];
  p.w2 = (const u16*)d_in[9]; p.b2 = (const u16*)d_in[10];
  p.out = (u16*)d_out; p.ws = (unsigned char*)d_ws;
  (void)hipFuncSetAttribute((const void*)fused, hipFuncAttributeMaxDynamicSharedMemorySize, LDS_BYTES);
  fused<<<256, 512, LDS_BYTES, stream>>>(p);
}
```

```cpp
#include <hip/hip_runtime.h>
#include <stdint.h>
#define NO_T0
#include <type_traits>
#include <utility>

namespace mk {
typedef unsigned short u16;
typedef unsigned u32;
typedef unsigned long long u64;
typedef u32 v4u __attribute__((ext_vector_type(4)));
typedef u32 v2u __attribute__((ext_vector_type(2)));
typedef float f4 __attribute__((ext_vector_type(4)));
typedef float f2 __attribute__((ext_vector_type(2)));
typedef float f16v __attribute__((ext_vector_type(16)));
typedef short s8v __attribute__((ext_vector_type(8)));
typedef short s4v __attribute__((ext_vector_type(4)));
typedef __bf16 bf2 __attribute__((ext_vector_type(2)));
#define GAS __attribute__((address_space(1)))
#define LAS __attribute__((address_space(3)))
#define CAS __attribute__((address_space(4)))
#define DI __device__ __forceinline__
typedef __amdgpu_buffer_rsrc_t srd_t;
typedef LAS unsigned char* lptr;

constexpr int S = 2048, D = 512, F = 1792;
constexpr float EPS = 1e-5f;
constexpr u32 WS_EPOCH = 0, WS_FKV = 4096, WS_FST = 36864, WS_FH = 40960, WS_FY = 69632, WS_STATS = 131072, WS_CSUM = 196608,
              WS_KVT = 1u << 20, WS_YT = 3u << 20, WS_H = 5u << 20, WS_BYTES = 13u << 20;
constexpr u32 FLAG_STRIDE = 128;
constexpr u32 L_SLOT = 32768;
constexpr u32 L_HC = 98304, HC_STRIDE = 4160, L_U = L_HC + 8 * HC_STRIDE, U_STRIDE = 4608;
constexpr u32 L_PK = L_U + 2 * U_STRIDE, L_PV = L_PK + 72 * 128, L_PW = L_PV + 64 * 128;
constexpr u32 L_JUNK = L_PW + 1024, L_CNT0 = L_JUNK + 256, L_B2 = L_CNT0 + 256, L_TC = L_B2 + 256;
constexpr u32 L_Y = L_PK, L_X = L_PK;
constexpr u32 LDS_BYTES = 163840;
static_assert(L_JUNK + 256 <= LDS_BYTES, "LDS map");
constexpr u32 SPIN_CAP = 1u << 16;

struct Params {
  const u16 *x, *k, *v, *sk, *ssm, *dd, *w1, *b1, *w2, *b2;
  u16* out;
  unsigned char* ws;
};

DI srd_t mk_srd(const void* p, u32 bytes) { return __builtin_amdgcn_make_buffer_rsrc((void*)p, (short)0, (int)bytes, 0x00020000); }
DI float asf(u32 d) { return __builtin_bit_cast(float, d); }
DI u32 asu(float f) { return __builtin_bit_cast(u32, f); }
DI bool all_lanes(bool c) { return __builtin_amdgcn_ballot_w64(!c) == 0; }
DI float bl(u32 d) { return asf(d << 16); }
DI float bh(u32 d) { return asf(d & 0xffff0000u); }
DI u32 pk(float a, float b) { f2 t = {a, b}; return __builtin_bit_cast(u32, __builtin_convertvector(t, bf2)); }
DI float rest(float v) { return v - bh(asu(v)); }
DI u32 pkt(float lo, float hi) { return __builtin_amdgcn_perm(asu(hi), asu(lo), 0x07060302u); }
DI f4 mfma(v4u a, v4u b, f4 c) { return __builtin_amdgcn_mfma_f32_16x16x32_bf16(__builtin_bit_cast(s8v, a), __builtin_bit_cast(s8v, b), c, 0, 0, 0); }
DI f16v mfma32(v4u a, v4u b, f16v c) { return __builtin_amdgcn_mfma_f32_32x32x16_bf16(__builtin_bit_cast(s8v, a), __builtin_bit_cast(s8v, b), c, 0, 0, 0); }
DI lptr lp(u32 a) { return (lptr)(uintptr_t)a; }
DI v2u trread(u32 a) { return __builtin_bit_cast(v2u, __builtin_amdgcn_ds_read_tr16_b64_v4i16((LAS s4v*)lp(a))); }
DI v4u trfrag(u32 a, u32 hi_off) { const v2u lo = trread(a), hi = trread(a + hi_off); return v4u{lo.x, lo.y, hi.x, hi.y}; }
DI v4u lr128(u32 a) { return *(LAS v4u*)lp(a); }
DI f4 lrf4(u32 a) { return *(LAS f4*)lp(a); }
DI void lw128(u32 a, v4u v) { *(LAS v4u*)lp(a) = v; }
DI void lwf4(u32 a, f4 v) { *(LAS f4*)lp(a) = v; }
#define DMA_(OP, FL, rs, voff, sb, SI, lb, LI) do { u32 t_; \
  asm volatile("s_add_u32 m0, %1, %2\n\ts_add_u32 %0, %3, %4\n\t" OP " %5, %6, %0 offen" FL " lds" \
               : "=&s"(t_) : "s"(lb), "i"(LI), "s"(sb), "i"(SI), "v"(voff), "s"(rs) : "memory", "scc"); } while (0)
#define DFL ""
#define DMA(rs, voff, sb, SI, lb, LI) DMA_("buffer_load_dwordx4", DFL, rs, voff, sb, SI, lb, LI)
#define SSM_FL " nt"
#define DMANT(rs, voff, sb, SI, lb, LI) DMA_("buffer_load_dwordx4", " nt", rs, voff, sb, SI, lb, LI)
#define M0SET(lb, LI) asm volatile("s_add_u32 m0, %0, %1\n\ts_nop 0" ::"s"(lb), "i"(LI) : "memory", "scc")
#define DML(rs, voff, so, OFF) asm volatile("buffer_load_dwordx4 %0, %1, %2 offen offset:%3 lds" ::"v"(voff), "s"(rs), "s"(so), "i"(OFF) : "memory")
#define TOUCH(rs, voff, sb, SI) DMA_("buffer_load_dword", "", rs, voff, sb, SI, 0u, L_JUNK)
#define ALD128(d, off, rs, FL) asm volatile("buffer_load_dwordx4 %0, %1, %2, 0 offen" FL : "=v"(d) : "v"(off), "s"(rs) : "memory")
#define ALD128S(d, off, rs, so, FL) asm volatile("buffer_load_dwordx4 %0, %1, %2, %3 offen" FL : "=v"(d) : "v"(off), "s"(rs), "s"(so) : "memory")
#define ALD64(d, off, rs, FL) asm volatile("buffer_load_dwordx2 %0, %1, %2, 0 offen" FL : "=v"(d) : "v"(off), "s"(rs) : "memory")
#define ALD32(d, off, rs, FL) asm volatile("buffer_load_dword %0, %1, %2, 0 offen" FL : "=v"(d) : "v"(off), "s"(rs) : "memory")
#define ALD16(d, off, rs, FL) asm volatile("buffer_load_ushort %0, %1, %2, 0 offen" FL : "=v"(d) : "v"(off), "s"(rs) : "memory")
#define WAITV(n) asm volatile("s_waitcnt vmcnt(" #n ")" ::: "memory")
#define PIN(a) asm volatile("" : "+v"(a)::"memory")
#define KEEP(a) asm volatile("" : "+v"(a))
#define KEEPP(a) asm volatile("" : "+s"(a))
#define KEEPS(a) do { a = __builtin_amdgcn_readfirstlane(a); asm volatile("" : "+s"(a)); } while (0)
#define WAITL() asm volatile("s_waitcnt lgkmcnt(0)" ::: "memory")
#define BAR() __builtin_amdgcn_s_barrier()
#define VBAR(n) do { __builtin_amdgcn_s_waitcnt(0xC07F); asm volatile("s_waitcnt vmcnt(" #n ") lgkmcnt(0)\n\ts_barrier" ::: "memory"); } while (0)
#define LBAR() do { __builtin_amdgcn_s_waitcnt(0xC07F); asm volatile("s_waitcnt lgkmcnt(0)\n\ts_barrier" ::: "memory"); } while (0)
#define SB() __builtin_amdgcn_sched_barrier(0)
DI void wave_sum2(float& a, float& b) {
  const auto x = __builtin_amdgcn_permlane32_swap(asu(a), asu(b), false, false);
  float v = asf(x[0]) + asf(x[1]);
  const auto y = __builtin_amdgcn_permlane16_swap(asu(v), asu(v), false, false);
  v = asf(y[0]) + asf(y[1]);
  v += asf(__builtin_amdgcn_update_dpp(0u, asu(v), 0xB1, 0xF, 0xF, true));
  v += asf(__builtin_amdgcn_update_dpp(0u, asu(v), 0x4E, 0xF, 0xF, true));
  v += asf(__builtin_amdgcn_update_dpp(0u, asu(v), 0x141, 0xF, 0xF, true));
  v += asf(__builtin_amdgcn_update_dpp(0u, asu(v), 0x140, 0xF, 0xF, true));
  const auto z = __builtin_amdgcn_permlane32_swap(asu(v), asu(v), false, false);
  a = asf(z[0]); b = asf(z[1]);
}
DI v4u ld_sc1(srd_t rs, u32 off, u32 soff = 0) { return __builtin_amdgcn_raw_buffer_load_b128(rs, off, soff, 16); }
template <int IMM = 0> DI void st_sc1(srd_t rs, u32 off, v4u v, u32 soff = 0) {
  asm volatile("buffer_store_dwordx4 %0, %1, %2, %3 offen offset:%4 sc1\n\ts_nop 1" ::"v"(v), "v"(off), "s"(rs), "s"(soff), "i"(IMM) : "memory");
}
DI void put_flag(srd_t rs, u32 l0, u32 off, u32 tagv) { __builtin_amdgcn_raw_buffer_store_b32(tagv, rs, l0, off, 16); }
DI u32 get_flag(srd_t rs, u32 off) { return __builtin_amdgcn_raw_buffer_load_b32(rs, off, 0, 16); }
DI void wait_flags(srd_t rs, u32 off, u32 tagv, u32 v) {
  if (!all_lanes(v == tagv)) {
#pragma unroll 1
    for (u32 spins = 0; spins < SPIN_CAP; ++spins) {
      u32 x;
      asm volatile("buffer_load_dword %0, %1, %2, 0 offen sc1\n\ts_waitcnt vmcnt(0)" : "=&v"(x) : "v"(off), "s"(rs) : "memory");
      if (all_lanes(x == tagv)) break;
      __builtin_amdgcn_s_sleep(1);
    }
  }
  asm volatile("" ::: "memory");
}
DI void arrive_publish(u32 cnt, srd_t rs, u32 l0, u32 off, u32 tag) {
  u32 old;
  asm volatile("s_mov_b64 exec, 1\n\tds_add_rtn_u32 %0, %1, %2\n\ts_mov_b64 exec, -1\n\ts_waitcnt lgkmcnt(0)" : "=&v"(old) : "v"(cnt), "v"(1u) : "memory");
  if (__builtin_amdgcn_readfirstlane(old) == 3u) put_flag(rs, l0, off, tag);
}
#define STAMP_AT(i)
#define STAMP_F(i)
struct ConvList { int kh[144], e[144]; bool newA[144], newB[144]; };
constexpr int KEEP_MASK[2] = {0x99, 0x66};
constexpr int TS[2][4] = {{0, 3, 4, 7}, {1, 2, 5, 6}};
constexpr ConvList make_list(int role) {
  ConvList L = {};
  int n = 0;
  for (int kl = 0; kl < 4; ++kl) {
    bool seen[16] = {};
    for (int part = 0; part < (kl == 3 ? 2 : 1); ++part) {
      const int mask = kl < 3 ? 0xff : KEEP_MASK[part ? role : role ^ 1];
      for (int kh = 0; kh < 8; ++kh)
        for (int T = 7; T >= kh; --T) {
          if (!((mask >> T) & 1)) continue;
          const int e = T - kh;
          L.kh[n] = kh; L.e[n] = e; L.newA[n] = !seen[kh]; L.newB[n] = !seen[8 + e];
          seen[kh] = seen[8 + e] = true;
          ++n;
        }
    }
  }
  return L;
}
template <class F, int... I> DI void static_for_(F&& f, std::integer_sequence<int, I...>) { (f(std::integral_constant<int, I>{}), ...); }
template <int N, class F> DI void static_for(F&& f) { static_for_(f, std::make_integer_sequence<int, N>{}); }
#define CLA 10
#define ZX 1
#define ZW1 1
#define ZW2 1

__global__ void __launch_bounds__(512, 2) fused(Params p) {
  const int tid = __builtin_amdgcn_workitem_id_x(), lane = tid & 63, w = __builtin_amdgcn_readfirstlane(tid >> 6);
  const int b = __builtin_amdgcn_workgroup_id_x(), xcd = b & 7, idx = b >> 3;
  const int g = lane >> 4, r16 = lane & 15, q4 = r16 >> 2, p4 = lane & 3, rr = lane >> 3;
  STAMP_AT(0);
  if (w == 0) {
    u64 pc; u32 len;
    asm volatile("mk_code_begin:\n\ts_getpc_b64 %0\n\ts_mov_b32 %1, mk_code_end-mk_code_begin\n\ts_max_i32 %1, %1, mk_code_end2-mk_code_begin" : "=s"(pc), "=s"(len) :: "scc");
    const srd_t rc = mk_srd((const void*)pc, len);
    TOUCH(rc, (u32)(lane * 128), (u32)((idx & 3) * 8192), 0);
  }
  SB();
  const int cp = 32 * xcd + idx;
  const bool is_g1 = idx < 28;
  u32 w1k = w * 1024, w4k = w * 4096;
  const int stok = 256 * xcd + 8 * idx + w;
  const int sidx = idx & 3, ti = 4 * (xcd >> 1) + sidx, t1 = idx >> 2, tj = 7 * (xcd & 1) + t1;
  const int hm = 4 * (xcd >> 1) + sidx, cgp = 14 * (xcd & 1) + (hm < 14 ? hm : 0);
  const int tm = 4 * xcd + sidx, tn = t1 ^ 4;
  const int mi = w & 3, ni = w >> 2, hl = lane >> 5, l31 = lane & 31;
  const int m2 = w & 1, nq = w >> 1;
  const u32 chsw = (u32)(((lane & 7) ^ (rr & 6)) << 4);
  u32 xro = (u32)(stok * 1024 + lane * 16);
  u32 xvo = (u32)((128 * ti + 16 * w + rr) * 1024 + (((lane & 7) ^ g) << 4)), xvo1 = (xvo ^ 64) + 7168;
  u32 wvo = (u32)((8 * w + (is_g1 ? g : rr)) * 3584 + (is_g1 ? tj * 256 : cgp * 128)) + (is_g1 ? (u32)((r16 ^ (g << 2)) << 4) : chsw), wvo1 = wvo + 13312;
#define ROT1 ((4 * ((t1 + sidx) & 1) + xcd) & 7)
  u32 sox = (u32)(ROT1) * 128, sow = sox * 1792;
  u32 kvo = (u32)(rr * 1024 + (((lane & 7) ^ (rr & 2)) << 4)), kso = (u32)((64 * idx + 8 * w) * 1024 + xcd * 128);
  u32 l0 = l31 == 0 ? 4 * hl : 0x80000000u, l01 = lane < 2 ? lane * 16 : 0x80000000u;
  u32 s_st = WS_STATS + (u32)stok * 32;
  u32 xa[2][4], wa[2];
#pragma unroll
  for (int ks = 0; ks < 4; ++ks) { xa[0][ks] = (4 * m2 + (g & 1)) * 4096 + r16 * 128 + (((2 * ks + hl) ^ (r16 >> 1)) << 4); xa[1][ks] = xa[0][ks] + 2 * L_SLOT; }
  wa[0] = 2048 + hl * 4096 + q4 * 256 + (((4 * nq + 2 * (g & 1) + (p4 >> 1)) ^ (q4 << 2)) << 4) + 8 * (p4 & 1);
  wa[1] = wa[0] + 2 * L_SLOT;
  KEEP(xro); KEEP(xvo); KEEP(xvo1); KEEP(wvo); KEEP(wvo1); KEEPS(sox); KEEPS(sow); KEEPS(w1k); KEEPS(w4k);
  KEEP(l0); KEEP(l01); KEEPS(s_st); KEEP(kvo); KEEPS(kso); KEEP(wa[0]); KEEP(wa[1]);
#pragma unroll
  for (int i = 0; i < 4; ++i) { KEEP(xa[0][i]); KEEP(xa[1][i]); }
  SB();
  KEEPP(p.ws); KEEPP(p.out); KEEPP(p.w2); KEEPP(p.b2); KEEPP(p.k); KEEPP(p.x); KEEPP(p.w1);
  const srd_t rx = mk_srd(p.x, S * D * 2 * ZX), rw1 = mk_srd(p.w1 - 1024, D * F * 2 * ZW1 + 2048);
  v4u xr;
  const srd_t rws = mk_srd(p.ws, WS_BYTES);
  u32 tagv;
  ALD32(tagv, (u32)(WS_EPOCH + 4 * hl), rws, " sc1");
  ALD128(xr, xro, rx, "");
#define NEXT_CHUNK() do { sox = (sox + 128) & 1023; sow = sox * 1792; } while (0)
#define PIECE(c, i) do { \
    if ((i) == 0) { M0SET(w4k, ((c) % 3) * L_SLOT); DML(rx, xvo, sox, 0); } \
    if ((i) == 1) DML(rx, xvo1, sox, 1024); \
    if ((i) == 2) DML(rw1, wvo, sow, 2048); \
    if ((i) == 3) { DML(rw1, wvo1, sow, 3072); NEXT_CHUNK(); } } while (0)
#define HPIECE(c) DMA(rw1, wvo, 0u, (c) * 64 * 3584 + 2048, w1k, (c) * 8192)
  if (is_g1) { PIECE(0, 0); PIECE(0, 1); PIECE(0, 2); PIECE(0, 3); }
  else { HPIECE(0); HPIECE(1); HPIECE(2); HPIECE(3); }
  SB();
  u32 ddp = *(const CAS u32*)(p.dd + 2 * cp);
  v2u hq = *(const CAS v2u*)(p.ssm + cp * 4096 + ((w & 3) ? w * 512 - 4 : 0));
  SB();
#define STAGE_K() do { const srd_t rk = mk_srd(p.k, S * D * 2); DMANT(rk, kvo, kso, 0, w1k, L_PK + 1024); \
    if (w == 0) DMANT(rk, kvo + kso - 8192u, 0u, 0, 0u, L_PK); } while (0)
#define STAGE_V() do { const srd_t rv = mk_srd(p.v, S * D * 2); DMANT(rv, kvo, kso, 0, w1k, L_PV); \
    if (w == 1) { const srd_t rsk = mk_srd(p.sk, D * 4 * 2); DMA(rsk, (u32)(lane * 16), (u32)(xcd * 512), 0, 0u, L_PW); } \
    if (w == 2) { const srd_t rb2 = mk_srd(p.b2, D * 2); DMA_("buffer_load_dword", "", rb2, (u32)(lane * 4), (u32)(tn * 128), 0, 0u, L_B2); } } while (0)
  constexpr u32 TR_HI = 1024;
  const srd_t rw2 = mk_srd(p.w2, F * D * 2 * ZW2), rssm = mk_srd(p.ssm + cp * 4096, 8192);
  const u32 l16 = lane * 16;
  u32 ksh[7];
  { int t = xcd;
#pragma unroll
    for (int s = 0; s < 7; ++s) { t = t >= 7 ? t - 7 : t; ksh[s] = (u32)t * 1024; KEEPS(ksh[s]); ++t; } }
  u32 w2base = (u32)(224 * w * 1024 + tn * 128), s_cp = (u32)cp * 8192, s_t0 = (u32)((224 * t1 + 8 * sidx) * 1024) + (ksh[0] << 5);
  u32 a_kv = L_PK + 512 + ni * 4096 + (8 * g + q4) * 128 + (((2 * mi + (p4 >> 1)) ^ (q4 & 2)) << 4) + 8 * (p4 & 1);
  u32 a_tap = L_PW + mi * 128 + r16 * 8;
  u32 o_kv = (u32)((r16 >> 1) * 8192 + (r16 & 1) * 64 + (3 - g) * 16);
  u32 s_kv = WS_KVT + (u32)((32 * xcd + 8 * mi) * 8192 + (63 - 2 * idx - ni) * 128);
  u32 w2vo[4];
#pragma unroll
  for (int i = 0; i < 4; ++i) { w2vo[i] = (u32)(rr * 1024 + i * 7168) + chsw; KEEP(w2vo[i]); }
  KEEPS(w2base); KEEPS(s_cp); KEEPS(s_t0); KEEP(a_kv); KEEP(a_tap); KEEP(o_kv); KEEPS(s_kv);
  if (w == 2) lw128(L_U + hl * U_STRIDE + 4096 + l31 * 16, v4u{0, 0, 0, 0});
  if (w == 3) lw128(L_CNT0 + (lane & 1) * 16, v4u{0, 0, 0, 0});
  u32 fo1;
  { const int m = 2 * t1 + (lane & 1);
    const u32 fa = l31 * 8 + xcd, fb = (16 * (ti & 1) + r16) * 8 + (ti >> 1), fc = (WS_FST - WS_FKV) / FLAG_STRIDE + ((m >> 2) * 2 + (xcd & 1)) * 4 + (m & 3);
    fo1 = WS_FKV + ((is_g1 && w == 7) ? ((lane & 16) ? fc : fb) : fa) * FLAG_STRIDE + 4 * hl; }
  u32 t16 = tid * 16, o_out = (u32)((64 * tm + 8 * w) * 1024 + 128 * tn) + rr * 1024 + (lane & 7) * 16;
  u32 o_xe = WS_STATS + (u32)((128 * ti + 64 * m2) * 32) + lane * 32, o_we = WS_CSUM + (u32)((128 * tj + 32 * nq) * 16) + l31 * 16;
  u32 ta[4];
  { const int trow = 16 * hl + 4 * (g & 1) + q4;
    ta[0] = L_SLOT + w4k + trow * 128 + (((p4 >> 1) ^ (trow & 6)) << 4) + 8 * (p4 & 1);
    KEEP(ta[0]);
    ta[1] = ta[0] ^ 32; ta[2] = ta[0] ^ 64; ta[3] = ta[0] ^ 96; }
  u32 a_hc = L_HC + ni * (4 * HC_STRIDE - 4096) + 32 + t16;
  u32 o_h = (u32)((g & 1) * 57344 + r16 * 16 + hl * 256), s_h = WS_H + (u32)(((8 * ti + 4 * m2) * 56 + 4 * tj + nq) * 1024);
  KEEP(fo1); KEEP(t16); KEEP(o_out); KEEP(o_xe); KEEP(o_we); KEEP(ta[1]); KEEP(ta[2]); KEEP(ta[3]); KEEP(a_hc); KEEP(o_h); KEEPS(s_h);
  auto make_kv = [&]() {
    const v2u tw = *(LAS v2u*)lp(a_tap);
    const v2u k0 = trread(a_kv), k1 = trread(a_kv + 512), k2 = trread(a_kv + 1024);
    const v2u v0 = trread(a_kv + (L_PV - L_PK - 512)), v1 = trread(a_kv + (L_PV - L_PK));
    const float kk[12] = {0.f, bh(k0.x), bl(k0.y), bh(k0.y), bl(k1.x), bh(k1.x), bl(k1.y), bh(k1.y), bl(k2.x), bh(k2.x), bl(k2.y), bh(k2.y)};
    const float vv[8] = {bl(v0.x), bh(v0.x), bl(v0.y), bh(v0.y), bl(v1.x), bh(v1.x), bl(v1.y), bh(v1.y)};
    const float w0 = bl(tw.x), w1 = bh(tw.x), w2 = bl(tw.y), w3 = bh(tw.y);
    float o[8];
#pragma unroll
    for (int j = 0; j < 8; j += 2)
      asm("v_mul_f32 %0, %2, %7\n\tv_mul_f32 %1, %3, %7\n\tv_fmac_f32 %0, %3, %8\n\tv_fmac_f32 %1, %4, %8\n\tv_fmac_f32 %0, %4, %9\n\tv_fmac_f32 %1, %5, %9\n\t"
          "v_fmac_f32 %0, %5, %10\n\tv_fmac_f32 %1, %6, %10\n\tv_mul_f32 %0, %0, %11\n\tv_mul_f32 %1, %1, %12"
          : "=&v"(o[j]), "=&v"(o[j + 1])
          : "v"(kk[j + 1]), "v"(kk[j + 2]), "v"(kk[j + 3]), "v"(kk[j + 4]), "v"(kk[j + 5]), "v"(w3), "v"(w2), "v"(w1), "v"(w0), "v"(vv[j]), "v"(vv[j + 1]));
    st_sc1(rws, o_kv, v4u{pk(o[7], o[6]), pk(o[5], o[4]), pk(o[3], o[2]), pk(o[1], o[0])}, s_kv);
  };
  SB();
  BAR();
  if (is_g1) { PIECE(1, 0); PIECE(1, 1); PIECE(1, 2); PIECE(1, 3); }
  else { HPIECE(4); HPIECE(5); HPIECE(6); HPIECE(7); }
  WAITV(8);
  PIN(xr); PIN(tagv);
  tagv += 0x9E3779B9u;
  STAMP_AT(1);
  {
    float s1 = 0.f, s2 = 0.f;
    u32 one2 = 0x3f803f80u;
    KEEP(one2);
#pragma unroll
    for (int j = 0; j < 4; ++j) {
      const u32 xj = xr[j];
      const bf2 v = __builtin_bit_cast(bf2, xj);
      s1 = __builtin_amdgcn_fdot2_f32_bf16(v, __builtin_bit_cast(bf2, one2), s1, false);
      s2 = __builtin_amdgcn_fdot2_f32_bf16(v, v, s2, false);
    }
    wave_sum2(s1, s2);
    const float mean = s1 * (1.0f / D), var = __builtin_fmaxf(s2 * (1.0f / D) - mean * mean, 0.f);
    const float iA = __builtin_amdgcn_sqrtf(var + EPS * (var + EPS));
    const float m0 = -mean, m1 = rest(m0), m2 = rest(m1);
    u32 d0 = pkt(m0, m0), d1 = pkt(m1, m1);
    const u32 mv = pk(mean, var);
    float rq = __builtin_amdgcn_rcpf(bl(pk(__builtin_amdgcn_sqrtf(bl(pk(bh(mv) + EPS, 0.f))), 0.f))), mq = -bl(mv) * rq;
    KEEP(d0); KEEP(d1); KEEP(rq); KEEP(mq);
    const bool l1 = lane & 1;
    st_sc1(rws, l01, v4u{l1 ? asu(rq) : d0, l1 ? asu(mq) : d1, pkt(m2, m0), pkt(iA, rest(iA))}, s_st);
  }
  SB();
  STAGE_K(); STAGE_V();
  v4u hd;
  ALD128(hd, t16, rssm, SSM_FL);
  SB();
  STAMP_AT(2);
  v4u w2f[7][4];
  u32 sw2;
#define W2SLOT(s) ((s) == 0 ? 3 : ((s) + 1) % 3)
#define W2_PIECE(s, i) do { if ((i) == 0) { sw2 = w2base + (ksh[s] << 5); M0SET(w4k, W2SLOT(s) * L_SLOT); } DML(rw2, w2vo[i], sw2, (i) * 1024); } while (0)
#define W2_STAGE(s) do { W2_PIECE(s, 0); W2_PIECE(s, 1); W2_PIECE(s, 2); W2_PIECE(s, 3); } while (0)
  v4u xv; v2u stv;
  v4u u0, u1;
#define LOAD_U() do { ALD128S(u0, t16, rws, s_cp + WS_KVT, " sc1"); ALD128S(u1, t16, rws, s_cp + WS_KVT + 4096, " sc1"); } while (0)
#define LOAD_LAST() ALD128(xv, o_out, rx, "")
#define LOAD_TC() ALD64(stv, WS_STATS + 16 + (64 * tm + lane) * 32, rws, " sc1")
  auto w2_read = [&](int s) {
#pragma unroll
    for (int t = 0; t < 4; ++t) w2f[s][t] = trfrag(ta[t] + W2SLOT(s) * L_SLOT - L_SLOT, TR_HI);
  };
  auto write_hc = [&](int rho) {
    const u32 p0 = __builtin_amdgcn_update_dpp(mi ? hq.x : 0u, hd.z, 0x138, 0xF, 0xF, false), p1 = __builtin_amdgcn_update_dpp(mi ? hq.y : 0u, hd.w, 0x138, 0xF, 0xF, false);
    const u32 a0 = __builtin_amdgcn_alignbit(p1, p0, 16), a1 = __builtin_amdgcn_alignbit(hd.x, p1, 16), a2 = __builtin_amdgcn_alignbit(hd.y, hd.x, 16),
              a3 = __builtin_amdgcn_alignbit(hd.z, hd.y, 16), a4 = __builtin_amdgcn_alignbit(hd.w, hd.z, 16);
    if (rho == 0) lw128(a_hc, hd);
    if (rho == 1) lw128(a_hc + HC_STRIDE, v4u{a1, a2, a3, a4});
    if (rho == 2) lw128(a_hc + 2 * HC_STRIDE, v4u{p1, hd.x, hd.y, hd.z});
    if (rho == 3) lw128(a_hc + 3 * HC_STRIDE, v4u{a0, a1, a2, a3});
  };

#define CONV_SETUP() do { PIN(hd); write_hc(0); write_hc(1); write_hc(2); write_hc(3); \
    if (w == 4) lw128(L_HC + rr * HC_STRIDE + (lane & 1) * 16, v4u{0, 0, 0, 0}); } while (0)

  if (__builtin_expect(is_g1, 1)) {
    f16v acc[2];
#pragma unroll
    for (int i = 0; i < 16; ++i) { acc[0][i] = 0.f; acc[1][i] = 0.f; }
    v4u xf[2][2][2], wf[2][2];
#define REQ(dc, i) do { if ((dc) >= 8) W2_PIECE((dc) & 3, i); else PIECE(dc, i); } while (0)
#define STEP(st, dc, d0, nd) do { \
      constexpr int hs_ = (st) & 1, hn_ = hs_ ^ 1, sl_ = (((st) + 1) >> 1) % 3, sb_ = sl_ >> 1; constexpr u32 so_ = (sl_ & 1) * L_SLOT; \
      constexpr bool rd_ = (st) < 15, mm_ = (st) >= 0; \
      _Pragma("unroll") for (int i_ = 0; i_ < 4; ++i_) { \
        if (mm_) acc[i_ & 1] = mfma32(wf[hs_][i_ >> 1], xf[hs_][i_ >> 1][i_ & 1], acc[i_ & 1]); \
        if (rd_) { \
          if (!(i_ & 1)) wf[hn_][i_ >> 1] = trfrag(wa[sb_] + so_ + (2 * hn_ + (i_ >> 1)) * 8192, 1024); \
          else { xf[hn_][i_ >> 1][0] = lr128(xa[sb_][2 * hn_ + (i_ >> 1)] + so_); xf[hn_][i_ >> 1][1] = lr128(xa[sb_][2 * hn_ + (i_ >> 1)] + so_ + 8192); } } \
        if ((nd) == 4) { if (i_ == 0) REQ(dc, 0); if (i_ == 1) REQ(dc, 1); if (i_ == 2) REQ(dc, 2); if (i_ == 3) REQ(dc, 3); } \
        if ((nd) == 2) { if (i_ == 0) REQ(dc, d0); if (i_ == 2) REQ(dc, (d0) + 1); } \
        SB(); } } while (0)
#define ITER(c) do { STAMP_F(16 + (c)); STEP(2 * (c) + 1, (c) + 3, 0, 2); STEP(2 * (c) + 2, (c) + 3, 2, 2); } while (0)
    u32 pv; v4u xe, we;
    VBAR(8);
    STAMP_AT(15);
    STEP(-1, 0, 0, 0);
    STEP(0, 2, 0, 4);
    VBAR(8);
    ITER(0);
    VBAR(4);
    STAMP_F(30);
    make_kv();
    SB();
    ITER(1); W2_PIECE(0, 0); W2_PIECE(0, 1);
    VBAR(6);
    if (w == 0) put_flag(rws, l0, WS_FKV + b * FLAG_STRIDE, tagv);
    ITER(2); M0SET(w4k, 3 * L_SLOT); W2_PIECE(0, 2); W2_PIECE(0, 3);
    VBAR(8);
    ITER(3);
    VBAR(6);
    ITER(4);
    if (w >= 6) { ALD32(pv, fo1, rws, " sc1"); WAITV(5); } else WAITV(4);
    LBAR();
    STAMP_F(21);
    STEP(11, 9, 0, 2); STEP(12, 9, 2, 2);
    WAITV(4);
    if (w >= 6) { PIN(pv); wait_flags(rws, fo1, tagv, pv); }
    LBAR();
    STAMP_F(22);
    ALD128(xe, o_xe, rws, " sc1");
    ALD128(we, o_we, rws, " sc1");
    if (!ni) LOAD_U();
    if (w == 3) LOAD_TC();
    STEP(13, 0, 0, 0); w2_read(0); STEP(14, 10, 0, 2);
    LBAR();
    STAMP_F(23);
    STEP(15, 10, 2, 2);
    if (ni) WAITV(4); else WAITV(6);
    PIN(xe); PIN(we);
    {
      const v4u z = {0, 0, 0, 0};
      acc[0] = mfma32(we, hl ? z : xe, acc[0]);
      acc[1] = mfma32(we, hl ? xe : z, acc[1]);
    }
    STAMP_AT(3);
    if (ni) { CONV_SETUP(); SB(); }
    f2 k1v[2], k3v[2], iAv[2];
    { const auto r = __builtin_amdgcn_permlane32_swap(xe.w, xe.w, false, false);
#pragma unroll
      for (int m = 0; m < 2; ++m) {
        const float iA = bl(r[m]) + bh(r[m]), A = __builtin_amdgcn_rcpf(iA), k1s = -2.3022082f * A, k3s = -0.10294324f * A * A * A;
        k1v[m] = f2{k1s, k1s}; k3v[m] = f2{k3s, k3s}; iAv[m] = f2{iA, iA};
      } }
#pragma unroll
    for (int qi = 0; qi < 4; ++qi) {
      const int n = qi >> 1, sp = qi & 1;
      if (qi == 0) W2_PIECE(3, 0);
      if (qi == 1) W2_PIECE(3, 1);
      if (qi == 2) W2_PIECE(3, 2);
      if (qi == 3) W2_PIECE(3, 3);
      v4u o;
#pragma unroll
      for (int j = 0; j < 4; ++j) {
        const f2 a = {acc[n][8 * sp + 2 * j], acc[n][8 * sp + 2 * j + 1]}, k1 = k1v[n], k3 = k3v[n], iA = iAv[n];
        const f2 z = a * __builtin_elementwise_fma(a * a, k3, k1);
        const f2 e = {__builtin_amdgcn_exp2f(z.x), __builtin_amdgcn_exp2f(z.y)};
        const f2 d = __builtin_elementwise_fma(e, iA, iA);
        const f2 hv = a * f2{__builtin_amdgcn_rcpf(d.x), __builtin_amdgcn_rcpf(d.y)};
        o[j] = pk(hv.x, hv.y);
      }
      if (qi == 0) st_sc1<0>(rws, o_h, o, s_h);
      if (qi == 1) st_sc1<512>(rws, o_h, o, s_h);
      if (qi == 2) st_sc1<0>(rws, o_h, o, s_h + 114688);
      if (qi == 3) st_sc1<512>(rws, o_h, o, s_h + 114688);
      SB();
    }
    STAMP_AT(4);
  } else {
    u32 b1v;
    { const srd_t rb1 = mk_srd(p.b1 + 64 * cgp, 128); ALD16(b1v, (u32)(16 * mi + 4 * g + p4) * 2, rb1, ""); }
    VBAR(0);
    STAMP_AT(15);
    if (w < 4) {
      const v4u ones = {0x3f803f80u, 0x3f803f80u, 0x3f803f80u, 0x3f803f80u};
      f4 s0 = {0, 0, 0, 0};
      const u32 a = (w == 0 ? ta[0] : w == 1 ? ta[1] : w == 2 ? ta[2] : ta[3]) - L_SLOT - w4k;
#pragma unroll
      for (int st = 0; st < 16; ++st) s0 = mfma(trfrag(a + st * 4096, TR_HI), ones, s0);
      const float ca = (p4 & 1) ? s0[1] : s0[0], cb = (p4 & 1) ? s0[3] : s0[2], c = (p4 & 2) ? cb : ca;
      const float c1 = rest(c), c2 = rest(c1);
      PIN(b1v);
      st_sc1(rws, r16 < 4 && hm < 14 ? WS_CSUM + (u32)(64 * cgp + 16 * w + 4 * g + p4) * 16 : 0x80000000u, v4u{pkt(c, c1), pkt(c, c1), pkt(c, c2), b1v * 0x10001u});
    }
    make_kv();
    VBAR(0);
    if (w == 0) { put_flag(rws, l0, WS_FST + (4 * xcd + sidx) * FLAG_STRIDE, tagv); put_flag(rws, l0, WS_FKV + b * FLAG_STRIDE, tagv); }
    STAMP_AT(3);
    W2_STAGE(0); W2_STAGE(1); W2_STAGE(2); W2_STAGE(3);
    if (b == 255) {
      wait_flags(rws, WS_FKV + (32 * w + l31) * FLAG_STRIDE + 4 * hl, tagv, ~tagv);
      BAR();
      if (w == 0) put_flag(rws, l0, WS_EPOCH, tagv);
    }
    wait_flags(rws, fo1, tagv, ~tagv);
    w2_read(0);
    STAMP_AT(4);
    LBAR();
    asm volatile("mk_code_end2:");
    if (ni) CONV_SETUP(); else LOAD_U();
    if (w == 3) LOAD_TC();
  }
  u32 fo2, hb;
#define LATE_SETUP() do { const int tja = ((224 * w) >> 7) + (rr & 3), tjb = (224 * w + 223) >> 7; \
    fo2 = WS_FH + ((tm >> 1) * 14 + (tja < tjb ? tja : tjb)) * FLAG_STRIDE + 4 * hl; hb = WS_H + (u32)((4 * tm * 56 + 7 * w) * 1024) + l16; KEEP(fo2); KEEP(hb); } while (0)
  v4u hf[7];
  u32 hbm;
#define MROT (2 * tn)
  const int mrot = MROT;
  auto load_h = [&](int m, int s) { if (s == 0) hbm = hb + (u32)(((m + mrot) & 3) * 57344); hf[s] = ld_sc1(rws, hbm, ksh[s]); };
  f4 yacc[4];
  u32 a_x, o_y;
  if (!ni) {
    CONV_SETUP();
    const int rho = (15 - r16) & 3;
    u32 a_A = L_HC + ((mi >> 1) * 4 + rho) * HC_STRIDE + (r16 + 1 + rho + 8 * g) * 2 + (mi & 1) * 256;
    u32 a_B = L_U + (mi >> 1) * U_STRIDE + (240 - 16 * r16 + 8 * g) * 2 + (mi & 1) * 256;
    u32 a_u = L_U + ((tid >> 2) & 1) * U_STRIDE + (tid >> 3) * 64 + (tid & 3) * 16;
    KEEP(a_A); KEEP(a_B); KEEP(a_u);
    LATE_SETUP();
    WAITV(0);
    PIN(u0); PIN(u1);
    lw128(a_u, u0); lw128(a_u + 2048, u1);
    if (w == 3) { PIN(stv); *(LAS v2u*)lp(L_TC + lane * 8) = stv; }
    w2_read(1); w2_read(2); w2_read(3);
    v4u AD;
    { const u32 dv = (mi & 2) ? ddp >> 16 : ddp & 0xffffu; const int pos = 15 - r16 - 8 * g;
#pragma unroll
      for (int j = 0; j < 4; ++j) AD[j] = (pos >> 1) == j ? dv << ((pos & 1) * 16) : 0u; }
    u32 a_B0 = a_B - (mi & 1) * 256;
    a_x = L_X + w4k + l16;
    o_y = s_cp + WS_YT + (mi >> 1) * 4096 + (mi & 1) * 512 + r16 * 32 + hl * 16 + (g & 1) * ((mi & 1) ? 512u : 1536u);
    KEEP(a_x); KEEP(a_B0); KEEP(o_y);
    LBAR();
    STAMP_AT(5);
    u32 pv2;
    auto conv = [&](auto ROLE) {
      constexpr int role = decltype(ROLE)::value;
      constexpr ConvList CL = make_list(role);
      f4 ac[8];
      v4u fa[4][8], fb[4][8], fd[4];
      auto rd = [&](auto N) {
        constexpr int n = decltype(N)::value, kl = n / 36;
        if constexpr (n < 144) {
          if constexpr (CL.newA[n]) {
            const v2u lo = *(volatile LAS v2u*)lp(a_A + CL.kh[n] * 512 + kl * 64), hi = *(volatile LAS v2u*)lp(a_A + CL.kh[n] * 512 + kl * 64 + 8);
            fa[kl][CL.kh[n]] = v4u{lo.x, lo.y, hi.x, hi.y};
          }
          if constexpr (CL.newB[n]) fb[kl][CL.e[n]] = lr128(a_B + (7 - CL.e[n]) * 512 + kl * 64);
        } else if constexpr (n < 148) fd[n - 144] = lr128(a_B0 + (7 - TS[role][n - 144]) * 512);
      };
      static_for<CLA>(rd);
      static_for<144>([&](auto N) {
        constexpr int n = decltype(N)::value, kl = n / 36, T = CL.kh[n] + CL.e[n];
        ac[T] = mfma(fa[kl][CL.kh[n]], fb[kl][CL.e[n]], n < 8 ? f4{0, 0, 0, 0} : ac[T]);
        rd(std::integral_constant<int, n + CLA>{});
        if constexpr (n >= 9 && n < 9 + 12 * 5 && (n - 9) % 5 == 0) { constexpr int q = (n - 9) / 5; W2_PIECE(4 + q / 4, q % 4); }
        if constexpr (n == 80) LOAD_LAST();
        if constexpr (n == 100) ALD32(pv2, fo2, rws, " sc1");
        if constexpr (n >= 128 && n < 136 && (n & 1) == 0) lwf4(a_x + ((n - 128) / 2) * 1024, ac[TS[role ^ 1][(n - 128) / 2]]);
        if constexpr (n == 136) { asm volatile("" ::: "memory"); *(volatile LAS u32*)lp(L_CNT0 + 16 + w * 4) = 1u; }
        SB();
      });
#pragma unroll
      for (int j = 0; j < 4; ++j) yacc[j] = mfma(AD, fd[j], ac[TS[role][j]]);
    };
    if (mi & 1) conv(std::integral_constant<int, 1>{}); else conv(std::integral_constant<int, 0>{});
    STAMP_AT(8);
    WAITV(0);
    PIN(pv2);
    wait_flags(rws, fo2, tagv, pv2);
  } else {
    LBAR();
    STAMP_AT(5);
    LATE_SETUP();
    WAITV(0);
    if (is_g1) arrive_publish(L_CNT0, rws, l0, WS_FH + (ti * 14 + tj) * FLAG_STRIDE, tagv);
    w2_read(1); w2_read(2); w2_read(3);
    WAITL();
    W2_STAGE(4); W2_STAGE(5); W2_STAGE(6);
    LOAD_LAST();
    STAMP_AT(8);
    wait_flags(rws, fo2, tagv, ~tagv);
  }
  STAMP_AT(9);
#pragma unroll
  for (int s = 0; s < 7; ++s) load_h(0, s);
  SB();
  if (!ni) {
#pragma unroll 1
    for (u32 spins = 0; spins < SPIN_CAP && *(volatile LAS u32*)lp(L_CNT0 + 16 + (w ^ 1) * 4) == 0u; ++spins) __builtin_amdgcn_s_sleep(1);
    asm volatile("" ::: "memory");
#pragma unroll
    for (int j = 0; j < 4; ++j) yacc[j] += lrf4(a_x + ((mi & 1) ? -4096 : 4096) + j * 1024);
#pragma unroll
    for (int j = 0; j < 4; j += 2) {
      const auto r0 = __builtin_amdgcn_permlane16_swap(pk(yacc[j][0], yacc[j][1]), pk(yacc[j + 1][0], yacc[j + 1][1]), false, false);
      const auto r1 = __builtin_amdgcn_permlane16_swap(pk(yacc[j][2], yacc[j][3]), pk(yacc[j + 1][2], yacc[j + 1][3]), false, false);
      if (j == 0) st_sc1<0>(rws, o_y, v4u{r0[0], r1[0], r0[1], r1[1]});
      else st_sc1<2048>(rws, o_y, v4u{r0[0], r1[0], r0[1], r1[1]});
    }
  }
  w2_read(4); w2_read(5); w2_read(6);
  STAMP_AT(10);
  const int tok = tid >> 3, chunk = tid & 7;

  u32 fo3 = WS_FY + (32 * tn + 4 * w + (rr & 3)) * FLAG_STRIDE + 4 * hl, o_yl = WS_YT + (u32)((64 * tn + tok) * 4096 + (64 * tm + 8 * chunk) * 2);
  u32 pp0 = w4k + (((lane & 48) | ((lane + 4 * hl) & 15)) << 4), yp = L_Y + (8 * chunk) * 128 + ((w ^ chunk) << 4) + (tok & 7) * 2;
  u32 rp = (((w >> 1) - mrot) & 3) * L_SLOT + (chunk >> 1) * 1024 + ((32 * (chunk & 1) + ((tok + 4 * (chunk & 3)) & 15)) << 4);
  u32 yr = L_Y + tok * 128 + ((chunk ^ w) << 4);
  KEEP(fo3); KEEP(o_yl); KEEP(pp0); KEEP(yp); KEEP(rp); KEEP(yr);
  PIN(xv);
  v4u b2v; v2u tc; u32 cdone;
  float xx[8], xn[8], bb[8];
  v4u yv;
  {
    u32 pv3 = 0;
    f4 acc[2][4];
#pragma unroll
    for (int m = 0; m < 4; ++m) {
#pragma unroll
      for (int s = 0; s < 7; ++s) {
#pragma unroll
        for (int t = 0; t < 4; ++t) acc[m & 1][t] = mfma(w2f[s][t], hf[s], s == 0 ? f4{0, 0, 0, 0} : acc[m & 1][t]);
        if (m < 3) load_h(m + 1, s);
        if (m == 0 && s == 5 && !ni) {
          SB();
          WAITV(6);
          arrive_publish(L_CNT0 + 4, rws, l0, WS_FY + cp * FLAG_STRIDE, tagv);
        }
        if (m > 0 && s >= 1 && s <= 4) lwf4(((pp0 + (m - 1) * L_SLOT) ^ (((s - 1) & 1) * 128)) + (s - 1) * 1024, acc[(m - 1) & 1][s - 1]);
        if (m == 2 && s == 0) pv3 = get_flag(rws, fo3);
        if (m == 3 && s == 1) {
          STAMP_F(28);
          wait_flags(rws, fo3, tagv, pv3);
          STAMP_F(29);
          yv = ld_sc1(rws, o_yl);
        }
        if (m == 3 && s == 2) { b2v = lr128(L_B2 + chunk * 16); tc = *(LAS v2u*)lp(L_TC + tok * 8); cdone = *(volatile LAS u32*)lp(L_CNT0 + 4); }
        if (m == 3 && s >= 3) {
          const int j = s - 3;
          xx[2 * j] = bl(xv[j]); xx[2 * j + 1] = bh(xv[j]); bb[2 * j] = bl(b2v[j]); bb[2 * j + 1] = bh(b2v[j]);
          xn[2 * j] = xx[2 * j] * asf(tc.x) + asf(tc.y); KEEP(xn[2 * j]);
          xn[2 * j + 1] = xx[2 * j + 1] * asf(tc.x) + asf(tc.y); KEEP(xn[2 * j + 1]);
          KEEP(xx[2 * j]); KEEP(xx[2 * j + 1]); KEEP(bb[2 * j]); KEEP(bb[2 * j + 1]);
        }
        SB();
      }
      STAMP_F(24 + m);
    }
    if (__builtin_amdgcn_readfirstlane(cdone) < 4u) {
#pragma unroll 1
      for (u32 spins = 0; spins < SPIN_CAP && *(volatile LAS u32*)lp(L_CNT0 + 4) < 4u; ++spins) __builtin_amdgcn_s_sleep(1);
    }
    asm volatile("" ::: "memory");
#pragma unroll
    for (int t = 0; t < 4; ++t) lwf4(((pp0 + 3 * L_SLOT) ^ ((t & 1) * 128)) + t * 1024, acc[1][t]);
  }
  STAMP_AT(11);
#pragma unroll
  for (int j = 0; j < 4; ++j) {
    *(LAS u16*)lp(yp + (2 * j) * 128) = (u16)(yv[j] & 0xffff);
    *(LAS u16*)lp(yp + (2 * j + 1) * 128) = (u16)(yv[j] >> 16);
  }
  LBAR();
  STAMP_AT(12);
  {
    const v4u yy = lr128(yr);
    const f4 o0 = ((lrf4(rp) + lrf4(rp + 4096)) + (lrf4(rp + 8192) + lrf4(rp + 12288))) +
                  ((lrf4(rp + 16384) + lrf4(rp + 20480)) + (lrf4(rp + 24576) + lrf4(rp + 28672)));
    const f4 o1 = ((lrf4(rp + 256) + lrf4(rp + 4352)) + (lrf4(rp + 8448) + lrf4(rp + 12544))) +
                  ((lrf4(rp + 16640) + lrf4(rp + 20736)) + (lrf4(rp + 24832) + lrf4(rp + 28928)));
    float res[8];
#pragma unroll
    for (int j = 0; j < 8; ++j) {
      const float ov = (j < 4 ? o0[j & 3] : o1[j & 3]) + bb[j];
      const float sg = __builtin_amdgcn_rcpf(1.0f + __builtin_amdgcn_exp2f(ov * -1.4426950409f));
      const float yj = (j & 1) ? bh(yy[j >> 1]) : bl(yy[j >> 1]);
      res[j] = xx[j] + ((xn[j] * yj) * ov) * sg;
    }
#define OUT_AUX 16
    __builtin_amdgcn_raw_buffer_store_b128(v4u{pk(res[0], res[1]), pk(res[2], res[3]), pk(res[4], res[5]), pk(res[6], res[7])}, mk_srd(p.out, S * D * 2), o_out, 0, OUT_AUX);
  }
  asm volatile("mk_code_end:");
}
}

extern "C" void kernel_launch(void* const* d_in, const int* in_sizes, int n_in, void* d_out, int out_size, void* d_ws, size_t ws_size,
                              hipStream_t stream) {
  using namespace mk;
  Params p;
  p.x = (const u16*)d_in[0]; p.k = (const u16*)d_in[1]; p.v = (const u16*)d_in[2]; p.sk = (const u16*)d_in[3];
  p.ssm = (const u16*)d_in[4]; p.dd = (const u16*)d_in[5]; p.w1 = (const u16*)d_in[7]; p.b1 = (const u16*)d_in[8];
  p.w2 = (const u16*)d_in[9]; p.b2 = (const u16*)d_in[10];
  p.out = (u16*)d_out; p.ws = (unsigned char*)d_ws;
  (void)hipFuncSetAttribute((const void*)fused, hipFuncAttributeMaxDynamicSharedMemorySize, LDS_BYTES);
  fused<<<256, 512, LDS_BYTES, stream>>>(p);
}
```

```cpp
#include <hip/hip_runtime.h>
#include <stdint.h>
#include <type_traits>
#include <utility>

namespace mk {
typedef unsigned short u16;
typedef unsigned u32;
typedef unsigned long long u64;
typedef u32 v4u __attribute__((ext_vector_type(4)));
typedef u32 v2u __attribute__((ext_vector_type(2)));
typedef float f4 __attribute__((ext_vector_type(4)));
typedef float f2 __attribute__((ext_vector_type(2)));
typedef float f16v __attribute__((ext_vector_type(16)));
typedef short s8v __attribute__((ext_vector_type(8)));
typedef short s4v __attribute__((ext_vector_type(4)));
typedef __bf16 bf2 __attribute__((ext_vector_type(2)));
#define LAS __attribute__((address_space(3)))
#define CAS __attribute__((address_space(4)))
#define DI __device__ __forceinline__
typedef __amdgpu_buffer_rsrc_t srd_t;
typedef LAS unsigned char* lptr;

constexpr int S = 2048, D = 512, F = 1792;
constexpr float EPS = 1e-5f;
constexpr u32 WS_EPOCH = 0, WS_FKV = 4096, WS_FST = 36864, WS_FH = 40960, WS_FY = 69632, WS_STATS = 131072, WS_CSUM = 196608,
              WS_KVT = 1u << 20, WS_YT = 3u << 20, WS_H = 5u << 20, WS_BYTES = 13u << 20;
constexpr u32 FLAG_STRIDE = 128;
constexpr u32 L_SLOT = 32768;
constexpr u32 L_HC = 98304, HC_STRIDE = 4160, L_U = L_HC + 8 * HC_STRIDE, U_STRIDE = 4608;
constexpr u32 L_PK = L_U + 2 * U_STRIDE, L_PV = L_PK + 72 * 128, L_PW = L_PV + 64 * 128;
constexpr u32 L_JUNK = L_PW + 1024, L_CNT0 = L_JUNK + 256, L_B2 = L_CNT0 + 256, L_TC = L_B2 + 256;
constexpr u32 L_Y = L_PK, L_X = L_PK;
constexpr u32 LDS_BYTES = 163840;
static_assert(L_TC + 512 <= LDS_BYTES, "LDS map");
constexpr u32 SPIN_CAP = 1u << 16;

struct Params {
  const u16 *x, *k, *v, *sk, *ssm, *dd, *w1, *b1, *w2, *b2;
  u16* out;
  unsigned char* ws;
};

DI srd_t mk_srd(const void* p, u32 bytes) { return __builtin_amdgcn_make_buffer_rsrc((void*)p, (short)0, (int)bytes, 0x00020000); }
DI float asf(u32 d) { return __builtin_bit_cast(float, d); }
DI u32 asu(float f) { return __builtin_bit_cast(u32, f); }
DI bool all_lanes(bool c) { return __builtin_amdgcn_ballot_w64(!c) == 0; }
DI float bl(u32 d) { return asf(d << 16); }
DI float bh(u32 d) { return asf(d & 0xffff0000u); }
DI u32 pk(float a, float b) { f2 t = {a, b}; return __builtin_bit_cast(u32, __builtin_convertvector(t, bf2)); }
DI float rest(float v) { return v - bh(asu(v)); }
DI u32 pkt(float lo, float hi) { return __builtin_amdgcn_perm(asu(hi), asu(lo), 0x07060302u); }
DI f4 mfma(v4u a, v4u b, f4 c) { return __builtin_amdgcn_mfma_f32_16x16x32_bf16(__builtin_bit_cast(s8v, a), __builtin_bit_cast(s8v, b), c, 0, 0, 0); }
DI f16v mfma32(v4u a, v4u b, f16v c) { return __builtin_amdgcn_mfma_f32_32x32x16_bf16(__builtin_bit_cast(s8v, a), __builtin_bit_cast(s8v, b), c, 0, 0, 0); }
DI lptr lp(u32 a) { return (lptr)(uintptr_t)a; }
DI v2u trread(u32 a) { return __builtin_bit_cast(v2u, __builtin_amdgcn_ds_read_tr16_b64_v4i16((LAS s4v*)lp(a))); }
DI v4u trfrag(u32 a, u32 hi_off) { const v2u lo = trread(a), hi = trread(a + hi_off); return v4u{lo.x, lo.y, hi.x, hi.y}; }
DI v4u lr128(u32 a) { return *(LAS v4u*)lp(a); }
DI f4 lrf4(u32 a) { return *(LAS f4*)lp(a); }
DI void lw128(u32 a, v4u v) { *(LAS v4u*)lp(a) = v; }
DI void lwf4(u32 a, f4 v) { *(LAS f4*)lp(a) = v; }
#define DMA_(OP, FL, rs, voff, sb, SI, lb, LI) do { u32 t_; \
  asm volatile("s_add_u32 m0, %1, %2\n\ts_add_u32 %0, %3, %4\n\t" OP " %5, %6, %0 offen" FL " lds" \
               : "=&s"(t_) : "s"(lb), "i"(LI), "s"(sb), "i"(SI), "v"(voff), "s"(rs) : "memory", "scc"); } while (0)
#define DMA(rs, voff, sb, SI, lb, LI) DMA_("buffer_load_dwordx4", "", rs, voff, sb, SI, lb, LI)
#define DMANT(rs, voff, sb, SI, lb, LI) DMA_("buffer_load_dwordx4", " nt", rs, voff, sb, SI, lb, LI)
#define M0SET(lb, LI) asm volatile("s_add_u32 m0, %0, %1\n\ts_nop 0" ::"s"(lb), "i"(LI) : "memory", "scc")
#define DML(rs, voff, so, OFF) asm volatile("buffer_load_dwordx4 %0, %1, %2 offen offset:%3 lds" ::"v"(voff), "s"(rs), "s"(so), "i"(OFF) : "memory")
#define TOUCH(rs, voff, sb, SI) DMA_("buffer_load_dword", "", rs, voff, sb, SI, 0u, L_JUNK)
#define ALD128(d, off, rs, FL) asm volatile("buffer_load_dwordx4 %0, %1, %2, 0 offen" FL : "=v"(d) : "v"(off), "s"(rs) : "memory")
#define ALD128S(d, off, rs, so, FL) asm volatile("buffer_load_dwordx4 %0, %1, %2, %3 offen" FL : "=v"(d) : "v"(off), "s"(rs), "s"(so) : "memory")
#define ALD64(d, off, rs, FL) asm volatile("buffer_load_dwordx2 %0, %1, %2, 0 offen" FL : "=v"(d) : "v"(off), "s"(rs) : "memory")
#define ALD32(d, off, rs, FL) asm volatile("buffer_load_dword %0, %1, %2, 0 offen" FL : "=v"(d) : "v"(off), "s"(rs) : "memory")
#define ALD16(d, off, rs, FL) asm volatile("buffer_load_ushort %0, %1, %2, 0 offen" FL : "=v"(d) : "v"(off), "s"(rs) : "memory")
#define WAITV(n) asm volatile("s_waitcnt vmcnt(" #n ")" ::: "memory")
#define PIN(a) asm volatile("" : "+v"(a)::"memory")
#define KEEP(a) asm volatile("" : "+v"(a))
#define KEEPP(a) asm volatile("" : "+s"(a))
#define KEEPS(a) do { a = __builtin_amdgcn_readfirstlane(a); asm volatile("" : "+s"(a)); } while (0)
#define WAITL() asm volatile("s_waitcnt lgkmcnt(0)" ::: "memory")
#define BAR() __builtin_amdgcn_s_barrier()
#define VBAR(n) do { __builtin_amdgcn_s_waitcnt(0xC07F); asm volatile("s_waitcnt vmcnt(" #n ") lgkmcnt(0)\n\ts_barrier" ::: "memory"); } while (0)
#define LBAR() do { __builtin_amdgcn_s_waitcnt(0xC07F); asm volatile("s_waitcnt lgkmcnt(0)\n\ts_barrier" ::: "memory"); } while (0)
#define SB() __builtin_amdgcn_sched_barrier(0)
DI void wave_sum2(float& a, float& b) {
  const auto x = __builtin_amdgcn_permlane32_swap(asu(a), asu(b), false, false);
  float v = asf(x[0]) + asf(x[1]);
  const auto y = __builtin_amdgcn_permlane16_swap(asu(v), asu(v), false, false);
  v = asf(y[0]) + asf(y[1]);
  v += asf(__builtin_amdgcn_update_dpp(0u, asu(v), 0xB1, 0xF, 0xF, true));
  v += asf(__builtin_amdgcn_update_dpp(0u, asu(v), 0x4E, 0xF, 0xF, true));
  v += asf(__builtin_amdgcn_update_dpp(0u, asu(v), 0x141, 0xF, 0xF, true));
  v += asf(__builtin_amdgcn_update_dpp(0u, asu(v), 0x140, 0xF, 0xF, true));
  const auto z = __builtin_amdgcn_permlane32_swap(asu(v), asu(v), false, false);
  a = asf(z[0]); b = asf(z[1]);
}
DI v4u ld_sc1(srd_t rs, u32 off, u32 soff = 0) { return __builtin_amdgcn_raw_buffer_load_b128(rs, off, soff, 16); }
template <int IMM = 0> DI void st_sc1(srd_t rs, u32 off, v4u v, u32 soff = 0) {
  asm volatile("buffer_store_dwordx4 %0, %1, %2, %3 offen offset:%4 sc1\n\ts_nop 1" ::"v"(v), "v"(off), "s"(rs), "s"(soff), "i"(IMM) : "memory");
}
DI void put_flag(srd_t rs, u32 l0, u32 off, u32 tagv) { __builtin_amdgcn_raw_buffer_store_b32(tagv, rs, l0, off, 16); }
DI u32 get_flag(srd_t rs, u32 off) { return __builtin_amdgcn_raw_buffer_load_b32(rs, off, 0, 16); }
DI void wait_flags(srd_t rs, u32 off, u32 tagv, u32 v) {
  if (!all_lanes(v == tagv)) {
#pragma unroll 1
    for (u32 spins = 0; spins < SPIN_CAP; ++spins) {
      u32 x;
      asm volatile("buffer_load_dword %0, %1, %2, 0 offen sc1\n\ts_waitcnt vmcnt(0)" : "=&v"(x) : "v"(off), "s"(rs) : "memory");
      if (all_lanes(x == tagv)) break;
      __builtin_amdgcn_s_sleep(1);
    }
  }
  asm volatile("" ::: "memory");
}
DI void arrive_publish(u32 cnt, srd_t rs, u32 l0, u32 off, u32 tag) {
  u32 old;
  asm volatile("s_mov_b64 exec, 1\n\tds_add_rtn_u32 %0, %1, %2\n\ts_mov_b64 exec, -1\n\ts_waitcnt lgkmcnt(0)" : "=&v"(old) : "v"(cnt), "v"(1u) : "memory");
  if (__builtin_amdgcn_readfirstlane(old) == 3u) put_flag(rs, l0, off, tag);
}
struct ConvList { int kh[144], e[144]; bool newA[144], newB[144]; };
constexpr int KEEP_MASK[2] = {0x99, 0x66};
constexpr int TS[2][4] = {{0, 3, 4, 7}, {1, 2, 5, 6}};
constexpr ConvList make_list(int role) {
  ConvList L = {};
  int n = 0;
  for (int kl = 0; kl < 4; ++kl) {
    bool seen[16] = {};
    for (int part = 0; part < (kl == 3 ? 2 : 1); ++part) {
      const int mask = kl < 3 ? 0xff : KEEP_MASK[part ? role : role ^ 1];
      for (int kh = 0; kh < 8; ++kh)
        for (int T = 7; T >= kh; --T) {
          if (!((mask >> T) & 1)) continue;
          const int e = T - kh;
          L.kh[n] = kh; L.e[n] = e; L.newA[n] = !seen[kh]; L.newB[n] = !seen[8 + e];
          seen[kh] = seen[8 + e] = true;
          ++n;
        }
    }
  }
  return L;
}
template <class F, int... I> DI void static_for_(F&& f, std::integer_sequence<int, I...>) { (f(std::integral_constant<int, I>{}), ...); }
template <int N, class F> DI void static_for(F&& f) { static_for_(f, std::make_integer_sequence<int, N>{}); }
constexpr int CLA = 10;

__global__ void __launch_bounds__(512, 2) fused(Params p) {
  const int tid = __builtin_amdgcn_workitem_id_x(), lane = tid & 63, w = __builtin_amdgcn_readfirstlane(tid >> 6);
  const int b = __builtin_amdgcn_workgroup_id_x(), xcd = b & 7, idx = b >> 3;
  const int g = lane >> 4, r16 = lane & 15, q4 = r16 >> 2, p4 = lane & 3, rr = lane >> 3;
  if (w == 0) {
    u64 pc; u32 len;
    asm volatile("mk_code_begin:\n\ts_getpc_b64 %0\n\ts_mov_b32 %1, mk_code_end-mk_code_begin\n\ts_max_i32 %1, %1, mk_code_end2-mk_code_begin" : "=s"(pc), "=s"(len) :: "scc");
    const srd_t rc = mk_srd((const void*)pc, len);
    TOUCH(rc, (u32)(lane * 128), (u32)((idx & 3) * 8192), 0);
  }
  SB();
  const int cp = 32 * xcd + idx;
  const bool is_g1 = idx < 28;
  u32 w1k = w * 1024, w4k = w * 4096;
  const int stok = 256 * xcd + 8 * idx + w;
  const int sidx = idx & 3, ti = 4 * (xcd >> 1) + sidx, t1 = idx >> 2, tj = 7 * (xcd & 1) + t1;
  const int hm = 4 * (xcd >> 1) + sidx, cgp = 14 * (xcd & 1) + (hm < 14 ? hm : 0);
  const int tm = 4 * xcd + sidx, tn = t1 ^ 4;
  const int mi = w & 3, ni = w >> 2, hl = lane >> 5, l31 = lane & 31;
  const int m2 = w & 1, nq = w >> 1;
  const u32 chsw = (u32)(((lane & 7) ^ (rr & 6)) << 4);
  u32 xro = (u32)(stok * 1024 + lane * 16);
  u32 xvo = (u32)((128 * ti + 16 * w + rr) * 1024 + (((lane & 7) ^ g) << 4)), xvo1 = (xvo ^ 64) + 7168;
  u32 wvo = (u32)((8 * w + (is_g1 ? g : rr)) * 3584 + (is_g1 ? tj * 256 : cgp * 128)) + (is_g1 ? (u32)((r16 ^ (g << 2)) << 4) : chsw), wvo1 = wvo + 13312;
  u32 sox = (u32)((4 * ((t1 + sidx) & 1) + xcd) & 7) * 128, sow = sox * 1792;
  u32 kvo = (u32)(rr * 1024 + (((lane & 7) ^ (rr & 2)) << 4)), kso = (u32)((64 * idx + 8 * w) * 1024 + xcd * 128);
  u32 l0 = l31 == 0 ? 4 * hl : 0x80000000u, l01 = lane < 2 ? lane * 16 : 0x80000000u;
  u32 s_st = WS_STATS + (u32)stok * 32;
  u32 xa[2][4], wa[2];
#pragma unroll
  for (int ks = 0; ks < 4; ++ks) { xa[0][ks] = (4 * m2 + (g & 1)) * 4096 + r16 * 128 + (((2 * ks + hl) ^ (r16 >> 1)) << 4); xa[1][ks] = xa[0][ks] + 2 * L_SLOT; }
  wa[0] = 2048 + hl * 4096 + q4 * 256 + (((4 * nq + 2 * (g & 1) + (p4 >> 1)) ^ (q4 << 2)) << 4) + 8 * (p4 & 1);
  wa[1] = wa[0] + 2 * L_SLOT;
  KEEP(xro); KEEP(xvo); KEEP(xvo1); KEEP(wvo); KEEP(wvo1); KEEPS(sox); KEEPS(sow); KEEPS(w1k); KEEPS(w4k);
  KEEP(l0); KEEP(l01); KEEPS(s_st); KEEP(kvo); KEEPS(kso); KEEP(wa[0]); KEEP(wa[1]);
#pragma unroll
  for (int i = 0; i < 4; ++i) { KEEP(xa[0][i]); KEEP(xa[1][i]); }
  SB();
  KEEPP(p.ws); KEEPP(p.out); KEEPP(p.w2); KEEPP(p.b2); KEEPP(p.k); KEEPP(p.x); KEEPP(p.w1);
  const srd_t rx = mk_srd(p.x, S * D * 2), rw1 = mk_srd(p.w1 - 1024, D * F * 2 + 2048);
  v4u xr;
  const srd_t rws = mk_srd(p.ws, WS_BYTES);
  ALD128(xr, xro, rx, "");
#define NEXT_CHUNK() do { sox = (sox + 128) & 1023; sow = sox * 1792; } while (0)
#define PIECE(c, i) do { \
    if ((i) == 0) { M0SET(w4k, ((c) % 3) * L_SLOT); DML(rx, xvo, sox, 0); } \
    if ((i) == 1) DML(rx, xvo1, sox, 1024); \
    if ((i) == 2) DML(rw1, wvo, sow, 2048); \
    if ((i) == 3) { DML(rw1, wvo1, sow, 3072); NEXT_CHUNK(); } } while (0)
#define HPIECE(c) DMA(rw1, wvo, 0u, (c) * 64 * 3584 + 2048, w1k, (c) * 8192)
  if (is_g1) { PIECE(0, 0); PIECE(0, 1); PIECE(0, 2); PIECE(0, 3); }
  else { HPIECE(0); HPIECE(1); HPIECE(2); HPIECE(3); }
  SB();
  u32 ddp = *(const CAS u32*)(p.dd + 2 * cp);
  v2u hq = *(const CAS v2u*)(p.ssm + cp * 4096 + ((w & 3) ? w * 512 - 4 : 0));
  SB();
#define STAGE_K() do { const srd_t rk = mk_srd(p.k, S * D * 2); DMANT(rk, kvo, kso, 0, w1k, L_PK + 1024); \
    if (w == 0) DMANT(rk, kvo + kso - 8192u, 0u, 0, 0u, L_PK); } while (0)
#define STAGE_V() do { const srd_t rv = mk_srd(p.v, S * D * 2); DMANT(rv, kvo, kso, 0, w1k, L_PV); \
    if (w == 1) { const srd_t rsk = mk_srd(p.sk, D * 4 * 2); DMA(rsk, (u32)(lane * 16), (u32)(xcd * 512), 0, 0u, L_PW); } \
    if (w == 2) { const srd_t rb2 = mk_srd(p.b2, D * 2); DMA_("buffer_load_dword", "", rb2, (u32)(lane * 4), (u32)(tn * 128), 0, 0u, L_B2); } } while (0)
  constexpr u32 TR_HI = 1024;
  const srd_t rw2 = mk_srd(p.w2, F * D * 2), rssm = mk_srd(p.ssm + cp * 4096, 8192);
  const u32 l16 = lane * 16;
  u32 ksh[7];
  { int t = xcd;
#pragma unroll
    for (int s = 0; s < 7; ++s) { t = t >= 7 ? t - 7 : t; ksh[s] = (u32)t * 1024; KEEPS(ksh[s]); ++t; } }
  u32 w2base = (u32)(224 * w * 1024 + tn * 128), s_cp = (u32)cp * 8192;
  u32 a_kv = L_PK + 512 + ni * 4096 + (8 * g + q4) * 128 + (((2 * mi + (p4 >> 1)) ^ (q4 & 2)) << 4) + 8 * (p4 & 1);
  u32 a_tap = L_PW + mi * 128 + r16 * 8;
  u32 o_kv = (u32)((r16 >> 1) * 8192 + (r16 & 1) * 64 + (3 - g) * 16);
  u32 s_kv = WS_KVT + (u32)((32 * xcd + 8 * mi) * 8192 + (63 - 2 * idx - ni) * 128);
  u32 w2vo[4];
#pragma unroll
  for (int i = 0; i < 4; ++i) { w2vo[i] = (u32)(rr * 1024 + i * 7168) + chsw; KEEP(w2vo[i]); }
  KEEPS(w2base); KEEPS(s_cp); KEEP(a_kv); KEEP(a_tap); KEEP(o_kv); KEEPS(s_kv);
  if (w == 2) lw128(L_U + hl * U_STRIDE + 4096 + l31 * 16, v4u{0, 0, 0, 0});
  if (w == 3) lw128(L_CNT0 + (lane & 1) * 16, v4u{0, 0, 0, 0});
  u32 fo1;
  { const int m = 2 * t1 + (lane & 1);
    const u32 fa = l31 * 8 + xcd, fb = (16 * (ti & 1) + r16) * 8 + (ti >> 1), fc = (WS_FST - WS_FKV) / FLAG_STRIDE + ((m >> 2) * 2 + (xcd & 1)) * 4 + (m & 3);
    fo1 = WS_FKV + ((is_g1 && w == 7) ? ((lane & 16) ? fc : fb) : fa) * FLAG_STRIDE + 4 * hl; }
  u32 t16 = tid * 16, o_out = (u32)((64 * tm + 8 * w) * 1024 + 128 * tn) + rr * 1024 + (lane & 7) * 16;
  u32 o_xe = WS_STATS + (u32)((128 * ti + 64 * m2) * 32) + lane * 32, o_we = WS_CSUM + (u32)((128 * tj + 32 * nq) * 16) + l31 * 16;
  u32 ta[4];
  { const int trow = 16 * hl + 4 * (g & 1) + q4;
    ta[0] = L_SLOT + w4k + trow * 128 + (((p4 >> 1) ^ (trow & 6)) << 4) + 8 * (p4 & 1);
    KEEP(ta[0]);
    ta[1] = ta[0] ^ 32; ta[2] = ta[0] ^ 64; ta[3] = ta[0] ^ 96; }
  u32 a_hc = L_HC + ni * (4 * HC_STRIDE - 4096) + 32 + t16;
  u32 o_h = (u32)((g & 1) * 57344 + r16 * 16 + hl * 256), s_h = WS_H + (u32)(((8 * ti + 4 * m2) * 56 + 4 * tj + nq) * 1024);
  KEEP(fo1); KEEP(t16); KEEP(o_out); KEEP(o_xe); KEEP(o_we); KEEP(ta[1]); KEEP(ta[2]); KEEP(ta[3]); KEEP(a_hc); KEEP(o_h); KEEPS(s_h);
  auto make_kv = [&]() {
    const v2u tw = *(LAS v2u*)lp(a_tap);
    const v2u k0 = trread(a_kv), k1 = trread(a_kv + 512), k2 = trread(a_kv + 1024);
    const v2u v0 = trread(a_kv + (L_PV - L_PK - 512)), v1 = trread(a_kv + (L_PV - L_PK));
    const float kk[12] = {0.f, bh(k0.x), bl(k0.y), bh(k0.y), bl(k1.x), bh(k1.x), bl(k1.y), bh(k1.y), bl(k2.x), bh(k2.x), bl(k2.y), bh(k2.y)};
    const float vv[8] = {bl(v0.x), bh(v0.x), bl(v0.y), bh(v0.y), bl(v1.x), bh(v1.x), bl(v1.y), bh(v1.y)};
    const float w0 = bl(tw.x), w1 = bh(tw.x), w2 = bl(tw.y), w3 = bh(tw.y);
    float o[8];
#pragma unroll
    for (int j = 0; j < 8; j += 2)
      asm("v_mul_f32 %0, %2, %7\n\tv_mul_f32 %1, %3, %7\n\tv_fmac_f32 %0, %3, %8\n\tv_fmac_f32 %1, %4, %8\n\tv_fmac_f32 %0, %4, %9\n\tv_fmac_f32 %1, %5, %9\n\t"
          "v_fmac_f32 %0, %5, %10\n\tv_fmac_f32 %1, %6, %10\n\tv_mul_f32 %0, %0, %11\n\tv_mul_f32 %1, %1, %12"
          : "=&v"(o[j]), "=&v"(o[j + 1])
          : "v"(kk[j + 1]), "v"(kk[j + 2]), "v"(kk[j + 3]), "v"(kk[j + 4]), "v"(kk[j + 5]), "v"(w3), "v"(w2), "v"(w1), "v"(w0), "v"(vv[j]), "v"(vv[j + 1]));
    st_sc1(rws, o_kv, v4u{pk(o[7], o[6]), pk(o[5], o[4]), pk(o[3], o[2]), pk(o[1], o[0])}, s_kv);
  };
  SB();
  BAR();
  if (is_g1) { PIECE(1, 0); PIECE(1, 1); PIECE(1, 2); PIECE(1, 3); }
  else { HPIECE(4); HPIECE(5); HPIECE(6); HPIECE(7); }
  WAITV(8);
  PIN(xr);
  {
    float s1 = 0.f, s2 = 0.f;
    u32 one2 = 0x3f803f80u;
    KEEP(one2);
#pragma unroll
    for (int j = 0; j < 4; ++j) {
      const u32 xj = xr[j];
      const bf2 v = __builtin_bit_cast(bf2, xj);
      s1 = __builtin_amdgcn_fdot2_f32_bf16(v, __builtin_bit_cast(bf2, one2), s1, false);
      s2 = __builtin_amdgcn_fdot2_f32_bf16(v, v, s2, false);
    }
    wave_sum2(s1, s2);
    const float mean = s1 * (1.0f / D), var = __builtin_fmaxf(s2 * (1.0f / D) - mean * mean, 0.f);
    const float iA = __builtin_amdgcn_sqrtf(var + EPS * (var + EPS));
    const float m0 = -mean, m1 = rest(m0), m2 = rest(m1);
    u32 d0 = pkt(m0, m0), d1 = pkt(m1, m1);
    const u32 mv = pk(mean, var);
    float rq = __builtin_amdgcn_rcpf(bl(pk(__builtin_amdgcn_sqrtf(bl(pk(bh(mv) + EPS, 0.f))), 0.f))), mq = -bl(mv) * rq;
    KEEP(d0); KEEP(d1); KEEP(rq); KEEP(mq);
    const bool l1 = lane & 1;
    st_sc1(rws, l01, v4u{l1 ? asu(rq) : d0, l1 ? asu(mq) : d1, pkt(m2, m0), pkt(iA, rest(iA))}, s_st);
  }
  SB();
  STAGE_K(); STAGE_V();
  v4u hd;
  ALD128(hd, t16, rssm, " nt");
  u32 tagv;
  ALD32(tagv, (u32)(WS_EPOCH + 4 * hl), rws, " sc1");
#define MAKE_TAG() do { PIN(tagv); tagv += 0x9E3779B9u; } while (0)
  SB();
  v4u w2f[7][4];
  u32 sw2;
#define W2SLOT(s) ((s) == 0 ? 3 : ((s) + 1) % 3)
#define W2_PIECE(s, i) do { if ((i) == 0) { sw2 = w2base + (ksh[s] << 5); M0SET(w4k, W2SLOT(s) * L_SLOT); } DML(rw2, w2vo[i], sw2, (i) * 1024); } while (0)
#define W2_STAGE(s) do { W2_PIECE(s, 0); W2_PIECE(s, 1); W2_PIECE(s, 2); W2_PIECE(s, 3); } while (0)
  v4u xv; v2u stv;
  v4u u0, u1;
#define LOAD_U() do { ALD128S(u0, t16, rws, s_cp + WS_KVT, " sc1"); ALD128S(u1, t16, rws, s_cp + WS_KVT + 4096, " sc1"); } while (0)
#define LOAD_LAST() ALD128(xv, o_out, rx, "")
#define LOAD_TC() ALD64(stv, WS_STATS + 16 + (64 * tm + lane) * 32, rws, " sc1")
  auto w2_read = [&](int s) {
#pragma unroll
    for (int t = 0; t < 4; ++t) w2f[s][t] = trfrag(ta[t] + W2SLOT(s) * L_SLOT - L_SLOT, TR_HI);
  };
  auto write_hc = [&](int rho) {
    const u32 p0 = __builtin_amdgcn_update_dpp(mi ? hq.x : 0u, hd.z, 0x138, 0xF, 0xF, false), p1 = __builtin_amdgcn_update_dpp(mi ? hq.y : 0u, hd.w, 0x138, 0xF, 0xF, false);
    const u32 a0 = __builtin_amdgcn_alignbit(p1, p0, 16), a1 = __builtin_amdgcn_alignbit(hd.x, p1, 16), a2 = __builtin_amdgcn_alignbit(hd.y, hd.x, 16),
              a3 = __builtin_amdgcn_alignbit(hd.z, hd.y, 16), a4 = __builtin_amdgcn_alignbit(hd.w, hd.z, 16);
    if (rho == 0) lw128(a_hc, hd);
    if (rho == 1) lw128(a_hc + HC_STRIDE, v4u{a1, a2, a3, a4});
    if (rho == 2) lw128(a_hc + 2 * HC_STRIDE, v4u{p1, hd.x, hd.y, hd.z});
    if (rho == 3) lw128(a_hc + 3 * HC_STRIDE, v4u{a0, a1, a2, a3});
  };

#define CONV_SETUP() do { PIN(hd); write_hc(0); write_hc(1); write_hc(2); write_hc(3); \
    if (w == 4) lw128(L_HC + rr * HC_STRIDE + (lane & 1) * 16, v4u{0, 0, 0, 0}); } while (0)

  if (__builtin_expect(is_g1, 1)) {
    f16v acc[2];
#pragma unroll
    for (int i = 0; i < 16; ++i) { acc[0][i] = 0.f; acc[1][i] = 0.f; }
    v4u xf[2][2][2], wf[2][2];
#define REQ(dc, i) do { if ((dc) >= 8) W2_PIECE((dc) & 3, i); else PIECE(dc, i); } while (0)
#define STEP(st, dc, d0, nd) do { \
      constexpr int hs_ = (st) & 1, hn_ = hs_ ^ 1, sl_ = (((st) + 1) >> 1) % 3, sb_ = sl_ >> 1; constexpr u32 so_ = (sl_ & 1) * L_SLOT; \
      constexpr bool rd_ = (st) < 15, mm_ = (st) >= 0; \
      _Pragma("unroll") for (int i_ = 0; i_ < 4; ++i_) { \
        if (mm_) acc[i_ & 1] = mfma32(wf[hs_][i_ >> 1], xf[hs_][i_ >> 1][i_ & 1], acc[i_ & 1]); \
        if (rd_) { \
          if (!(i_ & 1)) wf[hn_][i_ >> 1] = trfrag(wa[sb_] + so_ + (2 * hn_ + (i_ >> 1)) * 8192, 1024); \
          else { xf[hn_][i_ >> 1][0] = lr128(xa[sb_][2 * hn_ + (i_ >> 1)] + so_); xf[hn_][i_ >> 1][1] = lr128(xa[sb_][2 * hn_ + (i_ >> 1)] + so_ + 8192); } } \
        if ((nd) == 4) { if (i_ == 0) REQ(dc, 0); if (i_ == 1) REQ(dc, 1); if (i_ == 2) REQ(dc, 2); if (i_ == 3) REQ(dc, 3); } \
        if ((nd) == 2) { if (i_ == 0) REQ(dc, d0); if (i_ == 2) REQ(dc, (d0) + 1); } \
        SB(); } } while (0)
#define ITER(c) do { STEP(2 * (c) + 1, (c) + 3, 0, 2); STEP(2 * (c) + 2, (c) + 3, 2, 2); } while (0)
    u32 pv; v4u xe, we;
    VBAR(9);
    STEP(-1, 0, 0, 0);
    STEP(0, 2, 0, 4);
    VBAR(9);
    ITER(0);
    VBAR(4);
    MAKE_TAG();
    make_kv();
    SB();
    ITER(1); W2_PIECE(0, 0); W2_PIECE(0, 1);
    VBAR(6);
    if (w == 0) put_flag(rws, l0, WS_FKV + b * FLAG_STRIDE, tagv);
    ITER(2); M0SET(w4k, 3 * L_SLOT); W2_PIECE(0, 2); W2_PIECE(0, 3);
    VBAR(8);
    ITER(3);
    VBAR(6);
    ITER(4);
    if (w >= 6) { ALD32(pv, fo1, rws, " sc1"); WAITV(5); } else WAITV(4);
    LBAR();
    STEP(11, 9, 0, 2); STEP(12, 9, 2, 2);
    WAITV(4);
    if (w >= 6) { PIN(pv); wait_flags(rws, fo1, tagv, pv); }
    LBAR();
    ALD128(xe, o_xe, rws, " sc1");
    ALD128(we, o_we, rws, " sc1");
    if (!ni) LOAD_U();
    if (w == 3) LOAD_TC();
    STEP(13, 0, 0, 0); w2_read(0); STEP(14, 10, 0, 2);
    LBAR();
    STEP(15, 10, 2, 2);
    if (ni) WAITV(4); else WAITV(6);
    PIN(xe); PIN(we);
    {
      const v4u z = {0, 0, 0, 0};
      acc[0] = mfma32(we, hl ? z : xe, acc[0]);
      acc[1] = mfma32(we, hl ? xe : z, acc[1]);
    }
    if (ni) { CONV_SETUP(); SB(); }
    f2 k1v[2], k3v[2], iAv[2];
    { const auto r = __builtin_amdgcn_permlane32_swap(xe.w, xe.w, false, false);
#pragma unroll
      for (int m = 0; m < 2; ++m) {
        const float iA = bl(r[m]) + bh(r[m]), A = __builtin_amdgcn_rcpf(iA), k1s = -2.3022082f * A, k3s = -0.10294324f * A * A * A;
        k1v[m] = f2{k1s, k1s}; k3v[m] = f2{k3s, k3s}; iAv[m] = f2{iA, iA};
      } }
#pragma unroll
    for (int qi = 0; qi < 4; ++qi) {
      const int n = qi >> 1, sp = qi & 1;
      if (qi == 0) W2_PIECE(3, 0);
      if (qi == 1) W2_PIECE(3, 1);
      if (qi == 2) W2_PIECE(3, 2);
      if (qi == 3) W2_PIECE(3, 3);
      v4u o;
#pragma unroll
      for (int j = 0; j < 4; ++j) {
        const f2 a = {acc[n][8 * sp + 2 * j], acc[n][8 * sp + 2 * j + 1]}, k1 = k1v[n], k3 = k3v[n], iA = iAv[n];
        const f2 z = a * __builtin_elementwise_fma(a * a, k3, k1);
        const f2 e = {__builtin_amdgcn_exp2f(z.x), __builtin_amdgcn_exp2f(z.y)};
        const f2 d = __builtin_elementwise_fma(e, iA, iA);
        const f2 hv = a * f2{__builtin_amdgcn_rcpf(d.x), __builtin_amdgcn_rcpf(d.y)};
        o[j] = pk(hv.x, hv.y);
      }
      if (qi == 0) st_sc1<0>(rws, o_h, o, s_h);
      if (qi == 1) st_sc1<512>(rws, o_h, o, s_h);
      if (qi == 2) st_sc1<0>(rws, o_h, o, s_h + 114688);
      if (qi == 3) st_sc1<512>(rws, o_h, o, s_h + 114688);
      SB();
    }
  } else {
    u32 b1v;
    { const srd_t rb1 = mk_srd(p.b1 + 64 * cgp, 128); ALD16(b1v, (u32)(16 * mi + 4 * g + p4) * 2, rb1, ""); }
    VBAR(0);
    MAKE_TAG();
    if (w < 4) {
      const v4u ones = {0x3f803f80u, 0x3f803f80u, 0x3f803f80u, 0x3f803f80u};
      f4 s0 = {0, 0, 0, 0};
      const u32 a = (w == 0 ? ta[0] : w == 1 ? ta[1] : w == 2 ? ta[2] : ta[3]) - L_SLOT - w4k;
#pragma unroll
      for (int st = 0; st < 16; ++st) s0 = mfma(trfrag(a + st * 4096, TR_HI), ones, s0);
      const float ca = (p4 & 1) ? s0[1] : s0[0], cb = (p4 & 1) ? s0[3] : s0[2], c = (p4 & 2) ? cb : ca;
      const float c1 = rest(c), c2 = rest(c1);
      PIN(b1v);
      st_sc1(rws, r16 < 4 && hm < 14 ? WS_CSUM + (u32)(64 * cgp + 16 * w + 4 * g + p4) * 16 : 0x80000000u, v4u{pkt(c, c1), pkt(c, c1), pkt(c, c2), b1v * 0x10001u});
    }
    make_kv();
    VBAR(0);
    if (w == 0) { put_flag(rws, l0, WS_FST + (4 * xcd + sidx) * FLAG_STRIDE, tagv); put_flag(rws, l0, WS_FKV + b * FLAG_STRIDE, tagv); }
    W2_STAGE(0); W2_STAGE(1); W2_STAGE(2); W2_STAGE(3);
    if (b == 255) {
      wait_flags(rws, WS_FKV + (32 * w + l31) * FLAG_STRIDE + 4 * hl, tagv, ~tagv);
      BAR();
      if (w == 0) put_flag(rws, l0, WS_EPOCH, tagv);
    }
    wait_flags(rws, fo1, tagv, ~tagv);
    w2_read(0);
    LBAR();
    asm volatile("mk_code_end2:");
    if (ni) CONV_SETUP(); else LOAD_U();
    if (w == 3) LOAD_TC();
  }
  u32 fo2, hb;
#define LATE_SETUP() do { const int tja = ((224 * w) >> 7) + (rr & 3), tjb = (224 * w + 223) >> 7; \
    fo2 = WS_FH + ((tm >> 1) * 14 + (tja < tjb ? tja : tjb)) * FLAG_STRIDE + 4 * hl; hb = WS_H + (u32)((4 * tm * 56 + 7 * w) * 1024) + l16; KEEP(fo2); KEEP(hb); } while (0)
  v4u hf[7];
  u32 hbm;
  const int mrot = 2 * tn;
  auto load_h = [&](int m, int s) { if (s == 0) hbm = hb + (u32)(((m + mrot) & 3) * 57344); hf[s] = ld_sc1(rws, hbm, ksh[s]); };
  f4 yacc[4];
  u32 a_x, o_y;
  if (!ni) {
    CONV_SETUP();
    const int rho = (15 - r16) & 3;
    u32 a_A = L_HC + ((mi >> 1) * 4 + rho) * HC_STRIDE + (r16 + 1 + rho + 8 * g) * 2 + (mi & 1) * 256;
    u32 a_B = L_U + (mi >> 1) * U_STRIDE + (240 - 16 * r16 + 8 * g) * 2 + (mi & 1) * 256;
    u32 a_u = L_U + ((tid >> 2) & 1) * U_STRIDE + (tid >> 3) * 64 + (tid & 3) * 16;
    KEEP(a_A); KEEP(a_B); KEEP(a_u);
    LATE_SETUP();
    WAITV(0);
    PIN(u0); PIN(u1);
    lw128(a_u, u0); lw128(a_u + 2048, u1);
    if (w == 3) { PIN(stv); *(LAS v2u*)lp(L_TC + lane * 8) = stv; }
    w2_read(1); w2_read(2); w2_read(3);
    v4u AD;
    { const u32 dv = (mi & 2) ? ddp >> 16 : ddp & 0xffffu; const int pos = 15 - r16 - 8 * g;
#pragma unroll
      for (int j = 0; j < 4; ++j) AD[j] = (pos >> 1) == j ? dv << ((pos & 1) * 16) : 0u; }
    u32 a_B0 = a_B - (mi & 1) * 256;
    a_x = L_X + w4k + l16;
    o_y = s_cp + WS_YT + (mi >> 1) * 4096 + (mi & 1) * 512 + r16 * 32 + hl * 16 + (g & 1) * ((mi & 1) ? 512u : 1536u);
    KEEP(a_x); KEEP(a_B0); KEEP(o_y);
    LBAR();
    u32 pv2;
    auto conv = [&](auto ROLE) {
      constexpr int role = decltype(ROLE)::value;
      constexpr ConvList CL = make_list(role);
      f4 ac[8];
      v4u fa[4][8], fb[4][8], fd[4];
      auto rd = [&](auto N) {
        constexpr int n = decltype(N)::value, kl = n / 36;
        if constexpr (n < 144) {
          if constexpr (CL.newA[n]) {
            const v2u lo = *(volatile LAS v2u*)lp(a_A + CL.kh[n] * 512 + kl * 64), hi = *(volatile LAS v2u*)lp(a_A + CL.kh[n] * 512 + kl * 64 + 8);
            fa[kl][CL.kh[n]] = v4u{lo.x, lo.y, hi.x, hi.y};
          }
          if constexpr (CL.newB[n]) fb[kl][CL.e[n]] = lr128(a_B + (7 - CL.e[n]) * 512 + kl * 64);
        } else if constexpr (n < 148) fd[n - 144] = lr128(a_B0 + (7 - TS[role][n - 144]) * 512);
      };
      static_for<CLA>(rd);
      static_for<144>([&](auto N) {
        constexpr int n = decltype(N)::value, kl = n / 36, T = CL.kh[n] + CL.e[n];
        ac[T] = mfma(fa[kl][CL.kh[n]], fb[kl][CL.e[n]], n < 8 ? f4{0, 0, 0, 0} : ac[T]);
        rd(std::integral_constant<int, n + CLA>{});
        if constexpr (n >= 9 && n < 9 + 12 * 5 && (n - 9) % 5 == 0) { constexpr int q = (n - 9) / 5; W2_PIECE(4 + q / 4, q % 4); }
        if constexpr (n == 80) LOAD_LAST();
        if constexpr (n == 100) ALD32(pv2, fo2, rws, " sc1");
        if constexpr (n >= 128 && n < 136 && (n & 1) == 0) lwf4(a_x + ((n - 128) / 2) * 1024, ac[TS[role ^ 1][(n - 128) / 2]]);
        if constexpr (n == 136) { asm volatile("" ::: "memory"); *(volatile LAS u32*)lp(L_CNT0 + 16 + w * 4) = 1u; }
        SB();
      });
#pragma unroll
      for (int j = 0; j < 4; ++j) yacc[j] = mfma(AD, fd[j], ac[TS[role][j]]);
    };
    if (mi & 1) conv(std::integral_constant<int, 1>{}); else conv(std::integral_constant<int, 0>{});
    WAITV(0);
    PIN(pv2);
    wait_flags(rws, fo2, tagv, pv2);
  } else {
    LBAR();
    LATE_SETUP();
    WAITV(0);
    if (is_g1) arrive_publish(L_CNT0, rws, l0, WS_FH + (ti * 14 + tj) * FLAG_STRIDE, tagv);
    w2_read(1); w2_read(2); w2_read(3);
    WAITL();
    W2_STAGE(4); W2_STAGE(5); W2_STAGE(6);
    LOAD_LAST();
    wait_flags(rws, fo2, tagv, ~tagv);
  }
#pragma unroll
  for (int s = 0; s < 7; ++s) load_h(0, s);
  SB();
  if (!ni) {
#pragma unroll 1
    for (u32 spins = 0; spins < SPIN_CAP && *(volatile LAS u32*)lp(L_CNT0 + 16 + (w ^ 1) * 4) == 0u; ++spins) __builtin_amdgcn_s_sleep(1);
    asm volatile("" ::: "memory");
#pragma unroll
    for (int j = 0; j < 4; ++j) yacc[j] += lrf4(a_x + ((mi & 1) ? -4096 : 4096) + j * 1024);
#pragma unroll
    for (int j = 0; j < 4; j += 2) {
      const auto r0 = __builtin_amdgcn_permlane16_swap(pk(yacc[j][0], yacc[j][1]), pk(yacc[j + 1][0], yacc[j + 1][1]), false, false);
      const auto r1 = __builtin_amdgcn_permlane16_swap(pk(yacc[j][2], yacc[j][3]), pk(yacc[j + 1][2], yacc[j + 1][3]), false, false);
      if (j == 0) st_sc1<0>(rws, o_y, v4u{r0[0], r1[0], r0[1], r1[1]});
      else st_sc1<2048>(rws, o_y, v4u{r0[0], r1[0], r0[1], r1[1]});
    }
  }
  w2_read(4); w2_read(5); w2_read(6);
  const int tok = tid >> 3, chunk = tid & 7;

  u32 fo3 = WS_FY + (32 * tn + 4 * w + (rr & 3)) * FLAG_STRIDE + 4 * hl, o_yl = WS_YT + (u32)((64 * tn + tok) * 4096 + (64 * tm + 8 * chunk) * 2);
  u32 pp0 = w4k + (((lane & 48) | ((lane + 4 * hl) & 15)) << 4), yp = L_Y + (8 * chunk) * 128 + ((w ^ chunk) << 4) + (tok & 7) * 2;
  u32 rp = (((w >> 1) - mrot) & 3) * L_SLOT + (chunk >> 1) * 1024 + ((32 * (chunk & 1) + ((tok + 4 * (chunk & 3)) & 15)) << 4);
  u32 yr = L_Y + tok * 128 + ((chunk ^ w) << 4);
  KEEP(fo3); KEEP(o_yl); KEEP(pp0); KEEP(yp); KEEP(rp); KEEP(yr);
  PIN(xv);
  v4u b2v; v2u tc; u32 cdone;
  float xx[8], xn[8], bb[8];
  v4u yv;
  {
    u32 pv3 = 0;
    f4 acc[2][4];
#pragma unroll
    for (int m = 0; m < 4; ++m) {
#pragma unroll
      for (int s = 0; s < 7; ++s) {
#pragma unroll
        for (int t = 0; t < 4; ++t) acc[m & 1][t] = mfma(w2f[s][t], hf[s], s == 0 ? f4{0, 0, 0, 0} : acc[m & 1][t]);
        if (m < 3) load_h(m + 1, s);
        if (m == 0 && s == 5 && !ni) {
          SB();
          WAITV(6);
          arrive_publish(L_CNT0 + 4, rws, l0, WS_FY + cp * FLAG_STRIDE, tagv);
        }
        if (m > 0 && s >= 1 && s <= 4) lwf4(((pp0 + (m - 1) * L_SLOT) ^ (((s - 1) & 1) * 128)) + (s - 1) * 1024, acc[(m - 1) & 1][s - 1]);
        if (m == 2 && s == 0) pv3 = get_flag(rws, fo3);
        if (m == 3 && s == 1) {
          wait_flags(rws, fo3, tagv, pv3);
          yv = ld_sc1(rws, o_yl);
        }
        if (m == 3 && s == 2) { b2v = lr128(L_B2 + chunk * 16); tc = *(LAS v2u*)lp(L_TC + tok * 8); cdone = *(volatile LAS u32*)lp(L_CNT0 + 4); }
        if (m == 3 && s >= 3) {
          const int j = s - 3;
          xx[2 * j] = bl(xv[j]); xx[2 * j + 1] = bh(xv[j]); bb[2 * j] = bl(b2v[j]); bb[2 * j + 1] = bh(b2v[j]);
          xn[2 * j] = xx[2 * j] * asf(tc.x) + asf(tc.y); KEEP(xn[2 * j]);
          xn[2 * j + 1] = xx[2 * j + 1] * asf(tc.x) + asf(tc.y); KEEP(xn[2 * j + 1]);
          KEEP(xx[2 * j]); KEEP(xx[2 * j + 1]); KEEP(bb[2 * j]); KEEP(bb[2 * j + 1]);
        }
        SB();
      }
    }
    if (__builtin_amdgcn_readfirstlane(cdone) < 4u) {
#pragma unroll 1
      for (u32 spins = 0; spins < SPIN_CAP && *(volatile LAS u32*)lp(L_CNT0 + 4) < 4u; ++spins) __builtin_amdgcn_s_sleep(1);
    }
    asm volatile("" ::: "memory");
#pragma unroll
    for (int t = 0; t < 4; ++t) lwf4(((pp0 + 3 * L_SLOT) ^ ((t & 1) * 128)) + t * 1024, acc[1][t]);
  }
#pragma unroll
  for (int j = 0; j < 4; ++j) {
    *(LAS u16*)lp(yp + (2 * j) * 128) = (u16)(yv[j] & 0xffff);
    *(LAS u16*)lp(yp + (2 * j + 1) * 128) = (u16)(yv[j] >> 16);
  }
  LBAR();
  {
    const v4u yy = lr128(yr);
    const f4 o0 = ((lrf4(rp) + lrf4(rp + 4096)) + (lrf4(rp + 8192) + lrf4(rp + 12288))) +
                  ((lrf4(rp + 16384) + lrf4(rp + 20480)) + (lrf4(rp + 24576) + lrf4(rp + 28672)));
    const f4 o1 = ((lrf4(rp + 256) + lrf4(rp + 4352)) + (lrf4(rp + 8448) + lrf4(rp + 12544))) +
                  ((lrf4(rp + 16640) + lrf4(rp + 20736)) + (lrf4(rp + 24832) + lrf4(rp + 28928)));
    float res[8];
#pragma unroll
    for (int j = 0; j < 8; ++j) {
      const float ov = (j < 4 ? o0[j & 3] : o1[j & 3]) + bb[j];
      const float sg = __builtin_amdgcn_rcpf(1.0f + __builtin_amdgcn_exp2f(ov * -1.4426950409f));
      const float yj = (j & 1) ? bh(yy[j >> 1]) : bl(yy[j >> 1]);
      res[j] = xx[j] + ((xn[j] * yj) * ov) * sg;
    }
    __builtin_amdgcn_raw_buffer_store_b128(v4u{pk(res[0], res[1]), pk(res[2], res[3]), pk(res[4], res[5]), pk(res[6], res[7])}, mk_srd(p.out, S * D * 2), o_out, 0, 16);
  }
  asm volatile("mk_code_end:");
}
}

extern "C" void kernel_launch(void* const* d_in, const int* in_sizes, int n_in, void* d_out, int out_size, void* d_ws, size_t ws_size,
                              hipStream_t stream) {
  using namespace mk;
  Params p;
  p.x = (const u16*)d_in[0]; p.k = (const u16*)d_in[1]; p.v = (const u16*)d_in[2]; p.sk = (const u16*)d_in[3];
  p.ssm = (const u16*)d_in[4]; p.dd = (const u16*)d_in[5]; p.w1 = (const u16*)d_in[7]; p.b1 = (const u16*)d_in[8];
  p.w2 = (const u16*)d_in[9]; p.b2 = (const u16*)d_in[10];
  p.out = (u16*)d_out; p.ws = (unsigned char*)d_ws;
  (void)hipFuncSetAttribute((const void*)fused, hipFuncAttributeMaxDynamicSharedMemorySize, LDS_BYTES);
  fused<<<256, 512, LDS_BYTES, stream>>>(p);
}
```

```cpp
#include <hip/hip_runtime.h>
#include <stdint.h>
#include <type_traits>
#include <utility>

namespace mk {
typedef unsigned short u16;
typedef unsigned u32;
typedef unsigned long long u64;
typedef u32 v4u __attribute__((ext_vector_type(4)));
typedef u32 v2u __attribute__((ext_vector_type(2)));
typedef float f4 __attribute__((ext_vector_type(4)));
typedef float f2 __attribute__((ext_vector_type(2)));
typedef float f16v __attribute__((ext_vector_type(16)));
typedef short s8v __attribute__((ext_vector_type(8)));
typedef short s4v __attribute__((ext_vector_type(4)));
typedef __bf16 bf2 __attribute__((ext_vector_type(2)));
#define GAS __attribute__((address_space(1)))
#define LAS __attribute__((address_space(3)))
#define CAS __attribute__((address_space(4)))
#define DI __device__ __forceinline__
typedef __amdgpu_buffer_rsrc_t srd_t;
typedef LAS unsigned char* lptr;

constexpr int S = 2048, D = 512, F = 1792;
constexpr float EPS = 1e-5f;
constexpr u32 WS_EPOCH = 0, WS_FKV = 4096, WS_FST = 36864, WS_FH = 40960, WS_FY = 69632, WS_STATS = 131072, WS_CSUM = 196608,
              WS_KVT = 1u << 20, WS_YT = 3u << 20, WS_H = 5u << 20, WS_BYTES = 13u << 20;
constexpr u32 FLAG_STRIDE = 128;
constexpr u32 L_SLOT = 32768;
constexpr u32 L_HC = 98304, HC_STRIDE = 4160, L_U = L_HC + 8 * HC_STRIDE, U_STRIDE = 4608;
constexpr u32 L_PK = L_U + 2 * U_STRIDE, L_PV = L_PK + 72 * 128, L_PW = L_PV + 64 * 128;
constexpr u32 L_JUNK = L_PW + 1024, L_CNT0 = L_JUNK + 256, L_B2 = L_CNT0 + 256, L_TC = L_B2 + 256;
constexpr u32 L_Y = L_PK, L_X = L_PK;
constexpr u32 LDS_BYTES = 163840;
static_assert(L_JUNK + 256 <= LDS_BYTES, "LDS map");
constexpr u32 SPIN_CAP = 1u << 16;

struct Params {
  const u16 *x, *k, *v, *sk, *ssm, *dd, *w1, *b1, *w2, *b2;
  u16* out;
  unsigned char* ws;
};

DI srd_t mk_srd(const void* p, u32 bytes) { return __builtin_amdgcn_make_buffer_rsrc((void*)p, (short)0, (int)bytes, 0x00020000); }
DI float asf(u32 d) { return __builtin_bit_cast(float, d); }
DI u32 asu(float f) { return __builtin_bit_cast(u32, f); }
DI bool all_lanes(bool c) { return __builtin_amdgcn_ballot_w64(!c) == 0; }
DI float bl(u32 d) { return asf(d << 16); }
DI float bh(u32 d) { return asf(d & 0xffff0000u); }
DI u32 pk(float a, float b) { f2 t = {a, b}; return __builtin_bit_cast(u32, __builtin_convertvector(t, bf2)); }
DI float rest(float v) { return v - bh(asu(v)); }
DI u32 pkt(float lo, float hi) { return __builtin_amdgcn_perm(asu(hi), asu(lo), 0x07060302u); }
DI f4 mfma(v4u a, v4u b, f4 c) { return __builtin_amdgcn_mfma_f32_16x16x32_bf16(__builtin_bit_cast(s8v, a), __builtin_bit_cast(s8v, b), c, 0, 0, 0); }
DI f16v mfma32(v4u a, v4u b, f16v c) { return __builtin_amdgcn_mfma_f32_32x32x16_bf16(__builtin_bit_cast(s8v, a), __builtin_bit_cast(s8v, b), c, 0, 0, 0); }
DI lptr lp(u32 a) { return (lptr)(uintptr_t)a; }
DI v2u trread(u32 a) { return __builtin_bit_cast(v2u, __builtin_amdgcn_ds_read_tr16_b64_v4i16((LAS s4v*)lp(a))); }
DI v4u trfrag(u32 a, u32 hi_off) { const v2u lo = trread(a), hi = trread(a + hi_off); return v4u{lo.x, lo.y, hi.x, hi.y}; }
DI v4u lr128(u32 a) { return *(LAS v4u*)lp(a); }
DI f4 lrf4(u32 a) { return *(LAS f4*)lp(a); }
DI void lw128(u32 a, v4u v) { *(LAS v4u*)lp(a) = v; }
DI void lwf4(u32 a, f4 v) { *(LAS f4*)lp(a) = v; }
#define DMA_(OP, FL, rs, voff, sb, SI, lb, LI) do { u32 t_; \
  asm volatile("s_add_u32 m0, %1, %2\n\ts_add_u32 %0, %3, %4\n\t" OP " %5, %6, %0 offen" FL " lds" \
               : "=&s"(t_) : "s"(lb), "i"(LI), "s"(sb), "i"(SI), "v"(voff), "s"(rs) : "memory", "scc"); } while (0)
#define DFL ""
#define DMA(rs, voff, sb, SI, lb, LI) DMA_("buffer_load_dwordx4", DFL, rs, voff, sb, SI, lb, LI)
#define SSM_FL " nt"
#define DMANT(rs, voff, sb, SI, lb, LI) DMA_("buffer_load_dwordx4", " nt", rs, voff, sb, SI, lb, LI)
#define M0SET(lb, LI) asm volatile("s_add_u32 m0, %0, %1\n\ts_nop 0" ::"s"(lb), "i"(LI) : "memory", "scc")
#define DML(rs, voff, so, OFF) asm volatile("buffer_load_dwordx4 %0, %1, %2 offen offset:%3 lds" ::"v"(voff), "s"(rs), "s"(so), "i"(OFF) : "memory")
#define TOUCH(rs, voff, sb, SI) DMA_("buffer_load_dword", "", rs, voff, sb, SI, 0u, L_JUNK)
#define ALD128(d, off, rs, FL) asm volatile("buffer_load_dwordx4 %0, %1, %2, 0 offen" FL : "=v"(d) : "v"(off), "s"(rs) : "memory")
#define ALD128S(d, off, rs, so, FL) asm volatile("buffer_load_dwordx4 %0, %1, %2, %3 offen" FL : "=v"(d) : "v"(off), "s"(rs), "s"(so) : "memory")
#define ALD64(d, off, rs, FL) asm volatile("buffer_load_dwordx2 %0, %1, %2, 0 offen" FL : "=v"(d) : "v"(off), "s"(rs) : "memory")
#define ALD32(d, off, rs, FL) asm volatile("buffer_load_dword %0, %1, %2, 0 offen" FL : "=v"(d) : "v"(off), "s"(rs) : "memory")
#define ALD16(d, off, rs, FL) asm volatile("buffer_load_ushort %0, %1, %2, 0 offen" FL : "=v"(d) : "v"(off), "s"(rs) : "memory")
#define WAITV(n) asm volatile("s_waitcnt vmcnt(" #n ")" ::: "memory")
#define PIN(a) asm volatile("" : "+v"(a)::"memory")
#define KEEP(a) asm volatile("" : "+v"(a))
#define KEEPP(a) asm volatile("" : "+s"(a))
#define KEEPS(a) do { a = __builtin_amdgcn_readfirstlane(a); asm volatile("" : "+s"(a)); } while (0)
#define WAITL() asm volatile("s_waitcnt lgkmcnt(0)" ::: "memory")
#define BAR() __builtin_amdgcn_s_barrier()
#define VBAR(n) do { __builtin_amdgcn_s_waitcnt(0xC07F); asm volatile("s_waitcnt vmcnt(" #n ") lgkmcnt(0)\n\ts_barrier" ::: "memory"); } while (0)
#define LBAR() do { __builtin_amdgcn_s_waitcnt(0xC07F); asm volatile("s_waitcnt lgkmcnt(0)\n\ts_barrier" ::: "memory"); } while (0)
#define SB() __builtin_amdgcn_sched_barrier(0)
DI void wave_sum2(float& a, float& b) {
  const auto x = __builtin_amdgcn_permlane32_swap(asu(a), asu(b), false, false);
  float v = asf(x[0]) + asf(x[1]);
  const auto y = __builtin_amdgcn_permlane16_swap(asu(v), asu(v), false, false);
  v = asf(y[0]) + asf(y[1]);
  v += asf(__builtin_amdgcn_update_dpp(0u, asu(v), 0xB1, 0xF, 0xF, true));
  v += asf(__builtin_amdgcn_update_dpp(0u, asu(v), 0x4E, 0xF, 0xF, true));
  v += asf(__builtin_amdgcn_update_dpp(0u, asu(v), 0x141, 0xF, 0xF, true));
  v += asf(__builtin_amdgcn_update_dpp(0u, asu(v), 0x140, 0xF, 0xF, true));
  const auto z = __builtin_amdgcn_permlane32_swap(asu(v), asu(v), false, false);
  a = asf(z[0]); b = asf(z[1]);
}
DI v4u ld_sc1(srd_t rs, u32 off, u32 soff = 0) { return __builtin_amdgcn_raw_buffer_load_b128(rs, off, soff, 16); }
template <int IMM = 0> DI void st_sc1(srd_t rs, u32 off, v4u v, u32 soff = 0) {
  asm volatile("buffer_store_dwordx4 %0, %1, %2, %3 offen offset:%4 sc1\n\ts_nop 1" ::"v"(v), "v"(off), "s"(rs), "s"(soff), "i"(IMM) : "memory");
}
DI void put_flag(srd_t rs, u32 l0, u32 off, u64 tag) { __builtin_amdgcn_raw_buffer_store_b64(__builtin_bit_cast(v2u, tag), rs, l0, off, 16); }
DI u32 get_flag(srd_t rs, u32 off) { return __builtin_amdgcn_raw_buffer_load_b32(rs, off, 0, 16); }
DI void wait_flags(srd_t rs, u32 off, u32 tagv, u32 v) {
  if (!all_lanes(v == tagv)) {
#pragma unroll 1
    for (u32 spins = 0; spins < SPIN_CAP; ++spins) {
      u32 x;
      asm volatile("buffer_load_dword %0, %1, %2, 0 offen sc1\n\ts_waitcnt vmcnt(0)" : "=&v"(x) : "v"(off), "s"(rs) : "memory");
      if (all_lanes(x == tagv)) break;
      __builtin_amdgcn_s_sleep(1);
    }
  }
  asm volatile("" ::: "memory");
}
DI void arrive_publish(u32 cnt, srd_t rs, u32 l0, u32 off, u64 tag) {
  u32 old;
  asm volatile("s_mov_b64 exec, 1\n\tds_add_rtn_u32 %0, %1, %2\n\ts_mov_b64 exec, -1\n\ts_waitcnt lgkmcnt(0)" : "=&v"(old) : "v"(cnt), "v"(1u) : "memory");
  if (__builtin_amdgcn_readfirstlane(old) == 3u) put_flag(rs, l0, off, tag);
}
#define STAMP_AT(i)
#define STAMP_F(i)
struct ConvList { int kh[144], e[144]; bool newA[144], newB[144]; };
constexpr int KEEP_MASK[2] = {0x99, 0x66};
constexpr int TS[2][4] = {{0, 3, 4, 7}, {1, 2, 5, 6}};
constexpr ConvList make_list(int role) {
  ConvList L = {};
  int n = 0;
  for (int kl = 0; kl < 4; ++kl) {
    bool seen[16] = {};
    for (int part = 0; part < (kl == 3 ? 2 : 1); ++part) {
      const int mask = kl < 3 ? 0xff : KEEP_MASK[part ? role : role ^ 1];
      for (int kh = 0; kh < 8; ++kh)
        for (int T = 7; T >= kh; --T) {
          if (!((mask >> T) & 1)) continue;
          const int e = T - kh;
          L.kh[n] = kh; L.e[n] = e; L.newA[n] = !seen[kh]; L.newB[n] = !seen[8 + e];
          seen[kh] = seen[8 + e] = true;
          ++n;
        }
    }
  }
  return L;
}
template <class F, int... I> DI void static_for_(F&& f, std::integer_sequence<int, I...>) { (f(std::integral_constant<int, I>{}), ...); }
template <int N, class F> DI void static_for(F&& f) { static_for_(f, std::make_integer_sequence<int, N>{}); }
#define EARLY 1
#define CLA 10
#define NPF 0
#define ZX 1
#define ZW1 1
#define ZW2 1

__global__ void __launch_bounds__(512, 2) fused(Params p) {
  const int tid = __builtin_amdgcn_workitem_id_x(), lane = tid & 63, w = __builtin_amdgcn_readfirstlane(tid >> 6);
  const int b = __builtin_amdgcn_workgroup_id_x(), xcd = b & 7, idx = b >> 3;
  const int g = lane >> 4, r16 = lane & 15, q4 = r16 >> 2, p4 = lane & 3, rr = lane >> 3;
  STAMP_AT(0);
  if (w == 0) {
    u64 pc; u32 len;
    asm volatile("mk_code_begin:\n\ts_getpc_b64 %0\n\ts_mov_b32 %1, mk_code_end-mk_code_begin\n\ts_max_i32 %1, %1, mk_code_end2-mk_code_begin" : "=s"(pc), "=s"(len) :: "scc");
    const srd_t rc = mk_srd((const void*)pc, len);
    TOUCH(rc, (u32)(lane * 128), (u32)((idx & 3) * 8192), 0);
  }
  SB();
  const int cp = 32 * xcd + idx;
  const bool is_g1 = idx < 28;
  u32 w1k = w * 1024, w4k = w * 4096;
  const int stok = 256 * xcd + 8 * idx + w;
  const int sidx = idx & 3, ti = 4 * (xcd >> 1) + sidx, t1 = idx >> 2, tj = 7 * (xcd & 1) + t1;
  const int hm = 4 * (xcd >> 1) + sidx, cgp = 14 * (xcd & 1) + (hm < 14 ? hm : 0);
  const int tm = 4 * xcd + sidx, tn = t1 ^ 4;
  const int mi = w & 3, ni = w >> 2, hl = lane >> 5, l31 = lane & 31;
  const int m2 = w & 1, nq = w >> 1;
  const u32 chsw = (u32)(((lane & 7) ^ (rr & 6)) << 4);
  u32 xro = (u32)(stok * 1024 + lane * 16);
  u32 xvo = (u32)((128 * ti + 16 * w + rr) * 1024 + (((lane & 7) ^ g) << 4)), xvo1 = (xvo ^ 64) + 7168;
  u32 wvo = (u32)((8 * w + (is_g1 ? g : rr)) * 3584 + (is_g1 ? tj * 256 : cgp * 128)) + (is_g1 ? (u32)((r16 ^ (g << 2)) << 4) : chsw), wvo1 = wvo + 13312;
#define ROT1 ((4 * ((t1 + sidx) & 1) + xcd) & 7)
  u32 sox = (u32)(ROT1) * 128, sow = sox * 1792;
  u32 kvo = (u32)(rr * 1024 + (((lane & 7) ^ (rr & 2)) << 4)), kso = (u32)((64 * idx + 8 * w) * 1024 + xcd * 128);
  u32 l0 = lane == 0 ? 0u : 0x80000000u, l01 = lane < 2 ? lane * 16 : 0x80000000u;
  u32 s_st = WS_STATS + (u32)stok * 32;
  u32 xa[2][4], wa[2];
#pragma unroll
  for (int ks = 0; ks < 4; ++ks) { xa[0][ks] = (4 * m2 + (g & 1)) * 4096 + r16 * 128 + (((2 * ks + hl) ^ (r16 >> 1)) << 4); xa[1][ks] = xa[0][ks] + 2 * L_SLOT; }
  wa[0] = 2048 + hl * 4096 + q4 * 256 + (((4 * nq + 2 * (g & 1) + (p4 >> 1)) ^ (q4 << 2)) << 4) + 8 * (p4 & 1);
  wa[1] = wa[0] + 2 * L_SLOT;
  KEEP(xro); KEEP(xvo); KEEP(xvo1); KEEP(wvo); KEEP(wvo1); KEEPS(sox); KEEPS(sow); KEEPS(w1k); KEEPS(w4k);
  KEEP(l0); KEEP(l01); KEEPS(s_st); KEEP(kvo); KEEPS(kso); KEEP(wa[0]); KEEP(wa[1]);
#pragma unroll
  for (int i = 0; i < 4; ++i) { KEEP(xa[0][i]); KEEP(xa[1][i]); }
  SB();
  KEEPP(p.ws); KEEPP(p.out); KEEPP(p.w2); KEEPP(p.b2); KEEPP(p.k); KEEPP(p.x); KEEPP(p.w1);
  const srd_t rx = mk_srd(p.x, S * D * 2 * ZX), rw1 = mk_srd(p.w1 - 1024, D * F * 2 * ZW1 + 2048);
  v4u xr;
  ALD128(xr, xro, rx, "");
#define NEXT_CHUNK() do { sox = (sox + 128) & 1023; sow = sox * 1792; } while (0)
#define PIECE(c, i) do { \
    if ((i) == 0) { M0SET(w4k, ((c) % 3) * L_SLOT); DML(rx, xvo, sox, 0); } \
    if ((i) == 1) DML(rx, xvo1, sox, 1024); \
    if ((i) == 2) DML(rw1, wvo, sow, 2048); \
    if ((i) == 3) { DML(rw1, wvo1, sow, 3072); NEXT_CHUNK(); } } while (0)
#define HPIECE(c) DMA(rw1, wvo, 0u, (c) * 64 * 3584 + 2048, w1k, (c) * 8192)
  if (is_g1) { PIECE(0, 0); PIECE(0, 1); PIECE(0, 2); PIECE(0, 3); }
  else { HPIECE(0); HPIECE(1); HPIECE(2); HPIECE(3); }
  SB();
  u64 tag = *(const CAS u64*)(p.ws + WS_EPOCH);
  u32 ddp = *(const CAS u32*)(p.dd + 2 * cp);
  v2u hq = *(const CAS v2u*)(p.ssm + cp * 4096 + ((w & 3) ? w * 512 - 4 : 0));
  SB();
  const srd_t rws = mk_srd(p.ws, WS_BYTES);
#define STAGE_K() do { const srd_t rk = mk_srd(p.k, S * D * 2); DMANT(rk, kvo, kso, 0, w1k, L_PK + 1024); \
    if (w == 0) DMANT(rk, kvo + kso - 8192u, 0u, 0, 0u, L_PK); } while (0)
#define STAGE_V() do { const srd_t rv = mk_srd(p.v, S * D * 2); DMANT(rv, kvo, kso, 0, w1k, L_PV); \
    if (w == 1) { const srd_t rsk = mk_srd(p.sk, D * 4 * 2); DMA(rsk, (u32)(lane * 16), (u32)(xcd * 512), 0, 0u, L_PW); } \
    if (w == 2) { const srd_t rb2 = mk_srd(p.b2, D * 2); DMA_("buffer_load_dword", "", rb2, (u32)(lane * 4), (u32)(tn * 128), 0, 0u, L_B2); } } while (0)
  constexpr u32 TR_HI = 1024;
  const srd_t rw2 = mk_srd(p.w2, F * D * 2 * ZW2), rssm = mk_srd(p.ssm + cp * 4096, 8192);
  u32 a_kv, a_tap, o_kv, s_kv, w2vo[4], w2base, ksh[7], s_t0, ta[4], t16, a_hc, fo1, fo2, o_xe, o_we, o_out, o_h, s_h, hb, s_cp, tagv;
  v2u ck[3], cv[2], ctw;
  float cw[4], kk[12], vv[8], co[8];
  const u32 l16 = lane * 16;
  auto fill = [&](auto KK) {
    constexpr int K = decltype(KK)::value;
    if constexpr (K == 200) { s_t0 = (u32)((224 * t1 + 8 * sidx) * 1024) + (ksh[0] << 5); KEEPS(s_t0); }
    if constexpr (K == 201) { a_kv = L_PK + 512 + ni * 4096 + (8 * g + q4) * 128 + (((2 * mi + (p4 >> 1)) ^ (q4 & 2)) << 4) + 8 * (p4 & 1); KEEP(a_kv); }
    if constexpr (K == 202) { a_tap = L_PW + mi * 128 + r16 * 8; KEEP(a_tap);
#pragma unroll
      for (int i = 0; i < 4; ++i) { w2vo[i] = (u32)(rr * 1024 + i * 7168) + chsw; KEEP(w2vo[i]); } }
    if constexpr (K == 203) { o_kv = (u32)((r16 >> 1) * 8192 + (r16 & 1) * 64 + (3 - g) * 16); KEEP(o_kv); }
    if constexpr (K == 204) { s_kv = WS_KVT + (u32)((32 * xcd + 8 * mi) * 8192 + (63 - 2 * idx - ni) * 128); KEEPS(s_kv); }
    if constexpr (K == 205) {
      if (w == 2) lw128(L_U + hl * U_STRIDE + 4096 + l31 * 16, v4u{0, 0, 0, 0});
      if (w == 3) lw128(L_CNT0 + (lane & 1) * 16, v4u{0, 0, 0, 0});
    }
    if constexpr (K == 9) { const int ia = 8 * sidx * 8 + xcd, ib = (16 * (ti & 1) + 8 * m2) * 8 + (ti >> 1);
                            fo1 = ((lane & (is_g1 ? 8 : 0)) ? ib : ia) + 8 * (lane & 7); KEEP(fo1); }
    if constexpr (K == 13) { const u32 fkv = (31 - 4 * mi - p4 - 4 * (lane & 4)) * 8 + xcd; fo1 = (lane & 16) ? fkv : fo1; KEEP(fo1); }
    if constexpr (K == 17) { const int m = 2 * t1 + ni; const u32 fc = (WS_FST - WS_FKV) / FLAG_STRIDE + ((m >> 2) * 2 + (xcd & 1)) * 4 + (m & 3);
                             fo1 = (lane & 24) == (is_g1 ? 24 : 64) ? fc : fo1; fo1 = WS_FKV + fo1 * FLAG_STRIDE + 4 * hl; KEEP(fo1); }
    if constexpr (K == 21) { tagv = hl ? (u32)(tag >> 32) : (u32)tag; KEEP(tagv); }
    if constexpr (K == 29) { t16 = tid * 16; o_out = (u32)((64 * tm + 8 * w) * 1024 + 128 * tn) + rr * 1024 + (lane & 7) * 16; KEEP(t16); KEEP(o_out); }
    if constexpr (K == 33) { o_xe = WS_STATS + (u32)((128 * ti + 64 * m2) * 32) + lane * 32; o_we = WS_CSUM + (u32)((128 * tj + 32 * nq) * 16) + l31 * 16; KEEP(o_xe); KEEP(o_we); }
    if constexpr (K == 37) { const int trow = 16 * hl + 4 * (g & 1) + q4; ta[0] = L_SLOT + w4k + trow * 128 + (((p4 >> 1) ^ (trow & 6)) << 4) + 8 * (p4 & 1); KEEP(ta[0]); }
    if constexpr (K == 41) { ta[1] = ta[0] ^ 32; ta[2] = ta[0] ^ 64; ta[3] = ta[0] ^ 96; KEEP(ta[1]); KEEP(ta[2]); KEEP(ta[3]);
                             a_hc = L_HC + ni * (4 * HC_STRIDE - 4096) + 32 + t16; KEEP(a_hc); }
    if constexpr (K == 45) { o_h = (u32)((g & 1) * 57344 + r16 * 16 + hl * 256); KEEP(o_h); }
    if constexpr (K == 49) { s_h = WS_H + (u32)(((8 * ti + 4 * m2) * 56 + 4 * tj + nq) * 1024); KEEPS(s_h); }
    if constexpr (K == 100) {
      ctw = *(LAS v2u*)lp(a_tap);
      ck[0] = trread(a_kv); ck[1] = trread(a_kv + 512); ck[2] = trread(a_kv + 1024);
      cv[0] = trread(a_kv + (L_PV - L_PK - 512)); cv[1] = trread(a_kv + (L_PV - L_PK));
    }
    if constexpr (K == 102) {
      cw[0] = bl(ctw.x); cw[1] = bh(ctw.x); cw[2] = bl(ctw.y); cw[3] = bh(ctw.y);
      kk[1] = bh(ck[0].x); kk[2] = bl(ck[0].y); kk[3] = bh(ck[0].y);
#pragma unroll
      for (int i = 0; i < 4; ++i) KEEP(cw[i]);
#pragma unroll
      for (int i = 1; i < 4; ++i) KEEP(kk[i]);
    }
    if constexpr (K >= 103 && K < 111) {
      constexpr int j = K - 103;
      const u32 kd = ck[1 + j / 4][(j / 2) & 1], vd = cv[j / 4][(j / 2) & 1];
      kk[j + 4] = (j & 1) ? bh(kd) : bl(kd); vv[j] = (j & 1) ? bh(vd) : bl(vd);
      asm volatile("v_mul_f32 %0, %1, %5\n\tv_fmac_f32 %0, %2, %6\n\tv_fmac_f32 %0, %3, %7\n\tv_fmac_f32 %0, %4, %8\n\tv_mul_f32 %0, %0, %9"
                   : "=&v"(co[j]) : "v"(kk[j + 1]), "v"(kk[j + 2]), "v"(kk[j + 3]), "v"(kk[j + 4]), "v"(cw[3]), "v"(cw[2]), "v"(cw[1]), "v"(cw[0]), "v"(vv[j]));
    }
    if constexpr (K == 111) st_sc1(rws, o_kv, v4u{pk(co[7], co[6]), pk(co[5], co[4]), pk(co[3], co[2]), pk(co[1], co[0])}, s_kv);
  };
  SB();
  { int t = xcd;
    KEEPS(t);
#pragma unroll
    for (int s = 0; s < 7; ++s) { t = t >= 7 ? t - 7 : t; ksh[s] = (u32)t * 1024; KEEPS(ksh[s]); ++t; } }
  w2base = (u32)(224 * w * 1024 + tn * 128);
  s_cp = (u32)cp * 8192;
  KEEPS(w2base); KEEPS(s_cp);
  static_for<6>([&](auto I) { fill(std::integral_constant<int, 200 + decltype(I)::value>{}); });
  SB();
  BAR();
  STAGE_K(); STAGE_V();
  static_for<50>([&](auto I) { if constexpr (decltype(I)::value != 21) fill(I); });
  SB();
  WAITV(6);
  PIN(xr);
  STAMP_AT(1);
  {
    float s1 = 0.f, s2 = 0.f;
    u32 one2 = 0x3f803f80u;
    KEEP(one2);
#pragma unroll
    for (int j = 0; j < 4; ++j) {
      const u32 xj = xr[j];
      const bf2 v = __builtin_bit_cast(bf2, xj);
      s1 = __builtin_amdgcn_fdot2_f32_bf16(v, __builtin_bit_cast(bf2, one2), s1, false);
      s2 = __builtin_amdgcn_fdot2_f32_bf16(v, v, s2, false);
    }
    wave_sum2(s1, s2);
    const float mean = s1 * (1.0f / D), var = __builtin_fmaxf(s2 * (1.0f / D) - mean * mean, 0.f);
    const float iA = __builtin_amdgcn_sqrtf(var + EPS * (var + EPS));
    const float m0 = -mean, m1 = rest(m0), m2 = rest(m1);
    u32 d0 = pkt(m0, m0), d1 = pkt(m1, m1);
    const u32 mv = pk(mean, var);
    float rq = __builtin_amdgcn_rcpf(bl(pk(__builtin_amdgcn_sqrtf(bl(pk(bh(mv) + EPS, 0.f))), 0.f))), mq = -bl(mv) * rq;
    KEEP(d0); KEEP(d1); KEEP(rq); KEEP(mq);
    const bool l1 = lane & 1;
    st_sc1(rws, l01, v4u{l1 ? asu(rq) : d0, l1 ? asu(mq) : d1, pkt(m2, m0), pkt(iA, rest(iA))}, s_st);
  }
  SB();
  if (is_g1) { PIECE(1, 0); PIECE(1, 1); PIECE(1, 2); PIECE(1, 3); }
  else { HPIECE(4); HPIECE(5); HPIECE(6); HPIECE(7); }
  v4u hd;
  ALD128(hd, (u32)(tid * 16), rssm, SSM_FL);
  if (w == 6) TOUCH(rw2, (u32)(lane * 128), s_t0, 0);
#define MAKE_KV() static_for<12>([&](auto I) { fill(std::integral_constant<int, 100 + decltype(I)::value>{}); })
  SB();
  tag += 0x9E3779B97F4A7C15ull;
  fill(std::integral_constant<int, 21>{});
  STAMP_AT(2);
  v4u w2f[7][4];
  u32 sw2;
#define W2SLOT(s) ((s) == 0 ? 3 : ((s) + 1) % 3)
#define W2_PIECE(s, i) do { if ((i) == 0) { sw2 = w2base + (ksh[s] << 5); M0SET(w4k, W2SLOT(s) * L_SLOT); } DML(rw2, w2vo[i], sw2, (i) * 1024); } while (0)
#define W2_STAGE(s) do { W2_PIECE(s, 0); W2_PIECE(s, 1); W2_PIECE(s, 2); W2_PIECE(s, 3); } while (0)
  v4u xv; v2u stv;
  v4u u0, u1;
#define LOAD_U() do { ALD128S(u0, t16, rws, s_cp + WS_KVT, " sc1"); ALD128S(u1, t16, rws, s_cp + WS_KVT + 4096, " sc1"); } while (0)
#define LOAD_LAST() ALD128(xv, o_out, rx, "")
#define LOAD_TC() ALD64(stv, WS_STATS + 16 + (64 * tm + lane) * 32, rws, " sc1")
  auto w2_read = [&](int s) {
#pragma unroll
    for (int t = 0; t < 4; ++t) w2f[s][t] = trfrag(ta[t] + W2SLOT(s) * L_SLOT - L_SLOT, TR_HI);
  };
  auto write_hc = [&](int rho) {
    const u32 p0 = __builtin_amdgcn_update_dpp(mi ? hq.x : 0u, hd.z, 0x138, 0xF, 0xF, false), p1 = __builtin_amdgcn_update_dpp(mi ? hq.y : 0u, hd.w, 0x138, 0xF, 0xF, false);
    const u32 a0 = __builtin_amdgcn_alignbit(p1, p0, 16), a1 = __builtin_amdgcn_alignbit(hd.x, p1, 16), a2 = __builtin_amdgcn_alignbit(hd.y, hd.x, 16),
              a3 = __builtin_amdgcn_alignbit(hd.z, hd.y, 16), a4 = __builtin_amdgcn_alignbit(hd.w, hd.z, 16);
    if (rho == 0) lw128(a_hc, hd);
    if (rho == 1) lw128(a_hc + HC_STRIDE, v4u{a1, a2, a3, a4});
    if (rho == 2) lw128(a_hc + 2 * HC_STRIDE, v4u{p1, hd.x, hd.y, hd.z});
    if (rho == 3) lw128(a_hc + 3 * HC_STRIDE, v4u{a0, a1, a2, a3});
  };

#define CONV_SETUP() do { PIN(hd); write_hc(0); write_hc(1); write_hc(2); write_hc(3); \
    if (w == 4) lw128(L_HC + rr * HC_STRIDE + (lane & 1) * 16, v4u{0, 0, 0, 0}); } while (0)

  if (__builtin_expect(is_g1, 1)) {
    f16v acc[2];
#pragma unroll
    for (int i = 0; i < 16; ++i) { acc[0][i] = 0.f; acc[1][i] = 0.f; }
    v4u xf[2][2][2], wf[2][2];
#define REQ(dc, i) do { if ((dc) >= 8) W2_PIECE((dc) & 3, i); else PIECE(dc, i); } while (0)
#define STEP(st, dc, d0, nd) do { \
      constexpr int hs_ = (st) & 1, hn_ = hs_ ^ 1, sl_ = (((st) + 1) >> 1) % 3, sb_ = sl_ >> 1; constexpr u32 so_ = (sl_ & 1) * L_SLOT; \
      constexpr bool rd_ = (st) < 15, mm_ = (st) >= 0; \
      _Pragma("unroll") for (int i_ = 0; i_ < 4; ++i_) { \
        if (mm_) acc[i_ & 1] = mfma32(wf[hs_][i_ >> 1], xf[hs_][i_ >> 1][i_ & 1], acc[i_ & 1]); \
        if (rd_) { \
          if (!(i_ & 1)) wf[hn_][i_ >> 1] = trfrag(wa[sb_] + so_ + (2 * hn_ + (i_ >> 1)) * 8192, 1024); \
          else { xf[hn_][i_ >> 1][0] = lr128(xa[sb_][2 * hn_ + (i_ >> 1)] + so_); xf[hn_][i_ >> 1][1] = lr128(xa[sb_][2 * hn_ + (i_ >> 1)] + so_ + 8192); } } \
        if ((nd) == 4) { if (i_ == 0) REQ(dc, 0); if (i_ == 1) REQ(dc, 1); if (i_ == 2) REQ(dc, 2); if (i_ == 3) REQ(dc, 3); } \
        if ((nd) == 2) { if (i_ == 0) REQ(dc, d0); if (i_ == 2) REQ(dc, (d0) + 1); } \
        if (mm_ && !EARLY) { if (i_ == 0) fill(std::integral_constant<int, 4 * (st)>{}); if (i_ == 1) fill(std::integral_constant<int, 4 * (st) + 1>{}); \
                   if (i_ == 2) fill(std::integral_constant<int, 4 * (st) + 2>{}); if (i_ == 3) fill(std::integral_constant<int, 4 * (st) + 3>{}); } \
        SB(); } } while (0)
#define ITER(c) do { STAMP_F(16 + (c)); STEP(2 * (c) + 1, (c) + 3, 0, 2); STEP(2 * (c) + 2, (c) + 3, 2, 2); } while (0)
    u32 pv; v4u xe, we;
    VBAR(8);
    STAMP_AT(15);
    STEP(-1, 0, 0, 0);
    STEP(0, 2, 0, 4);
    VBAR(10);
    STAMP_F(30);
    MAKE_KV();
    SB();
    VBAR(6);
    ITER(0);
    VBAR(5);
    ITER(1); W2_PIECE(0, 0); W2_PIECE(0, 1);
    VBAR(6);
    if (w == 0) put_flag(rws, l0, WS_FKV + b * FLAG_STRIDE, tag);
    ITER(2); M0SET(w4k, 3 * L_SLOT); W2_PIECE(0, 2); W2_PIECE(0, 3);
    VBAR(8);
    ITER(3);
    VBAR(6);
    ITER(4);
    ALD32(pv, fo1, rws, " sc1");
    VBAR(5);
    STAMP_F(21);
    STEP(11, 9, 0, 2); STEP(12, 9, 2, 2);
    VBAR(4);
    STAMP_F(22);
    PIN(pv);
    wait_flags(rws, fo1, tagv, pv);
    ALD128(xe, o_xe, rws, " sc1");
    ALD128(we, o_we, rws, " sc1");
    if (!ni) LOAD_U();
    if (w == 3) LOAD_TC();
    STEP(13, 0, 0, 0); w2_read(0); STEP(14, 10, 0, 2);
    LBAR();
    STAMP_F(23);
    STEP(15, 10, 2, 2);
    if (ni) WAITV(4); else WAITV(6);
    PIN(xe); PIN(we);
    {
      const v4u z = {0, 0, 0, 0};
      acc[0] = mfma32(we, hl ? z : xe, acc[0]);
      acc[1] = mfma32(we, hl ? xe : z, acc[1]);
    }
    STAMP_AT(3);
    if (ni) { CONV_SETUP(); SB(); }
    f2 k1v[2], k3v[2], iAv[2];
    { const auto r = __builtin_amdgcn_permlane32_swap(xe.w, xe.w, false, false);
#pragma unroll
      for (int m = 0; m < 2; ++m) {
        const float iA = bl(r[m]) + bh(r[m]), A = __builtin_amdgcn_rcpf(iA), k1s = -2.3022082f * A, k3s = -0.10294324f * A * A * A;
        k1v[m] = f2{k1s, k1s}; k3v[m] = f2{k3s, k3s}; iAv[m] = f2{iA, iA};
      } }
#pragma unroll
    for (int qi = 0; qi < 4; ++qi) {
      const int n = qi >> 1, sp = qi & 1;
      if (qi == 0) W2_PIECE(3, 0);
      if (qi == 1) W2_PIECE(3, 1);
      if (qi == 2) W2_PIECE(3, 2);
      if (qi == 3) W2_PIECE(3, 3);
      v4u o;
#pragma unroll
      for (int j = 0; j < 4; ++j) {
        const f2 a = {acc[n][8 * sp + 2 * j], acc[n][8 * sp + 2 * j + 1]}, k1 = k1v[n], k3 = k3v[n], iA = iAv[n];
        const f2 z = a * __builtin_elementwise_fma(a * a, k3, k1);
        const f2 e = {__builtin_amdgcn_exp2f(z.x), __builtin_amdgcn_exp2f(z.y)};
        const f2 d = __builtin_elementwise_fma(e, iA, iA);
        const f2 hv = a * f2{__builtin_amdgcn_rcpf(d.x), __builtin_amdgcn_rcpf(d.y)};
        o[j] = pk(hv.x, hv.y);
      }
      if (qi == 0) st_sc1<0>(rws, o_h, o, s_h);
      if (qi == 1) st_sc1<512>(rws, o_h, o, s_h);
      if (qi == 2) st_sc1<0>(rws, o_h, o, s_h + 114688);
      if (qi == 3) st_sc1<512>(rws, o_h, o, s_h + 114688);
      SB();
    }
    STAMP_AT(4);
  } else {
    u32 b1v;
    { const srd_t rb1 = mk_srd(p.b1 + 64 * cgp, 128); ALD16(b1v, (u32)(16 * mi + 4 * g + p4) * 2, rb1, ""); }
    VBAR(0);
    STAMP_AT(15);
    if (w < 4) {
      const v4u ones = {0x3f803f80u, 0x3f803f80u, 0x3f803f80u, 0x3f803f80u};
      f4 s0 = {0, 0, 0, 0};
      const u32 a = (w == 0 ? ta[0] : w == 1 ? ta[1] : w == 2 ? ta[2] : ta[3]) - L_SLOT - w4k;
#pragma unroll
      for (int st = 0; st < 16; ++st) s0 = mfma(trfrag(a + st * 4096, TR_HI), ones, s0);
      const float ca = (p4 & 1) ? s0[1] : s0[0], cb = (p4 & 1) ? s0[3] : s0[2], c = (p4 & 2) ? cb : ca;
      const float c1 = rest(c), c2 = rest(c1);
      PIN(b1v);
      st_sc1(rws, r16 < 4 && hm < 14 ? WS_CSUM + (u32)(64 * cgp + 16 * w + 4 * g + p4) * 16 : 0x80000000u, v4u{pkt(c, c1), pkt(c, c1), pkt(c, c2), b1v * 0x10001u});
    }
    MAKE_KV();
    VBAR(0);
    if (w == 0) { put_flag(rws, l0, WS_FST + (4 * xcd + sidx) * FLAG_STRIDE, tag); put_flag(rws, l0, WS_FKV + b * FLAG_STRIDE, tag); }
    STAMP_AT(3);
#pragma unroll
    for (int i = 1; i <= NPF; ++i) { u32 kk = ksh[i]; KEEPS(kk); TOUCH(rw2, (u32)(lane * 128), (u32)((224 * w + 8 * sidx) * 1024) + (kk << 5), 0); }
    W2_STAGE(0); W2_STAGE(1); W2_STAGE(2); W2_STAGE(3);
    if (b == 255) {
      wait_flags(rws, WS_FKV + (32 * w + l31) * FLAG_STRIDE + 4 * hl, tagv, ~tagv);
      BAR();
      if (w == 0) put_flag(rws, l0, WS_EPOCH, tag);
    }
    wait_flags(rws, fo1, tagv, ~tagv);
    w2_read(0);
    STAMP_AT(4);
    LBAR();
    asm volatile("mk_code_end2:");
    if (ni) CONV_SETUP(); else LOAD_U();
    if (w == 3) LOAD_TC();
  }
#define LATE_SETUP() do { const int tja = ((224 * w) >> 7) + p4, tjb = (224 * w + 223) >> 7; \
    fo2 = WS_FH + ((tm >> 1) * 14 + (tja < tjb ? tja : tjb)) * FLAG_STRIDE + 4 * hl; hb = WS_H + (u32)((4 * tm * 56 + 7 * w) * 1024) + l16; KEEP(fo2); KEEP(hb); } while (0)
  v4u hf[7];
  u32 hbm;
#define MROT (2 * tn)
  const int mrot = MROT;
  auto load_h = [&](int m, int s) { if (s == 0) hbm = hb + (u32)(((m + mrot) & 3) * 57344); hf[s] = ld_sc1(rws, hbm, ksh[s]); };
  f4 yacc[4];
  u32 a_x, o_y;
  if (!ni) {
    CONV_SETUP();
    const int rho = (15 - r16) & 3;
    u32 a_A = L_HC + ((mi >> 1) * 4 + rho) * HC_STRIDE + (r16 + 1 + rho + 8 * g) * 2 + (mi & 1) * 256;
    u32 a_B = L_U + (mi >> 1) * U_STRIDE + (240 - 16 * r16 + 8 * g) * 2 + (mi & 1) * 256;
    u32 a_u = L_U + ((tid >> 2) & 1) * U_STRIDE + (tid >> 3) * 64 + (tid & 3) * 16;
    KEEP(a_A); KEEP(a_B); KEEP(a_u);
    LATE_SETUP();
    WAITV(0);
    PIN(u0); PIN(u1);
    lw128(a_u, u0); lw128(a_u + 2048, u1);
    if (w == 3) { PIN(stv); *(LAS v2u*)lp(L_TC + lane * 8) = stv; }
    w2_read(1); w2_read(2); w2_read(3);
    v4u AD;
    { const u32 dv = (mi & 2) ? ddp >> 16 : ddp & 0xffffu; const int pos = 15 - r16 - 8 * g;
#pragma unroll
      for (int j = 0; j < 4; ++j) AD[j] = (pos >> 1) == j ? dv << ((pos & 1) * 16) : 0u; }
    u32 a_B0 = a_B - (mi & 1) * 256;
    a_x = L_X + w4k + l16;
    o_y = s_cp + WS_YT + (mi >> 1) * 4096 + (mi & 1) * 512 + r16 * 32 + hl * 16 + (g & 1) * ((mi & 1) ? 512u : 1536u);
    KEEP(a_x); KEEP(a_B0); KEEP(o_y);
    LBAR();
    STAMP_AT(5);
    u32 pv2;
    auto conv = [&](auto ROLE) {
      constexpr int role = decltype(ROLE)::value;
      constexpr ConvList CL = make_list(role);
      f4 ac[8];
      v4u fa[4][8], fb[4][8], fd[4];
      auto rd = [&](auto N) {
        constexpr int n = decltype(N)::value, kl = n / 36;
        if constexpr (n < 144) {
          if constexpr (CL.newA[n]) {
            const v2u lo = *(volatile LAS v2u*)lp(a_A + CL.kh[n] * 512 + kl * 64), hi = *(volatile LAS v2u*)lp(a_A + CL.kh[n] * 512 + kl * 64 + 8);
            fa[kl][CL.kh[n]] = v4u{lo.x, lo.y, hi.x, hi.y};
          }
          if constexpr (CL.newB[n]) fb[kl][CL.e[n]] = lr128(a_B + (7 - CL.e[n]) * 512 + kl * 64);
        } else if constexpr (n < 148) fd[n - 144] = lr128(a_B0 + (7 - TS[role][n - 144]) * 512);
      };
      static_for<CLA>(rd);
      static_for<144>([&](auto N) {
        constexpr int n = decltype(N)::value, kl = n / 36, T = CL.kh[n] + CL.e[n];
        ac[T] = mfma(fa[kl][CL.kh[n]], fb[kl][CL.e[n]], n < 8 ? f4{0, 0, 0, 0} : ac[T]);
        rd(std::integral_constant<int, n + CLA>{});
        if constexpr (n >= 9 && n < 9 + 12 * 5 && (n - 9) % 5 == 0) { constexpr int q = (n - 9) / 5; W2_PIECE(4 + q / 4, q % 4); }
        if constexpr (n == 80) LOAD_LAST();
        if constexpr (n == 100) ALD32(pv2, fo2, rws, " sc1");
        if constexpr (n >= 128 && n < 136 && (n & 1) == 0) lwf4(a_x + ((n - 128) / 2) * 1024, ac[TS[role ^ 1][(n - 128) / 2]]);
        if constexpr (n == 136) *(volatile LAS u32*)lp(L_CNT0 + 16 + w * 4) = 1u;
        SB();
      });
#pragma unroll
      for (int j = 0; j < 4; ++j) yacc[j] = mfma(AD, fd[j], ac[TS[role][j]]);
    };
    if (mi & 1) conv(std::integral_constant<int, 1>{}); else conv(std::integral_constant<int, 0>{});
    STAMP_AT(8);
    WAITV(0);
    PIN(pv2);
    wait_flags(rws, fo2, tagv, pv2);
  } else {
    LBAR();
    STAMP_AT(5);
    LATE_SETUP();
    WAITV(0);
    if (is_g1) arrive_publish(L_CNT0, rws, l0, WS_FH + (ti * 14 + tj) * FLAG_STRIDE, tag);
    w2_read(1); w2_read(2); w2_read(3);
    WAITL();
    W2_STAGE(4); W2_STAGE(5); W2_STAGE(6);
    LOAD_LAST();
    STAMP_AT(8);
    wait_flags(rws, fo2, tagv, ~tagv);
  }
  STAMP_AT(9);
#pragma unroll
  for (int s = 0; s < 7; ++s) load_h(0, s);
  SB();
  if (!ni) {
#pragma unroll 1
    for (u32 spins = 0; spins < SPIN_CAP && *(volatile LAS u32*)lp(L_CNT0 + 16 + (w ^ 1) * 4) == 0u; ++spins) __builtin_amdgcn_s_sleep(1);
    asm volatile("" ::: "memory");
#pragma unroll
    for (int j = 0; j < 4; ++j) yacc[j] += lrf4(a_x + ((mi & 1) ? -4096 : 4096) + j * 1024);
#pragma unroll
    for (int j = 0; j < 4; j += 2) {
      const auto r0 = __builtin_amdgcn_permlane16_swap(pk(yacc[j][0], yacc[j][1]), pk(yacc[j + 1][0], yacc[j + 1][1]), false, false);
      const auto r1 = __builtin_amdgcn_permlane16_swap(pk(yacc[j][2], yacc[j][3]), pk(yacc[j + 1][2], yacc[j + 1][3]), false, false);
      if (j == 0) st_sc1<0>(rws, o_y, v4u{r0[0], r1[0], r0[1], r1[1]});
      else st_sc1<2048>(rws, o_y, v4u{r0[0], r1[0], r0[1], r1[1]});
    }
  }
  w2_read(4); w2_read(5); w2_read(6);
  STAMP_AT(10);
  const int tok = tid >> 3, chunk = tid & 7;

  u32 fo3 = WS_FY + (32 * tn + 4 * w + p4) * FLAG_STRIDE + 4 * hl, o_yl = WS_YT + (u32)((64 * tn + tok) * 4096 + (64 * tm + 8 * chunk) * 2);
  u32 pp0 = w4k + (((lane & 48) | ((lane + 4 * hl) & 15)) << 4), yp = L_Y + (8 * chunk) * 128 + ((w ^ chunk) << 4) + (tok & 7) * 2;
  u32 rp = (((w >> 1) - mrot) & 3) * L_SLOT + (chunk >> 1) * 1024 + ((32 * (chunk & 1) + ((tok + 4 * (chunk & 3)) & 15)) << 4);
  u32 yr = L_Y + tok * 128 + ((chunk ^ w) << 4);
  KEEP(fo3); KEEP(o_yl); KEEP(pp0); KEEP(yp); KEEP(rp); KEEP(yr);
  PIN(xv);
  v4u b2v; v2u tc; u32 cdone;
  float xx[8], xn[8], bb[8];
  v4u yv;
  {
    u32 pv3 = 0;
    f4 acc[2][4];
#pragma unroll
    for (int m = 0; m < 4; ++m) {
#pragma unroll
      for (int s = 0; s < 7; ++s) {
#pragma unroll
        for (int t = 0; t < 4; ++t) acc[m & 1][t] = mfma(w2f[s][t], hf[s], s == 0 ? f4{0, 0, 0, 0} : acc[m & 1][t]);
        if (m < 3) load_h(m + 1, s);
        if (m == 0 && s == 5 && !ni) {
          SB();
          WAITV(6);
          arrive_publish(L_CNT0 + 4, rws, l0, WS_FY + cp * FLAG_STRIDE, tag);
        }
        if (m > 0 && s >= 1 && s <= 4) lwf4(((pp0 + (m - 1) * L_SLOT) ^ (((s - 1) & 1) * 128)) + (s - 1) * 1024, acc[(m - 1) & 1][s - 1]);
        if (m == 2 && s == 0) pv3 = get_flag(rws, fo3);
        if (m == 3 && s == 1) {
          STAMP_F(28);
          wait_flags(rws, fo3, tagv, pv3);
          STAMP_F(29);
          yv = ld_sc1(rws, o_yl);
        }
        if (m == 3 && s == 2) { b2v = lr128(L_B2 + chunk * 16); tc = *(LAS v2u*)lp(L_TC + tok * 8); cdone = *(volatile LAS u32*)lp(L_CNT0 + 4); }
        if (m == 3 && s >= 3) {
          const int j = s - 3;
          xx[2 * j] = bl(xv[j]); xx[2 * j + 1] = bh(xv[j]); bb[2 * j] = bl(b2v[j]); bb[2 * j + 1] = bh(b2v[j]);
          xn[2 * j] = xx[2 * j] * asf(tc.x) + asf(tc.y); KEEP(xn[2 * j]);
          xn[2 * j + 1] = xx[2 * j + 1] * asf(tc.x) + asf(tc.y); KEEP(xn[2 * j + 1]);
          KEEP(xx[2 * j]); KEEP(xx[2 * j + 1]); KEEP(bb[2 * j]); KEEP(bb[2 * j + 1]);
        }
        SB();
      }
      STAMP_F(24 + m);
    }
    if (__builtin_amdgcn_readfirstlane(cdone) < 4u) {
#pragma unroll 1
      for (u32 spins = 0; spins < SPIN_CAP && *(volatile LAS u32*)lp(L_CNT0 + 4) < 4u; ++spins) __builtin_amdgcn_s_sleep(1);
    }
    asm volatile("" ::: "memory");
#pragma unroll
    for (int t = 0; t < 4; ++t) lwf4(((pp0 + 3 * L_SLOT) ^ ((t & 1) * 128)) + t * 1024, acc[1][t]);
  }
  STAMP_AT(11);
#pragma unroll
  for (int j = 0; j < 4; ++j) {
    *(LAS u16*)lp(yp + (2 * j) * 128) = (u16)(yv[j] & 0xffff);
    *(LAS u16*)lp(yp + (2 * j + 1) * 128) = (u16)(yv[j] >> 16);
  }
  LBAR();
  STAMP_AT(12);
  {
    const v4u yy = lr128(yr);
    const f4 o0 = ((lrf4(rp) + lrf4(rp + 4096)) + (lrf4(rp + 8192) + lrf4(rp + 12288))) +
                  ((lrf4(rp + 16384) + lrf4(rp + 20480)) + (lrf4(rp + 24576) + lrf4(rp + 28672)));
    const f4 o1 = ((lrf4(rp + 256) + lrf4(rp + 4352)) + (lrf4(rp + 8448) + lrf4(rp + 12544))) +
                  ((lrf4(rp + 16640) + lrf4(rp + 20736)) + (lrf4(rp + 24832) + lrf4(rp + 28928)));
    float res[8];
#pragma unroll
    for (int j = 0; j < 8; ++j) {
      const float ov = (j < 4 ? o0[j & 3] : o1[j & 3]) + bb[j];
      const float sg = __builtin_amdgcn_rcpf(1.0f + __builtin_amdgcn_exp2f(ov * -1.4426950409f));
      const float yj = (j & 1) ? bh(yy[j >> 1]) : bl(yy[j >> 1]);
      res[j] = xx[j] + ((xn[j] * yj) * ov) * sg;
    }
#define OUT_AUX 16
    __builtin_amdgcn_raw_buffer_store_b128(v4u{pk(res[0], res[1]), pk(res[2], res[3]), pk(res[4], res[5]), pk(res[6], res[7])}, mk_srd(p.out, S * D * 2), o_out, 0, OUT_AUX);
  }
  asm volatile("mk_code_end:");
}
}

extern "C" void kernel_launch(void* const* d_in, const int* in_sizes, int n_in, void* d_out, int out_size, void* d_ws, size_t ws_size,
                              hipStream_t stream) {
  using namespace mk;
  Params p;
  p.x = (const u16*)d_in[0]; p.k = (const u16*)d_in[1]; p.v = (const u16*)d_in[2]; p.sk = (const u16*)d_in[3];
  p.ssm = (const u16*)d_in[4]; p.dd = (const u16*)d_in[5]; p.w1 = (const u16*)d_in[7]; p.b1 = (const u16*)d_in[8];
  p.w2 = (const u16*)d_in[9]; p.b2 = (const u16*)d_in[10];
  p.out = (u16*)d_out; p.ws = (unsigned char*)d_ws;
  (void)hipFuncSetAttribute((const void*)fused, hipFuncAttributeMaxDynamicSharedMemorySize, LDS_BYTES);
  fused<<<256, 512, LDS_BYTES, stream>>>(p);
}
```

```cpp
#include <hip/hip_runtime.h>
#include <stdint.h>
#include <type_traits>
#include <utility>

namespace mk {
typedef unsigned short u16;
typedef unsigned u32;
typedef unsigned long long u64;
typedef u32 v4u __attribute__((ext_vector_type(4)));
typedef u32 v2u __attribute__((ext_vector_type(2)));
typedef float f4 __attribute__((ext_vector_type(4)));
typedef float f2 __attribute__((ext_vector_type(2)));
typedef float f16v __attribute__((ext_vector_type(16)));
typedef short s8v __attribute__((ext_vector_type(8)));
typedef short s4v __attribute__((ext_vector_type(4)));
typedef __bf16 bf2 __attribute__((ext_vector_type(2)));
#define LAS __attribute__((address_space(3)))
#define CAS __attribute__((address_space(4)))
#define DI __device__ __forceinline__
typedef __amdgpu_buffer_rsrc_t srd_t;
typedef LAS unsigned char* lptr;

constexpr int S = 2048, D = 512, F = 1792;
constexpr float EPS = 1e-5f;
constexpr u32 WS_EPOCH = 0, WS_FKV = 4096, WS_FST = 36864, WS_FH = 40960, WS_FY = 69632, WS_STATS = 131072, WS_CSUM = 196608,
              WS_KVT = 1u << 20, WS_YT = 3u << 20, WS_H = 5u << 20, WS_BYTES = 13u << 20;
constexpr u32 FLAG_STRIDE = 128;
constexpr u32 L_SLOT = 32768;
constexpr u32 L_HC = 98304, HC_STRIDE = 4160, L_U = L_HC + 8 * HC_STRIDE, U_STRIDE = 4608;
constexpr u32 L_PK = L_U + 2 * U_STRIDE, L_PV = L_PK + 72 * 128, L_PW = L_PV + 64 * 128;
constexpr u32 L_JUNK = L_PW + 1024, L_CNT0 = L_JUNK + 256, L_B2 = L_CNT0 + 256, L_TC = L_B2 + 256;
constexpr u32 L_Y = L_PK, L_X = L_PK;
constexpr u32 LDS_BYTES = 163840;
static_assert(L_TC + 512 <= LDS_BYTES, "LDS map");
constexpr u32 SPIN_CAP = 1u << 16;

struct Params {
  const u16 *x, *k, *v, *sk, *ssm, *dd, *w1, *b1, *w2, *b2;
  u16* out;
  unsigned char* ws;
};

DI srd_t mk_srd(const void* p, u32 bytes) { return __builtin_amdgcn_make_buffer_rsrc((void*)p, (short)0, (int)bytes, 0x00020000); }
DI float asf(u32 d) { return __builtin_bit_cast(float, d); }
DI u32 asu(float f) { return __builtin_bit_cast(u32, f); }
DI bool all_lanes(bool c) { return __builtin_amdgcn_ballot_w64(!c) == 0; }
DI float bl(u32 d) { return asf(d << 16); }
DI float bh(u32 d) { return asf(d & 0xffff0000u); }
DI u32 pk(float a, float b) { f2 t = {a, b}; return __builtin_bit_cast(u32, __builtin_convertvector(t, bf2)); }
DI float rest(float v) { return v - bh(asu(v)); }
DI u32 pkt(float lo, float hi) { return __builtin_amdgcn_perm(asu(hi), asu(lo), 0x07060302u); }
DI f4 mfma(v4u a, v4u b, f4 c) { return __builtin_amdgcn_mfma_f32_16x16x32_bf16(__builtin_bit_cast(s8v, a), __builtin_bit_cast(s8v, b), c, 0, 0, 0); }
DI f16v mfma32(v4u a, v4u b, f16v c) { return __builtin_amdgcn_mfma_f32_32x32x16_bf16(__builtin_bit_cast(s8v, a), __builtin_bit_cast(s8v, b), c, 0, 0, 0); }
DI lptr lp(u32 a) { return (lptr)(uintptr_t)a; }
DI v2u trread(u32 a) { return __builtin_bit_cast(v2u, __builtin_amdgcn_ds_read_tr16_b64_v4i16((LAS s4v*)lp(a))); }
DI v4u trfrag(u32 a, u32 hi_off) { const v2u lo = trread(a), hi = trread(a + hi_off); return v4u{lo.x, lo.y, hi.x, hi.y}; }
DI v4u lr128(u32 a) { return *(LAS v4u*)lp(a); }
DI f4 lrf4(u32 a) { return *(LAS f4*)lp(a); }
DI void lw128(u32 a, v4u v) { *(LAS v4u*)lp(a) = v; }
DI void lwf4(u32 a, f4 v) { *(LAS f4*)lp(a) = v; }
#define DMA_(OP, FL, rs, voff, sb, SI, lb, LI) do { u32 t_; \
  asm volatile("s_add_u32 m0, %1, %2\n\ts_add_u32 %0, %3, %4\n\t" OP " %5, %6, %0 offen" FL " lds" \
               : "=&s"(t_) : "s"(lb), "i"(LI), "s"(sb), "i"(SI), "v"(voff), "s"(rs) : "memory", "scc"); } while (0)
#define DMA(rs, voff, sb, SI, lb, LI) DMA_("buffer_load_dwordx4", "", rs, voff, sb, SI, lb, LI)
#define DMANT(rs, voff, sb, SI, lb, LI) DMA_("buffer_load_dwordx4", " nt", rs, voff, sb, SI, lb, LI)
#define M0SET(lb, LI) asm volatile("s_add_u32 m0, %0, %1\n\ts_nop 0" ::"s"(lb), "i"(LI) : "memory", "scc")
#define DML(rs, voff, so, OFF) asm volatile("buffer_load_dwordx4 %0, %1, %2 offen offset:%3 lds" ::"v"(voff), "s"(rs), "s"(so), "i"(OFF) : "memory")
#define TOUCH(rs, voff, sb, SI) DMA_("buffer_load_dword", "", rs, voff, sb, SI, 0u, L_JUNK)
#define ALD128(d, off, rs, FL) asm volatile("buffer_load_dwordx4 %0, %1, %2, 0 offen" FL : "=v"(d) : "v"(off), "s"(rs) : "memory")
#define ALD128S(d, off, rs, so, FL) asm volatile("buffer_load_dwordx4 %0, %1, %2, %3 offen" FL : "=v"(d) : "v"(off), "s"(rs), "s"(so) : "memory")
#define ALD64(d, off, rs, FL) asm volatile("buffer_load_dwordx2 %0, %1, %2, 0 offen" FL : "=v"(d) : "v"(off), "s"(rs) : "memory")
#define ALD32(d, off, rs, FL) asm volatile("buffer_load_dword %0, %1, %2, 0 offen" FL : "=v"(d) : "v"(off), "s"(rs) : "memory")
#define ALD16(d, off, rs, FL) asm volatile("buffer_load_ushort %0, %1, %2, 0 offen" FL : "=v"(d) : "v"(off), "s"(rs) : "memory")
#define WAITV(n) asm volatile("s_waitcnt vmcnt(" #n ")" ::: "memory")
#define PIN(a) asm volatile("" : "+v"(a)::"memory")
#define KEEP(a) asm volatile("" : "+v"(a))
#define KEEPP(a) asm volatile("" : "+s"(a))
#define KEEPS(a) do { a = __builtin_amdgcn_readfirstlane(a); asm volatile("" : "+s"(a)); } while (0)
#define WAITL() asm volatile("s_waitcnt lgkmcnt(0)" ::: "memory")
#define BAR() __builtin_amdgcn_s_barrier()
#define VBAR(n) do { __builtin_amdgcn_s_waitcnt(0xC07F); asm volatile("s_waitcnt vmcnt(" #n ") lgkmcnt(0)\n\ts_barrier" ::: "memory"); } while (0)
#define LBAR() do { __builtin_amdgcn_s_waitcnt(0xC07F); asm volatile("s_waitcnt lgkmcnt(0)\n\ts_barrier" ::: "memory"); } while (0)
#define SB() __builtin_amdgcn_sched_barrier(0)
DI void wave_sum2(float& a, float& b) {
  const auto x = __builtin_amdgcn_permlane32_swap(asu(a), asu(b), false, false);
  float v = asf(x[0]) + asf(x[1]);
  const auto y = __builtin_amdgcn_permlane16_swap(asu(v), asu(v), false, false);
  v = asf(y[0]) + asf(y[1]);
  v += asf(__builtin_amdgcn_update_dpp(0u, asu(v), 0xB1, 0xF, 0xF, true));
  v += asf(__builtin_amdgcn_update_dpp(0u, asu(v), 0x4E, 0xF, 0xF, true));
  v += asf(__builtin_amdgcn_update_dpp(0u, asu(v), 0x141, 0xF, 0xF, true));
  v += asf(__builtin_amdgcn_update_dpp(0u, asu(v), 0x140, 0xF, 0xF, true));
  const auto z = __builtin_amdgcn_permlane32_swap(asu(v), asu(v), false, false);
  a = asf(z[0]); b = asf(z[1]);
}
DI v4u ld_sc1(srd_t rs, u32 off, u32 soff = 0) { return __builtin_amdgcn_raw_buffer_load_b128(rs, off, soff, 16); }
template <int IMM = 0> DI void st_sc1(srd_t rs, u32 off, v4u v, u32 soff = 0) {
  asm volatile("buffer_store_dwordx4 %0, %1, %2, %3 offen offset:%4 sc1\n\ts_nop 1" ::"v"(v), "v"(off), "s"(rs), "s"(soff), "i"(IMM) : "memory");
}
DI void put_flag(srd_t rs, u32 l0, u32 off, u32 tagv) { __builtin_amdgcn_raw_buffer_store_b32(tagv, rs, l0, off, 16); }
DI u32 get_flag(srd_t rs, u32 off) { return __builtin_amdgcn_raw_buffer_load_b32(rs, off, 0, 16); }
DI void wait_flags(srd_t rs, u32 off, u32 tagv, u32 v) {
  if (!all_lanes(v == tagv)) {
#pragma unroll 1
    for (u32 spins = 0; spins < SPIN_CAP; ++spins) {
      u32 x;
      asm volatile("buffer_load_dword %0, %1, %2, 0 offen sc1\n\ts_waitcnt vmcnt(0)" : "=&v"(x) : "v"(off), "s"(rs) : "memory");
      if (all_lanes(x == tagv)) break;
      __builtin_amdgcn_s_sleep(1);
    }
  }
  asm volatile("" ::: "memory");
}
DI void arrive_publish(u32 cnt, srd_t rs, u32 l0, u32 off, u32 tag) {
  u32 old;
  asm volatile("s_mov_b64 exec, 1\n\tds_add_rtn_u32 %0, %1, %2\n\ts_mov_b64 exec, -1\n\ts_waitcnt lgkmcnt(0)" : "=&v"(old) : "v"(cnt), "v"(1u) : "memory");
  if (__builtin_amdgcn_readfirstlane(old) == 3u) put_flag(rs, l0, off, tag);
}
struct ConvList { int kh[144], e[144]; bool newA[144], newB[144]; };
constexpr int KEEP_MASK[2] = {0x99, 0x66};
constexpr int TS[2][4] = {{0, 3, 4, 7}, {1, 2, 5, 6}};
constexpr ConvList make_list(int role) {
  ConvList L = {};
  int n = 0;
  for (int kl = 0; kl < 4; ++kl) {
    bool seen[16] = {};
    for (int part = 0; part < (kl == 3 ? 2 : 1); ++part) {
      const int mask = kl < 3 ? 0xff : KEEP_MASK[part ? role : role ^ 1];
      for (int kh = 0; kh < 8; ++kh)
        for (int T = 7; T >= kh; --T) {
          if (!((mask >> T) & 1)) continue;
          const int e = T - kh;
          L.kh[n] = kh; L.e[n] = e; L.newA[n] = !seen[kh]; L.newB[n] = !seen[8 + e];
          seen[kh] = seen[8 + e] = true;
          ++n;
        }
    }
  }
  return L;
}
template <class F, int... I> DI void static_for_(F&& f, std::integer_sequence<int, I...>) { (f(std::integral_constant<int, I>{}), ...); }
template <int N, class F> DI void static_for(F&& f) { static_for_(f, std::make_integer_sequence<int, N>{}); }
constexpr int CLA = 10;

__global__ void __launch_bounds__(512, 2) fused(Params p) {
  const int tid = __builtin_amdgcn_workitem_id_x(), lane = tid & 63, w = __builtin_amdgcn_readfirstlane(tid >> 6);
  const int b = __builtin_amdgcn_workgroup_id_x(), xcd = b & 7, idx = b >> 3;
  const int g = lane >> 4, r16 = lane & 15, q4 = r16 >> 2, p4 = lane & 3, rr = lane >> 3;
  if (w == 0) {
    u64 pc; u32 len;
    asm volatile("mk_code_begin:\n\ts_getpc_b64 %0\n\ts_mov_b32 %1, mk_code_end-mk_code_begin\n\ts_max_i32 %1, %1, mk_code_end2-mk_code_begin" : "=s"(pc), "=s"(len) :: "scc");
    const srd_t rc = mk_srd((const void*)pc, len);
    TOUCH(rc, (u32)(lane * 128 + (idx & 3) * 8192), 0u, 0);
  }
  SB();
  const int cp = 32 * xcd + idx;
  const bool is_g1 = idx < 28;
  u32 w1k = w * 1024, w4k = w * 4096;
  const int stok = 256 * xcd + 8 * idx + w;
  const int sidx = idx & 3, ti = 4 * (xcd >> 1) + sidx, t1 = idx >> 2, tj = 7 * (xcd & 1) + t1;
  const int hm = 4 * (xcd >> 1) + sidx, cgp = 14 * (xcd & 1) + (hm < 14 ? hm : 0);
  const int tm = 4 * xcd + sidx, tn = t1 ^ 4;
  const int mi = w & 3, ni = w >> 2, hl = lane >> 5, l31 = lane & 31;
  const int m2 = w & 1, nq = w >> 1;
  const u32 chsw = (u32)(((lane & 7) ^ (rr & 6)) << 4);
  u32 xro = (u32)(stok * 1024 + lane * 16);
  u32 xvo = (u32)((128 * ti + 16 * w + rr) * 1024 + (((lane & 7) ^ g) << 4)), xvo1 = (xvo ^ 64) + 7168;
  u32 wvo = (u32)((8 * w + (is_g1 ? g : rr)) * 3584 + (is_g1 ? tj * 256 : cgp * 128)) + (is_g1 ? (u32)((r16 ^ (g << 2)) << 4) : chsw), wvo1 = wvo + 13312;
  u32 sox = (u32)((4 * ((t1 + sidx) & 1) + xcd) & 7) * 128, sow = sox * 1792;
  u32 kvo = (u32)(rr * 1024 + (((lane & 7) ^ (rr & 2)) << 4)), kso = (u32)((64 * idx + 8 * w) * 1024 + xcd * 128);
  u32 l0 = l31 == 0 ? 4 * hl : 0x80000000u, l01 = lane < 2 ? lane * 16 : 0x80000000u;
  u32 s_st = WS_STATS + (u32)stok * 32;
  u32 xa[2][4], wa[2];
#pragma unroll
  for (int ks = 0; ks < 4; ++ks) { xa[0][ks] = (4 * m2 + (g & 1)) * 4096 + r16 * 128 + (((2 * ks + hl) ^ (r16 >> 1)) << 4); xa[1][ks] = xa[0][ks] + 2 * L_SLOT; }
  wa[0] = 2048 + hl * 4096 + q4 * 256 + (((4 * nq + 2 * (g & 1) + (p4 >> 1)) ^ (q4 << 2)) << 4) + 8 * (p4 & 1);
  wa[1] = wa[0] + 2 * L_SLOT;
  KEEP(xro); KEEP(xvo); KEEP(xvo1); KEEP(wvo); KEEP(wvo1); KEEPS(sox); KEEPS(sow); KEEPS(w1k); KEEPS(w4k);
  KEEP(l0); KEEP(l01); KEEPS(s_st); KEEP(kvo); KEEPS(kso); KEEP(wa[0]); KEEP(wa[1]);
#pragma unroll
  for (int i = 0; i < 4; ++i) { KEEP(xa[0][i]); KEEP(xa[1][i]); }
  SB();
  KEEPP(p.ws); KEEPP(p.out); KEEPP(p.w2); KEEPP(p.b2); KEEPP(p.k); KEEPP(p.x); KEEPP(p.w1);
  const srd_t rx = mk_srd(p.x, S * D * 2), rw1 = mk_srd(p.w1 - 1024, D * F * 2 + 2048);
  v4u xr;
  const srd_t rws = mk_srd(p.ws, WS_BYTES);
  ALD128(xr, xro, rx, "");
#define NEXT_CHUNK() do { sox = (sox + 128) & 1023; sow = sox * 1792; } while (0)
#define PIECE(c, i) do { \
    if ((i) == 0) { M0SET(w4k, ((c) % 3) * L_SLOT); DML(rx, xvo, sox, 0); } \
    if ((i) == 1) DML(rx, xvo1, sox, 1024); \
    if ((i) == 2) DML(rw1, wvo, sow, 2048); \
    if ((i) == 3) { DML(rw1, wvo1, sow, 3072); NEXT_CHUNK(); } } while (0)
#define HPIECE(c) DMA(rw1, wvo, 0u, (c) * 64 * 3584 + 2048, w1k, (c) * 8192)
  if (is_g1) { PIECE(0, 0); PIECE(0, 1); PIECE(0, 2); PIECE(0, 3); }
  else { HPIECE(0); HPIECE(1); HPIECE(2); HPIECE(3); }
  SB();
  u32 ddp = *(const CAS u32*)(p.dd + 2 * cp);
  v2u hq = *(const CAS v2u*)(p.ssm + cp * 4096 + ((w & 3) ? w * 512 - 4 : 0));
  SB();
#define STAGE_K() do { const srd_t rk = mk_srd(p.k, S * D * 2); DMANT(rk, kvo, kso, 0, w1k, L_PK + 1024); \
    if (w == 0) DMANT(rk, kvo + kso - 8192u, 0u, 0, 0u, L_PK); } while (0)
#define STAGE_V() do { const srd_t rv = mk_srd(p.v, S * D * 2); DMANT(rv, kvo, kso, 0, w1k, L_PV); \
    if (w == 1) { const srd_t rsk = mk_srd(p.sk, D * 4 * 2); DMA(rsk, (u32)(lane * 16 + xcd * 512), 0u, 0, 0u, L_PW); } \
    if (w == 2) { const srd_t rb2 = mk_srd(p.b2, D * 2); DMA_("buffer_load_dword", "", rb2, (u32)(lane * 4 + tn * 128), 0u, 0, 0u, L_B2); } } while (0)
  constexpr u32 TR_HI = 1024;
  const srd_t rw2 = mk_srd(p.w2, F * D * 2), rssm = mk_srd(p.ssm + cp * 4096, 8192);
  const u32 l16 = lane * 16;
  u32 ksh[7];
  { int t = xcd;
#pragma unroll
    for (int s = 0; s < 7; ++s) { t = t >= 7 ? t - 7 : t; ksh[s] = (u32)t * 1024; KEEPS(ksh[s]); ++t; } }
  u32 w2base = (u32)(224 * w * 1024 + tn * 128), s_cp = (u32)cp * 8192;
  u32 a_kv = L_PK + 512 + ni * 4096 + (8 * g + q4) * 128 + (((2 * mi + (p4 >> 1)) ^ (q4 & 2)) << 4) + 8 * (p4 & 1);
  u32 a_tap = L_PW + mi * 128 + r16 * 8;
  u32 o_kv = (u32)((r16 >> 1) * 8192 + (r16 & 1) * 64 + (3 - g) * 16);
  u32 s_kv = WS_KVT + (u32)((32 * xcd + 8 * mi) * 8192 + (63 - 2 * idx - ni) * 128);
  u32 w2vo[4];
#pragma unroll
  for (int i = 0; i < 4; ++i) { w2vo[i] = (u32)(rr * 1024 + i * 7168) + chsw; KEEP(w2vo[i]); }
  KEEPS(w2base); KEEPS(s_cp); KEEP(a_kv); KEEP(a_tap); KEEP(o_kv); KEEPS(s_kv);
  if (w == 2) lw128(L_U + hl * U_STRIDE + 4096 + l31 * 16, v4u{0, 0, 0, 0});
  if (w == 3) lw128(L_CNT0 + (lane & 1) * 16, v4u{0, 0, 0, 0});
  u32 fo1;
  { const int m = 2 * t1 + (lane & 1);
    const u32 fa = l31 * 8 + xcd, fb = (16 * (ti & 1) + r16) * 8 + (ti >> 1), fc = (WS_FST - WS_FKV) / FLAG_STRIDE + ((m >> 2) * 2 + (xcd & 1)) * 4 + (m & 3);
    fo1 = WS_FKV + ((is_g1 && w == 7) ? ((lane & 16) ? fc : fb) : fa) * FLAG_STRIDE + 4 * hl; }
  u32 t16 = tid * 16, o_out = (u32)((64 * tm + 8 * w) * 1024 + 128 * tn) + rr * 1024 + (lane & 7) * 16;
  u32 o_xe = WS_STATS + (u32)((128 * ti + 64 * m2) * 32) + lane * 32, o_we = WS_CSUM + (u32)((128 * tj + 32 * nq) * 16) + l31 * 16;
  u32 ta[4];
  { const int trow = 16 * hl + 4 * (g & 1) + q4;
    ta[0] = L_SLOT + w4k + trow * 128 + (((p4 >> 1) ^ (trow & 6)) << 4) + 8 * (p4 & 1);
    KEEP(ta[0]);
    ta[1] = ta[0] ^ 32; ta[2] = ta[0] ^ 64; ta[3] = ta[0] ^ 96; }
  u32 a_hc = L_HC + ni * (4 * HC_STRIDE - 4096) + 32 + t16;
  u32 o_h = (u32)((g & 1) * 57344 + r16 * 16 + hl * 256), s_h = WS_H + (u32)(((8 * ti + 4 * m2) * 56 + 4 * tj + nq) * 1024);
  KEEP(fo1); KEEP(t16); KEEP(o_out); KEEP(o_xe); KEEP(o_we); KEEP(ta[1]); KEEP(ta[2]); KEEP(ta[3]); KEEP(a_hc); KEEP(o_h); KEEPS(s_h);
  auto make_kv = [&]() {
    const v2u tw = *(LAS v2u*)lp(a_tap);
    const v2u k0 = trread(a_kv), k1 = trread(a_kv + 512), k2 = trread(a_kv + 1024);
    const v2u v0 = trread(a_kv + (L_PV - L_PK - 512)), v1 = trread(a_kv + (L_PV - L_PK));
    const float kk[12] = {0.f, bh(k0.x), bl(k0.y), bh(k0.y), bl(k1.x), bh(k1.x), bl(k1.y), bh(k1.y), bl(k2.x), bh(k2.x), bl(k2.y), bh(k2.y)};
    const float vv[8] = {bl(v0.x), bh(v0.x), bl(v0.y), bh(v0.y), bl(v1.x), bh(v1.x), bl(v1.y), bh(v1.y)};
    const float w0 = bl(tw.x), w1 = bh(tw.x), w2 = bl(tw.y), w3 = bh(tw.y);
    float o[8];
#pragma unroll
    for (int j = 0; j < 8; j += 2)
      asm("v_mul_f32 %0, %2, %7\n\tv_mul_f32 %1, %3, %7\n\tv_fmac_f32 %0, %3, %8\n\tv_fmac_f32 %1, %4, %8\n\tv_fmac_f32 %0, %4, %9\n\tv_fmac_f32 %1, %5, %9\n\t"
          "v_fmac_f32 %0, %5, %10\n\tv_fmac_f32 %1, %6, %10\n\tv_mul_f32 %0, %0, %11\n\tv_mul_f32 %1, %1, %12"
          : "=&v"(o[j]), "=&v"(o[j + 1])
          : "v"(kk[j + 1]), "v"(kk[j + 2]), "v"(kk[j + 3]), "v"(kk[j + 4]), "v"(kk[j + 5]), "v"(w3), "v"(w2), "v"(w1), "v"(w0), "v"(vv[j]), "v"(vv[j + 1]));
    st_sc1(rws, o_kv, v4u{pk(o[7], o[6]), pk(o[5], o[4]), pk(o[3], o[2]), pk(o[1], o[0])}, s_kv);
  };
  SB();
  BAR();
  if (is_g1) { PIECE(1, 0); PIECE(1, 1); PIECE(1, 2); PIECE(1, 3); }
  else { HPIECE(4); HPIECE(5); HPIECE(6); HPIECE(7); }
  WAITV(8);
  PIN(xr);
  {
    float s1 = 0.f, s2 = 0.f;
    u32 one2 = 0x3f803f80u;
    KEEP(one2);
#pragma unroll
    for (int j = 0; j < 4; ++j) {
      const u32 xj = xr[j];
      const bf2 v = __builtin_bit_cast(bf2, xj);
      s1 = __builtin_amdgcn_fdot2_f32_bf16(v, __builtin_bit_cast(bf2, one2), s1, false);
      s2 = __builtin_amdgcn_fdot2_f32_bf16(v, v, s2, false);
    }
    wave_sum2(s1, s2);
    const float mean = s1 * (1.0f / D), var = __builtin_fmaxf(s2 * (1.0f / D) - mean * mean, 0.f);
    const float iA = __builtin_amdgcn_sqrtf(var + EPS * (var + EPS));
    const float m0 = -mean, m1 = rest(m0), m2 = rest(m1);
    u32 d0 = pkt(m0, m0), d1 = pkt(m1, m1);
    const u32 mv = pk(mean, var);
    float rq = __builtin_amdgcn_rcpf(bl(pk(__builtin_amdgcn_sqrtf(bl(pk(bh(mv) + EPS, 0.f))), 0.f))), mq = -bl(mv) * rq;
    KEEP(d0); KEEP(d1); KEEP(rq); KEEP(mq);
    const bool l1 = lane & 1;
    st_sc1(rws, l01, v4u{l1 ? asu(rq) : d0, l1 ? asu(mq) : d1, pkt(m2, m0), pkt(iA, rest(iA))}, s_st);
  }
  SB();
  STAGE_K(); STAGE_V();
  v4u hd;
  ALD128(hd, t16, rssm, " nt");
  u32 tagv;
  ALD32(tagv, (u32)(WS_EPOCH + 4 * hl), rws, " sc1");
#define MAKE_TAG() do { PIN(tagv); tagv += 0x9E3779B9u; } while (0)
  SB();
  v4u w2f[7][4];
  u32 sw2;
#define W2SLOT(s) ((s) == 0 ? 3 : ((s) + 1) % 3)
#define W2_PIECE(s, i) do { if ((i) == 0) { sw2 = w2base + (ksh[s] << 5); M0SET(w4k, W2SLOT(s) * L_SLOT); } DML(rw2, w2vo[i], sw2, (i) * 1024); } while (0)
#define W2_STAGE(s) do { W2_PIECE(s, 0); W2_PIECE(s, 1); W2_PIECE(s, 2); W2_PIECE(s, 3); } while (0)
  v4u xv; v2u stv;
  v4u u0, u1;
#define LOAD_U() do { ALD128S(u0, t16, rws, s_cp + WS_KVT, " sc1"); ALD128S(u1, t16, rws, s_cp + WS_KVT + 4096, " sc1"); } while (0)
#define LOAD_LAST() ALD128(xv, o_out, rx, "")
#define LOAD_TC() ALD64(stv, WS_STATS + 16 + (64 * tm + lane) * 32, rws, " sc1")
  auto w2_read = [&](int s) {
#pragma unroll
    for (int t = 0; t < 4; ++t) w2f[s][t] = trfrag(ta[t] + W2SLOT(s) * L_SLOT - L_SLOT, TR_HI);
  };
  auto write_hc = [&](int rho) {
    const u32 p0 = __builtin_amdgcn_update_dpp(mi ? hq.x : 0u, hd.z, 0x138, 0xF, 0xF, false), p1 = __builtin_amdgcn_update_dpp(mi ? hq.y : 0u, hd.w, 0x138, 0xF, 0xF, false);
    const u32 a0 = __builtin_amdgcn_alignbit(p1, p0, 16), a1 = __builtin_amdgcn_alignbit(hd.x, p1, 16), a2 = __builtin_amdgcn_alignbit(hd.y, hd.x, 16),
              a3 = __builtin_amdgcn_alignbit(hd.z, hd.y, 16), a4 = __builtin_amdgcn_alignbit(hd.w, hd.z, 16);
    if (rho == 0) lw128(a_hc, hd);
    if (rho == 1) lw128(a_hc + HC_STRIDE, v4u{a1, a2, a3, a4});
    if (rho == 2) lw128(a_hc + 2 * HC_STRIDE, v4u{p1, hd.x, hd.y, hd.z});
    if (rho == 3) lw128(a_hc + 3 * HC_STRIDE, v4u{a0, a1, a2, a3});
  };

#define CONV_SETUP() do { PIN(hd); write_hc(0); write_hc(1); write_hc(2); write_hc(3); \
    if (w == 4) lw128(L_HC + rr * HC_STRIDE + (lane & 1) * 16, v4u{0, 0, 0, 0}); } while (0)

  if (__builtin_expect(is_g1, 1)) {
    f16v acc[2];
#pragma unroll
    for (int i = 0; i < 16; ++i) { acc[0][i] = 0.f; acc[1][i] = 0.f; }
    v4u xf[2][2][2], wf[2][2];
#define REQ(dc, i) do { if ((dc) >= 8) W2_PIECE((dc) & 3, i); else PIECE(dc, i); } while (0)
#define STEP(st, dc, d0, nd) do { \
      constexpr int hs_ = (st) & 1, hn_ = hs_ ^ 1, sl_ = (((st) + 1) >> 1) % 3, sb_ = sl_ >> 1; constexpr u32 so_ = (sl_ & 1) * L_SLOT; \
      constexpr bool rd_ = (st) < 15, mm_ = (st) >= 0; \
      _Pragma("unroll") for (int i_ = 0; i_ < 4; ++i_) { \
        if (mm_) acc[i_ & 1] = mfma32(wf[hs_][i_ >> 1], xf[hs_][i_ >> 1][i_ & 1], acc[i_ & 1]); \
        if (rd_) { \
          if (!(i_ & 1)) wf[hn_][i_ >> 1] = trfrag(wa[sb_] + so_ + (2 * hn_ + (i_ >> 1)) * 8192, 1024); \
          else { xf[hn_][i_ >> 1][0] = lr128(xa[sb_][2 * hn_ + (i_ >> 1)] + so_); xf[hn_][i_ >> 1][1] = lr128(xa[sb_][2 * hn_ + (i_ >> 1)] + so_ + 8192); } } \
        if ((nd) == 4) { if (i_ == 0) REQ(dc, 0); if (i_ == 1) REQ(dc, 1); if (i_ == 2) REQ(dc, 2); if (i_ == 3) REQ(dc, 3); } \
        if ((nd) == 2) { if (i_ == 0) REQ(dc, d0); if (i_ == 2) REQ(dc, (d0) + 1); } \
        SB(); } } while (0)
#define ITER(c) do { STEP(2 * (c) + 1, (c) + 3, 0, 2); STEP(2 * (c) + 2, (c) + 3, 2, 2); } while (0)
    u32 pv; v4u xe, we;
    VBAR(9);
    STEP(-1, 0, 0, 0);
    STEP(0, 2, 0, 4);
    VBAR(9);
    ITER(0);
    VBAR(4);
    MAKE_TAG();
    make_kv();
    SB();
    ITER(1); W2_PIECE(0, 0); W2_PIECE(0, 1);
    VBAR(6);
    if (w == 0) put_flag(rws, l0, WS_FKV + b * FLAG_STRIDE, tagv);
    ITER(2); M0SET(w4k, 3 * L_SLOT); W2_PIECE(0, 2); W2_PIECE(0, 3);
    VBAR(8);
    ITER(3);
    VBAR(6);
    ITER(4);
    if (w >= 6) { ALD32(pv, fo1, rws, " sc1"); WAITV(5); } else WAITV(4);
    LBAR();
    STEP(11, 9, 0, 2); STEP(12, 9, 2, 2);
    WAITV(4);
    if (w >= 6) { PIN(pv); wait_flags(rws, fo1, tagv, pv); }
    LBAR();
    ALD128(xe, o_xe, rws, " sc1");
    ALD128(we, o_we, rws, " sc1");
    if (!ni) LOAD_U();
    if (w == 3) LOAD_TC();
    STEP(13, 0, 0, 0); w2_read(0); STEP(14, 10, 0, 2);
    LBAR();
    STEP(15, 10, 2, 2);
    if (ni) WAITV(4); else WAITV(6);
    PIN(xe); PIN(we);
    {
      const v4u z = {0, 0, 0, 0};
      acc[0] = mfma32(we, hl ? z : xe, acc[0]);
      acc[1] = mfma32(we, hl ? xe : z, acc[1]);
    }
    if (ni) { CONV_SETUP(); SB(); }
    f2 k1v[2], k3v[2], iAv[2];
    { const auto r = __builtin_amdgcn_permlane32_swap(xe.w, xe.w, false, false);
#pragma unroll
      for (int m = 0; m < 2; ++m) {
        const float iA = bl(r[m]) + bh(r[m]), A = __builtin_amdgcn_rcpf(iA), k1s = -2.3022082f * A, k3s = -0.10294324f * A * A * A;
        k1v[m] = f2{k1s, k1s}; k3v[m] = f2{k3s, k3s}; iAv[m] = f2{iA, iA};
      } }
#pragma unroll
    for (int qi = 0; qi < 4; ++qi) {
      const int n = qi >> 1, sp = qi & 1;
      if (qi == 0) W2_PIECE(3, 0);
      if (qi == 1) W2_PIECE(3, 1);
      if (qi == 2) W2_PIECE(3, 2);
      if (qi == 3) W2_PIECE(3, 3);
      v4u o;
#pragma unroll
      for (int j = 0; j < 4; ++j) {
        const f2 a = {acc[n][8 * sp + 2 * j], acc[n][8 * sp + 2 * j + 1]}, k1 = k1v[n], k3 = k3v[n], iA = iAv[n];
        const f2 z = a * __builtin_elementwise_fma(a * a, k3, k1);
        const f2 e = {__builtin_amdgcn_exp2f(z.x), __builtin_amdgcn_exp2f(z.y)};
        const f2 d = __builtin_elementwise_fma(e, iA, iA);
        const f2 hv = a * f2{__builtin_amdgcn_rcpf(d.x), __builtin_amdgcn_rcpf(d.y)};
        o[j] = pk(hv.x, hv.y);
      }
      if (qi == 0) st_sc1<0>(rws, o_h, o, s_h);
      if (qi == 1) st_sc1<512>(rws, o_h, o, s_h);
      if (qi == 2) st_sc1<0>(rws, o_h, o, s_h + 114688);
      if (qi == 3) st_sc1<512>(rws, o_h, o, s_h + 114688);
      SB();
    }
  } else {
    u32 b1v;
    { const srd_t rb1 = mk_srd(p.b1 + 64 * cgp, 128); ALD16(b1v, (u32)(16 * mi + 4 * g + p4) * 2, rb1, ""); }
    VBAR(0);
    MAKE_TAG();
    if (w < 4) {
      const v4u ones = {0x3f803f80u, 0x3f803f80u, 0x3f803f80u, 0x3f803f80u};
      f4 s0 = {0, 0, 0, 0};
      const u32 a = (w == 0 ? ta[0] : w == 1 ? ta[1] : w == 2 ? ta[2] : ta[3]) - L_SLOT - w4k;
#pragma unroll
      for (int st = 0; st < 16; ++st) s0 = mfma(trfrag(a + st * 4096, TR_HI), ones, s0);
      const float ca = (p4 & 1) ? s0[1] : s0[0], cb = (p4 & 1) ? s0[3] : s0[2], c = (p4 & 2) ? cb : ca;
      const float c1 = rest(c), c2 = rest(c1);
      PIN(b1v);
      st_sc1(rws, r16 < 4 && hm < 14 ? WS_CSUM + (u32)(64 * cgp + 16 * w + 4 * g + p4) * 16 : 0x80000000u, v4u{pkt(c, c1), pkt(c, c1), pkt(c, c2), b1v * 0x10001u});
    }
    make_kv();
    VBAR(0);
    if (w == 0) { put_flag(rws, l0, WS_FST + (4 * xcd + sidx) * FLAG_STRIDE, tagv); put_flag(rws, l0, WS_FKV + b * FLAG_STRIDE, tagv); }
    W2_STAGE(0); W2_STAGE(1); W2_STAGE(2); W2_STAGE(3);
    if (b == 255) {
      wait_flags(rws, WS_FKV + (32 * w + l31) * FLAG_STRIDE + 4 * hl, tagv, ~tagv);
      BAR();
      if (w == 0) put_flag(rws, l0, WS_EPOCH, tagv);
    }
    wait_flags(rws, fo1, tagv, ~tagv);
    w2_read(0);
    LBAR();
    asm volatile("mk_code_end2:");
    if (ni) CONV_SETUP(); else LOAD_U();
    if (w == 3) LOAD_TC();
  }
  v4u hf[7];
  u32 hbm;
  const int mrot = 2 * tn;
  const int tok = tid >> 3, chunk = tid & 7;
  u32 fo2, hb, fo3, o_yl, pp0, yp, rp, yr;
  auto late_setup = [&]() {
    const int tja = ((224 * w) >> 7) + (rr & 3), tjb = (224 * w + 223) >> 7;
    fo2 = WS_FH + ((tm >> 1) * 14 + (tja < tjb ? tja : tjb)) * FLAG_STRIDE + 4 * hl;
    hb = WS_H + (u32)((4 * tm * 56 + 7 * w) * 1024) + l16;
    fo3 = WS_FY + (32 * tn + 4 * w + (rr & 3)) * FLAG_STRIDE + 4 * hl;
    o_yl = WS_YT + (u32)((64 * tn + tok) * 4096 + (64 * tm + 8 * chunk) * 2);
    pp0 = w4k + (((lane & 48) | ((lane + 4 * hl) & 15)) << 4);
    yp = L_Y + (8 * chunk) * 128 + ((w ^ chunk) << 4) + (tok & 7) * 2;
    rp = (((w >> 1) - mrot) & 3) * L_SLOT + (chunk >> 1) * 1024 + ((32 * (chunk & 1) + ((tok + 4 * (chunk & 3)) & 15)) << 4);
    yr = L_Y + tok * 128 + ((chunk ^ w) << 4);
    KEEP(fo2); KEEP(hb); KEEP(fo3); KEEP(o_yl); KEEP(pp0); KEEP(yp); KEEP(rp); KEEP(yr);
  };
  auto load_h = [&](int m, int s) { if (s == 0) hbm = hb + (u32)(((m + mrot) & 3) * 57344); hf[s] = ld_sc1(rws, hbm, ksh[s]); };
  f4 yacc[4];
  u32 a_x, o_y;
  if (!ni) {
    CONV_SETUP();
    const int rho = (15 - r16) & 3;
    u32 a_A = L_HC + ((mi >> 1) * 4 + rho) * HC_STRIDE + (r16 + 1 + rho + 8 * g) * 2 + (mi & 1) * 256;
    u32 a_B = L_U + (mi >> 1) * U_STRIDE + (240 - 16 * r16 + 8 * g) * 2 + (mi & 1) * 256;
    u32 a_u = L_U + ((tid >> 2) & 1) * U_STRIDE + (tid >> 3) * 64 + (tid & 3) * 16;
    KEEP(a_A); KEEP(a_B); KEEP(a_u);
    late_setup();
    WAITV(0);
    PIN(u0); PIN(u1);
    lw128(a_u, u0); lw128(a_u + 2048, u1);
    if (w == 3) { PIN(stv); *(LAS v2u*)lp(L_TC + lane * 8) = stv; }
    w2_read(1); w2_read(2); w2_read(3);
    v4u AD;
    { const u32 dv = (mi & 2) ? ddp >> 16 : ddp & 0xffffu; const int pos = 15 - r16 - 8 * g;
#pragma unroll
      for (int j = 0; j < 4; ++j) AD[j] = (pos >> 1) == j ? dv << ((pos & 1) * 16) : 0u; }
    u32 a_B0 = a_B - (mi & 1) * 256;
    a_x = L_X + w4k + l16;
    o_y = s_cp + WS_YT + (mi >> 1) * 4096 + (mi & 1) * 512 + r16 * 32 + hl * 16 + (g & 1) * ((mi & 1) ? 512u : 1536u);
    KEEP(a_x); KEEP(a_B0); KEEP(o_y);
    LBAR();
    u32 pv2;
    auto conv = [&](auto ROLE) {
      constexpr int role = decltype(ROLE)::value;
      constexpr ConvList CL = make_list(role);
      f4 ac[8];
      v4u fa[4][8], fb[4][8], fd[4];
      auto rd = [&](auto N) {
        constexpr int n = decltype(N)::value, kl = n / 36;
        if constexpr (n < 144) {
          if constexpr (CL.newA[n]) {
            const v2u lo = *(volatile LAS v2u*)lp(a_A + CL.kh[n] * 512 + kl * 64), hi = *(volatile LAS v2u*)lp(a_A + CL.kh[n] * 512 + kl * 64 + 8);
            fa[kl][CL.kh[n]] = v4u{lo.x, lo.y, hi.x, hi.y};
          }
          if constexpr (CL.newB[n]) fb[kl][CL.e[n]] = lr128(a_B + (7 - CL.e[n]) * 512 + kl * 64);
        } else if constexpr (n < 148) fd[n - 144] = lr128(a_B0 + (7 - TS[role][n - 144]) * 512);
      };
      static_for<CLA>(rd);
      static_for<144>([&](auto N) {
        constexpr int n = decltype(N)::value, kl = n / 36, T = CL.kh[n] + CL.e[n];
        ac[T] = mfma(fa[kl][CL.kh[n]], fb[kl][CL.e[n]], n < 8 ? f4{0, 0, 0, 0} : ac[T]);
        rd(std::integral_constant<int, n + CLA>{});
        if constexpr (n >= 9 && n < 9 + 12 * 5 && (n - 9) % 5 == 0) { constexpr int q = (n - 9) / 5; W2_PIECE(4 + q / 4, q % 4); }
        if constexpr (n == 80) LOAD_LAST();
        if constexpr (n == 100) ALD32(pv2, fo2, rws, " sc1");
        if constexpr (n >= 128 && n < 136 && (n & 1) == 0) lwf4(a_x + ((n - 128) / 2) * 1024, ac[TS[role ^ 1][(n - 128) / 2]]);
        if constexpr (n == 136) { asm volatile("" ::: "memory"); *(volatile LAS u32*)lp(L_CNT0 + 16 + w * 4) = 1u; }
        SB();
      });
#pragma unroll
      for (int j = 0; j < 4; ++j) yacc[j] = mfma(AD, fd[j], ac[TS[role][j]]);
    };
    if (mi & 1) conv(std::integral_constant<int, 1>{}); else conv(std::integral_constant<int, 0>{});
    WAITV(0);
    PIN(pv2);
    wait_flags(rws, fo2, tagv, pv2);
  } else {
    LBAR();
    late_setup();
    WAITV(0);
    if (is_g1) arrive_publish(L_CNT0, rws, l0, WS_FH + (ti * 14 + tj) * FLAG_STRIDE, tagv);
    w2_read(1); w2_read(2); w2_read(3);
    WAITL();
    W2_STAGE(4); W2_STAGE(5); W2_STAGE(6);
    LOAD_LAST();
    wait_flags(rws, fo2, tagv, ~tagv);
  }
#pragma unroll
  for (int s = 0; s < 7; ++s) load_h(0, s);
  SB();
  if (!ni) {
#pragma unroll 1
    for (u32 spins = 0; spins < SPIN_CAP && *(volatile LAS u32*)lp(L_CNT0 + 16 + (w ^ 1) * 4) == 0u; ++spins) __builtin_amdgcn_s_sleep(1);
    asm volatile("" ::: "memory");
#pragma unroll
    for (int j = 0; j < 4; ++j) yacc[j] += lrf4(a_x + ((mi & 1) ? -4096 : 4096) + j * 1024);
#pragma unroll
    for (int j = 0; j < 4; j += 2) {
      const auto r0 = __builtin_amdgcn_permlane16_swap(pk(yacc[j][0], yacc[j][1]), pk(yacc[j + 1][0], yacc[j + 1][1]), false, false);
      const auto r1 = __builtin_amdgcn_permlane16_swap(pk(yacc[j][2], yacc[j][3]), pk(yacc[j + 1][2], yacc[j + 1][3]), false, false);
      if (j == 0) st_sc1<0>(rws, o_y, v4u{r0[0], r1[0], r0[1], r1[1]});
      else st_sc1<2048>(rws, o_y, v4u{r0[0], r1[0], r0[1], r1[1]});
    }
  }
  w2_read(4); w2_read(5); w2_read(6);

  PIN(xv);
  v4u b2v; v2u tc; u32 cdone;
  float xx[8], xn[8], bb[8];
  v4u yv;
  {
    u32 pv3 = 0;
    f4 acc[2][4];
#pragma unroll
    for (int m = 0; m < 4; ++m) {
#pragma unroll
      for (int s = 0; s < 7; ++s) {
#pragma unroll
        for (int t = 0; t < 4; ++t) acc[m & 1][t] = mfma(w2f[s][t], hf[s], s == 0 ? f4{0, 0, 0, 0} : acc[m & 1][t]);
        if (m < 3) load_h(m + 1, s);
        if (m == 0 && s == 5 && !ni) {
          SB();
          WAITV(6);
          arrive_publish(L_CNT0 + 4, rws, l0, WS_FY + cp * FLAG_STRIDE, tagv);
        }
        if (m > 0 && s >= 1 && s <= 4) lwf4(((pp0 + (m - 1) * L_SLOT) ^ (((s - 1) & 1) * 128)) + (s - 1) * 1024, acc[(m - 1) & 1][s - 1]);
        if (m == 2 && s == 3) pv3 = get_flag(rws, fo3);
        if (m == 3 && s == 4) {
          wait_flags(rws, fo3, tagv, pv3);
          yv = ld_sc1(rws, o_yl);
        }
        if (m == 3 && s == 2) { b2v = lr128(L_B2 + chunk * 16); tc = *(LAS v2u*)lp(L_TC + tok * 8); cdone = *(volatile LAS u32*)lp(L_CNT0 + 4); }
        if (m == 3 && s >= 3) {
          const int j = s - 3;
          xx[2 * j] = bl(xv[j]); xx[2 * j + 1] = bh(xv[j]); bb[2 * j] = bl(b2v[j]); bb[2 * j + 1] = bh(b2v[j]);
          xn[2 * j] = xx[2 * j] * asf(tc.x) + asf(tc.y); KEEP(xn[2 * j]);
          xn[2 * j + 1] = xx[2 * j + 1] * asf(tc.x) + asf(tc.y); KEEP(xn[2 * j + 1]);
          KEEP(xx[2 * j]); KEEP(xx[2 * j + 1]); KEEP(bb[2 * j]); KEEP(bb[2 * j + 1]);
        }
        SB();
      }
    }
    if (__builtin_amdgcn_readfirstlane(cdone) < 4u) {
#pragma unroll 1
      for (u32 spins = 0; spins < SPIN_CAP && *(volatile LAS u32*)lp(L_CNT0 + 4) < 4u; ++spins) __builtin_amdgcn_s_sleep(1);
    }
    asm volatile("" ::: "memory");
#pragma unroll
    for (int t = 0; t < 4; ++t) lwf4(((pp0 + 3 * L_SLOT) ^ ((t & 1) * 128)) + t * 1024, acc[1][t]);
  }
#pragma unroll
  for (int j = 0; j < 4; ++j) {
    *(LAS u16*)lp(yp + (2 * j) * 128) = (u16)(yv[j] & 0xffff);
    *(LAS u16*)lp(yp + (2 * j + 1) * 128) = (u16)(yv[j] >> 16);
  }
  LBAR();
  {
    const v4u yy = lr128(yr);
    const f4 o0 = ((lrf4(rp) + lrf4(rp + 4096)) + (lrf4(rp + 8192) + lrf4(rp + 12288))) +
                  ((lrf4(rp + 16384) + lrf4(rp + 20480)) + (lrf4(rp + 24576) + lrf4(rp + 28672)));
    const f4 o1 = ((lrf4(rp + 256) + lrf4(rp + 4352)) + (lrf4(rp + 8448) + lrf4(rp + 12544))) +
                  ((lrf4(rp + 16640) + lrf4(rp + 20736)) + (lrf4(rp + 24832) + lrf4(rp + 28928)));
    float res[8];
#pragma unroll
    for (int j = 0; j < 8; ++j) {
      const float ov = (j < 4 ? o0[j & 3] : o1[j & 3]) + bb[j];
      const float sg = __builtin_amdgcn_rcpf(1.0f + __builtin_amdgcn_exp2f(ov * -1.4426950409f));
      const float yj = (j & 1) ? bh(yy[j >> 1]) : bl(yy[j >> 1]);
      res[j] = xx[j] + ((xn[j] * yj) * ov) * sg;
    }
    __builtin_amdgcn_raw_buffer_store_b128(v4u{pk(res[0], res[1]), pk(res[2], res[3]), pk(res[4], res[5]), pk(res[6], res[7])}, mk_srd(p.out, S * D * 2), o_out, 0, 16);
  }
  asm volatile("mk_code_end:");
}
}

extern "C" void kernel_launch(void* const* d_in, const int* in_sizes, int n_in, void* d_out, int out_size, void* d_ws, size_t ws_size,
                              hipStream_t stream) {
  using namespace mk;
  Params p;
  p.x = (const u16*)d_in[0]; p.k = (const u16*)d_in[1]; p.v = (const u16*)d_in[2]; p.sk = (const u16*)d_in[3];
  p.ssm = (const u16*)d_in[4]; p.dd = (const u16*)d_in[5]; p.w1 = (const u16*)d_in[7]; p.b1 = (const u16*)d_in[8];
  p.w2 = (const u16*)d_in[9]; p.b2 = (const u16*)d_in[10];
  p.out = (u16*)d_out; p.ws = (unsigned char*)d_ws;
  (void)hipFuncSetAttribute((const void*)fused, hipFuncAttributeMaxDynamicSharedMemorySize, LDS_BYTES);
  fused<<<256, 512, LDS_BYTES, stream>>>(p);
}
```
